```python
import torch

def _softmax(x, axis=-1):
    return torch.softmax(x, dim=axis)
def _gelu(x):
    return torch.nn.functional.gelu(x, approximate="tanh")
def _silu(x):
    return torch.nn.functional.silu(x)
def _sigmoid(x):
    return torch.sigmoid(x)
def _layernorm(x, axis=-1, eps=1e-5):
    m = x.mean(dim=axis, keepdim=True)
    v = x.var(dim=axis, keepdim=True, unbiased=False)
    return (x - m) / torch.sqrt(v + eps)
def _scatter_add(tgt, idx, src, axis=0):
    return tgt.clone().index_add_(axis, idx, src)
def _segment_sum(src, seg, n):
    out = torch.zeros((n,) + src.shape[1:], dtype=src.dtype)
    return out.index_add_(0, seg, src)
def _pad(x, pw):
    flat = tuple(v for pair in reversed(pw) for v in pair)
    return torch.nn.functional.pad(x, flat)

kernel_name = 'sequential_preLN__ssm_conv_s4__mixer_s_mlp__prompt'

def setup_inputs():

    _w = (lambda a: (lambda t: t.to(torch.bfloat16) if t.is_floating_point() else t)(torch.as_tensor(a)))
    def _A_setup():
        B, L, D, N = 1, 2048, 512, 128
        A_re = -(lambda _x: torch.exp(torch.as_tensor(_x)))((lambda *s: torch.randn(*s, dtype=torch.bfloat16) * 3.2)(D, N))
        A_im = torch.pi * (lambda *s: torch.randn(*s, dtype=torch.bfloat16) * 0.05)(D, N)
        B_re = (lambda *s: torch.randn(*s, dtype=torch.bfloat16) * 0.1)(D, N)
        B_im = (lambda *s: torch.randn(*s, dtype=torch.bfloat16) * 0.1)(D, N)
        C_re = (lambda *s: torch.randn(*s, dtype=torch.bfloat16) * 0.1)(D, N)
        C_im = (lambda *s: torch.randn(*s, dtype=torch.bfloat16) * 0.1)(D, N)
        log_dt = (lambda *s: torch.randn(*s, dtype=torch.bfloat16) * 0.05)(D) * 0.5 - 4.0
        Dskip = (lambda *s: torch.randn(*s, dtype=torch.bfloat16) * 0.05)(D)
        return {
            "u": _w((lambda *s: torch.randn(*s, dtype=torch.bfloat16) * 0.05)(B, L, D)),
            "A_re": _w(A_re),
            "A_im": _w(A_im),
            "B_re": _w(B_re),
            "B_im": _w(B_im),
            "C_re": _w(C_re),
            "C_im": _w(C_im),
            "log_dt": _w(log_dt),
            "Dskip": _w(Dskip),
        }
    _A_in = _A_setup()

    def _B_setup():
        B, S, D, F = 1, 2048, 512, 1792
        return {
            "x":  _w((lambda *s: torch.randn(*s, dtype=torch.bfloat16) * 0.05)(B, S, D)),
            "w1": _w((lambda *s: torch.randn(*s, dtype=torch.bfloat16) * 3.2)(D, D) * (D ** -0.5)),
            "w2": _w((lambda *s: torch.randn(*s, dtype=torch.bfloat16) * 3.2)(D, D) * (D ** -0.5)),
            "w3": _w((lambda *s: torch.randn(*s, dtype=torch.bfloat16) * 3.2)(D, F) * (D ** -0.5)),
            "w4": _w((lambda *s: torch.randn(*s, dtype=torch.bfloat16) * 1.6)(F, D) * (F ** -0.5)),
        }
    _B_in = _B_setup()

    return {
        "A_u": _A_in["u"],
        "A_A_re": _A_in["A_re"],
        "A_A_im": _A_in["A_im"],
        "A_B_re": _A_in["B_re"],
        "A_B_im": _A_in["B_im"],
        "A_C_re": _A_in["C_re"],
        "A_C_im": _A_in["C_im"],
        "A_log_dt": _A_in["log_dt"],
        "A_Dskip": _A_in["Dskip"],
        "B_x": _B_in["x"],
        "B_w1": _B_in["w1"],
        "B_w2": _B_in["w2"],
        "B_w3": _B_in["w3"],
        "B_w4": _B_in["w4"],
    }

def reference(A_u, A_A_re, A_A_im, A_B_re, A_B_im, A_C_re, A_C_im, A_log_dt, A_Dskip, B_x, B_w1, B_w2, B_w3, B_w4):
    def _A_ref(u, A_re, A_im, B_re, B_im, C_re, C_im, log_dt, Dskip):
        Bsz, L, D = u.shape
        dt = torch.exp(log_dt)[:, None]
        A = A_re + 1j * A_im
        Bc = B_re + 1j * B_im
        Cc = C_re + 1j * C_im
        logdA = dt * A
        dB = dt * Bc
        l = (lambda x, dt: x.to(dt))(torch.arange(L), A_re.dtype)
        P = torch.exp(logdA[:, :, None] * l[None, None, :])
        K = torch.einsum("dn,dnl,dn->dl", Cc, P, dB).real
        L2 = 2 * L
        Kp = _pad(K, ((0, 0), (0, L)))
        up = (lambda x, *d: x.permute(*d))(u, 0, 2, 1)
        up = _pad(up, ((0, 0), (0, 0), (0, L)))
        Kf = (lambda x, axes=(-2,-1): torch.fft.rfft2(x.float(), dim=axes))(Kp[:, None, :], axes=(-2, -1))
        Uf = (lambda x, axes=(-2,-1): torch.fft.rfft2(x.float(), dim=axes))(up[:, :, None, :], axes=(-2, -1))
        Yf = Uf * Kf[None, :, :, :]
        y = (lambda x, s, axes=(-2,-1): torch.fft.irfft2(x, s=s, dim=axes).to(torch.bfloat16))(Yf, (1, L2), axes=(-2, -1))[:, :, 0, :L]
        y = (lambda x, *d: x.permute(*d))(y, 0, 2, 1) + u * Dskip[None, None, :]
        return y

    def _B_ref(x, w1, w2, w3, w4):
        B, S, D = x.shape
        h = _gelu(_layernorm(x, axis=-1) @ w1)
        c = D // 4
        g0 = _pad(h[:, :, 0*c:1*c], ((0, 0), (1, 0), (0, 0)))[:, :S, :]
        g1 = _pad(h[:, :, 1*c:2*c], ((0, 0), (0, 1), (0, 0)))[:, 1:, :]
        g2 = _pad(h[:, :, 2*c:3*c], ((0, 0), (2, 0), (0, 0)))[:, :S, :]
        g3 = _pad(h[:, :, 3*c:   ], ((0, 0), (0, 2), (0, 0)))[:, 2:, :]
        hs = torch.concatenate([g0, g1, g2, g3], axis=-1) @ w2
        x1 = x + hs
        h2 = _gelu(_layernorm(x1, axis=-1) @ w3) @ w4
        return x1 + h2

    x = A_u
    h = x + _A_ref(_layernorm(x, axis=-1), A_A_re, A_A_im, A_B_re, A_B_im, A_C_re, A_C_im, A_log_dt, A_Dskip)
    h2 = h + _B_ref(_layernorm(h, axis=-1), B_w1, B_w2, B_w3, B_w4)
    return h2
```

```cpp
#include <hip/hip_runtime.h>
#include <hip/hip_bf16.h>
#include <stdint.h>

typedef unsigned short u16;
__device__ __forceinline__ float bf2f(u16 v) { return __uint_as_float(((unsigned)v) << 16); }
__device__ __forceinline__ u16 f2bf(float f) { __hip_bfloat16 b = __float2bfloat16(f); return *reinterpret_cast<u16*>(&b); }
__device__ __forceinline__ float rbf(float f) { return bf2f(f2bf(f)); }

#define L_ 2048
#define D_ 512
#define N_ 128
#define F_ 1792

__global__ void k_ln(const u16* __restrict__ x, u16* __restrict__ out) {
  int row = blockIdx.x; int lane = threadIdx.x;
  float v[8]; float s = 0;
  for (int i = 0; i < 8; i++) { v[i] = bf2f(x[row * D_ + lane * 8 + i]); s += v[i]; }
  for (int o = 32; o > 0; o >>= 1) s += __shfl_xor(s, o);
  float mean = s / D_;
  float q = 0;
  for (int i = 0; i < 8; i++) { float d = v[i] - mean; q += d * d; }
  for (int o = 32; o > 0; o >>= 1) q += __shfl_xor(q, o);
  float var = rbf(q / D_);
  float m = rbf(mean);
  float sd = rbf(sqrtf(rbf(var + 1e-5f)));
  for (int i = 0; i < 8; i++) out[row * D_ + lane * 8 + i] = f2bf(rbf(v[i] - m) / sd);
}

__global__ void k_kgen(const u16* Are, const u16* Aim, const u16* Bre, const u16* Bim, const u16* Cre, const u16* Cim,
                       const u16* logdt, float* K) {
  int d = blockIdx.y; int l = blockIdx.x * blockDim.x + threadIdx.x;
  float dt = rbf(expf(bf2f(logdt[d])));
  float lf = rbf((float)l);
  float acc = 0;
  for (int n = 0; n < N_; n++) {
    float ar = dt * bf2f(Are[d * N_ + n]), ai = dt * bf2f(Aim[d * N_ + n]);
    float br = dt * bf2f(Bre[d * N_ + n]), bi = dt * bf2f(Bim[d * N_ + n]);
    float cr = bf2f(Cre[d * N_ + n]), ci = bf2f(Cim[d * N_ + n]);
    float e = expf(ar * lf); float sn, cs; sincosf(ai * lf, &sn, &cs);
    float pr = e * cs, pi = e * sn;
    float cbr = cr * br - ci * bi, cbi = cr * bi + ci * br;
    acc += cbr * pr - cbi * pi;
  }
  K[d * L_ + l] = acc;
}

__global__ void k_conv(const u16* x, const u16* u, const float* K, const u16* Dskip, u16* h) {
  int d = blockIdx.x * blockDim.x + threadIdx.x; int l = blockIdx.y;
  float acc = 0;
  for (int j = 0; j <= l; j++) acc += K[d * L_ + j] * bf2f(u[(l - j) * D_ + d]);
  float y = rbf(acc);
  float t = rbf(bf2f(u[l * D_ + d]) * bf2f(Dskip[d]));
  y = rbf(y + t);
  h[l * D_ + d] = f2bf(bf2f(x[l * D_ + d]) + y);
}

__device__ __forceinline__ float gelu(float x) {
  float in = 0.7978845608028654f * (x + 0.044715f * x * x * x);
  return 0.5f * x * (1.f + tanhf(in));
}

template <int MODE>
__global__ void k_gemm(const u16* A, const u16* W, u16* out, int K, int N, const u16* res, const u16* res2) {
  int n = blockIdx.x * blockDim.x + threadIdx.x; int m = blockIdx.y;
  float acc = 0;
  for (int k = 0; k < K; k++) {
    float a;
    if (MODE == 1) {
      int g = k >> 7; int sh = g == 0 ? -1 : g == 1 ? 1 : g == 2 ? -2 : 2; int r = m + sh;
      a = (r >= 0 && r < L_) ? bf2f(A[r * K + k]) : 0.f;
    } else a = bf2f(A[m * K + k]);
    acc += a * bf2f(W[k * N + n]);
  }
  float v = rbf(acc);
  if (MODE == 0) v = gelu(v);
  else if (MODE == 1) v = bf2f(res[m * N + n]) + v;
  else v = bf2f(res2[m * N + n]) + rbf(bf2f(res[m * N + n]) + v);
  out[m * N + n] = f2bf(v);
}

extern "C" void kernel_launch(void* const* d_in, const int* in_sizes, int n_in,
                              void* d_out, int out_size, void* d_ws, size_t ws_size,
                              hipStream_t stream) {
  const u16* x = (const u16*)d_in[0];
  char* ws = (char*)d_ws;
  u16* u = (u16*)(ws);
  float* K = (float*)(ws + (2 << 20));
  u16* h = (u16*)(ws + (6 << 20));
  u16* xb = (u16*)(ws + (8 << 20));
  u16* a1 = (u16*)(ws + (10 << 20));
  u16* h1 = (u16*)(ws + (12 << 20));
  u16* x1 = (u16*)(ws + (14 << 20));
  u16* a3 = (u16*)(ws + (16 << 20));
  u16* hid = (u16*)(ws + (18 << 20));
  k_ln<<<L_, 64, 0, stream>>>(x, u);
  k_kgen<<<dim3(L_ / 256, D_), 256, 0, stream>>>((const u16*)d_in[1], (const u16*)d_in[2], (const u16*)d_in[3], (const u16*)d_in[4],
      (const u16*)d_in[5], (const u16*)d_in[6], (const u16*)d_in[7], K);
  k_conv<<<dim3(D_ / 256, L_), 256, 0, stream>>>(x, u, K, (const u16*)d_in[8], h);
  k_ln<<<L_, 64, 0, stream>>>(h, xb);
  k_ln<<<L_, 64, 0, stream>>>(xb, a1);
  k_gemm<0><<<dim3(D_ / 256, L_), 256, 0, stream>>>(a1, (const u16*)d_in[10], h1, D_, D_, nullptr, nullptr);
  k_gemm<1><<<dim3(D_ / 256, L_), 256, 0, stream>>>(h1, (const u16*)d_in[11], x1, D_, D_, xb, nullptr);
  k_ln<<<L_, 64, 0, stream>>>(x1, a3);
  k_gemm<0><<<dim3(F_ / 256, L_), 256, 0, stream>>>(a3, (const u16*)d_in[12], hid, D_, F_, nullptr, nullptr);
  k_gemm<2><<<dim3(D_ / 256, L_), 256, 0, stream>>>(hid, (const u16*)d_in[13], (u16*)d_out, F_, D_, x1, h);
}
```

```cpp
#include <hip/hip_runtime.h>
#include <stdint.h>

typedef unsigned short u16;
typedef unsigned int u32;
typedef unsigned long long u64;
typedef __bf16 bf16x2 __attribute__((ext_vector_type(2)));
typedef __bf16 bf16x8 __attribute__((ext_vector_type(8)));
typedef _Float16 f16x2 __attribute__((ext_vector_type(2)));
typedef _Float16 f16x8 __attribute__((ext_vector_type(8)));
typedef float f32x2 __attribute__((ext_vector_type(2)));
typedef float f32x4 __attribute__((ext_vector_type(4)));
typedef short s16x4 __attribute__((ext_vector_type(4)));
typedef u32 u32x2 __attribute__((ext_vector_type(2)));
typedef u32 u32x4 __attribute__((ext_vector_type(4)));
#define LDSP __attribute__((address_space(3)))
#define DEV __device__ __forceinline__

constexpr int L_ = 2048, D_ = 512, NS_ = 128, F_ = 1792;
constexpr u32 WS_U8 = 0;
constexpr u32 WS_HT = 7u << 20;
constexpr u32 WS_H1 = 13u << 20;
constexpr u32 WS_X1 = 16u << 20;
constexpr u32 WS_ST = 18u << 20;
constexpr u32 WS_HID = 19u << 20;
constexpr u32 WS_FL = 28u << 20;
constexpr u32 FL_EPOCH = WS_FL, FL_1 = WS_FL + 4096, FL_2 = FL_1 + 32768, FL_3 = FL_2 + 32768, FL_4 = FL_3 + 16384, FL_5 = FL_4 + 16384, FL_6 = FL_5 + 16384;

struct Params {
  const u16 *x, *are, *aim, *bre, *bim, *cre, *cim, *logdt, *dskip, *w1, *w2, *w3, *w4;
  u16* out;
  char* ws;
};

DEV float bf2f(u32 v) { return __uint_as_float(v << 16); }
DEV float bflo(u32 v) { return __uint_as_float(v << 16); }
DEV float bfhi(u32 v) { return __uint_as_float(v & 0xffff0000u); }
DEV f32x2 bfup(u32 v) { return f32x2{bflo(v), bfhi(v)}; }
DEV u32 pkbf2(f32x2 v) { return __builtin_bit_cast(u32, __builtin_convertvector(v, bf16x2)); }
DEV u32 pkbf(float a, float b) { return pkbf2(f32x2{a, b}); }
DEV u32 pkh(float a, float b) { f32x2 v = {a, b}; return __builtin_bit_cast(u32, __builtin_convertvector(v, f16x2)); }
DEV float rbf(float a) { return bflo(pkbf(a, a)); }
DEV f32x2 hup(u32 v) { return __builtin_convertvector(__builtin_bit_cast(f16x2, v), f32x2); }

template <int CTRL> DEV float dppf(float v) {
  return __builtin_bit_cast(float, __builtin_amdgcn_update_dpp(0, __builtin_bit_cast(int, v), CTRL, 0xf, 0xf, true));
}
DEV float gsum(float v) {
  u32 a = __float_as_uint(v);
  auto r = __builtin_amdgcn_permlane16_swap(a, a, false, false);
  v = __uint_as_float(r[0]) + __uint_as_float(r[1]);
  a = __float_as_uint(v);
  auto q = __builtin_amdgcn_permlane32_swap(a, a, false, false);
  return __uint_as_float(q[0]) + __uint_as_float(q[1]);
}
DEV float gmax(float v) {
  u32 a = __float_as_uint(v);
  auto r = __builtin_amdgcn_permlane16_swap(a, a, false, false);
  v = fmaxf(__uint_as_float(r[0]), __uint_as_float(r[1]));
  a = __float_as_uint(v);
  auto q = __builtin_amdgcn_permlane32_swap(a, a, false, false);
  return fmaxf(__uint_as_float(q[0]), __uint_as_float(q[1]));
}
DEV float wave_sum(float v) {
  v += dppf<0xB1>(v); v += dppf<0x4E>(v); v += dppf<0x141>(v); v += dppf<0x140>(v);
  return gsum(v);
}
DEV float wave_max(float v) {
  v = fmaxf(v, dppf<0xB1>(v)); v = fmaxf(v, dppf<0x4E>(v)); v = fmaxf(v, dppf<0x141>(v)); v = fmaxf(v, dppf<0x140>(v));
  return gmax(v);
}
DEV u32x2 tr_read(const char* p) {
  return __builtin_bit_cast(u32x2, __builtin_amdgcn_ds_read_tr16_b64_v4i16((LDSP s16x4*)p));
}
DEV void mma(f32x4& c, u32x4 a, u32x4 b) { asm volatile("v_mfma_f32_16x16x32_bf16 %0, %1, %2, %0" : "+v"(c) : "v"(a), "v"(b)); }
DEV void mma0(f32x4& c, u32x4 a, u32x4 b) { asm volatile("v_mfma_f32_16x16x32_bf16 %0, %1, %2, 0" : "=&v"(c) : "v"(a), "v"(b)); }
DEV void mmah(f32x4& c, u32x4 a, u32x4 b) { asm volatile("v_mfma_f32_16x16x32_f16 %0, %1, %2, %0" : "+v"(c) : "v"(a), "v"(b)); }
DEV void mmah0(f32x4& c, u32x4 a, u32x4 b) { asm volatile("v_mfma_f32_16x16x32_f16 %0, %1, %2, 0" : "=&v"(c) : "v"(a), "v"(b)); }
DEV void mmaz(bool first, f32x4& c, u32x4 a, u32x4 b) { if (first) mma0(c, a, b); else mma(c, a, b); }
DEV void mfence() { asm volatile("s_nop 7" ::: "memory"); __builtin_amdgcn_sched_barrier(0); }
DEV float smul(float a, float b) { float r; asm("v_mul_f32 %0, %1, %2" : "=v"(r) : "v"(a), "v"(b)); return r; }
DEV float sfma(float a, float b, float c) { float r; asm("v_fma_f32 %0, %1, %2, %3" : "=v"(r) : "v"(a), "v"(b), "v"(c)); return r; }
DEV float sfmak(float a, float k1, float k0) { float r; asm("v_fma_f32 %0, %1, %2, %3" : "=v"(r) : "v"(a), "s"(k1), "v"(k0)); return r; }
DEV float sadd1(float a) { float r; asm("v_add_f32 %0, 1.0, %1" : "=v"(r) : "v"(a)); return r; }
DEV void pin(f32x4& c) { asm volatile("" : "+v"(c)); }
DEV float dot2(u32 a, u32 b, float c) {
  return __builtin_amdgcn_fdot2_f32_bf16(__builtin_bit_cast(bf16x2, a), __builtin_bit_cast(bf16x2, b), c, false);
}
template <int LOFF, int MOFF> DEV void glds(u32 voff, const void* sbase, u32 lds) {
  asm volatile("s_add_u32 m0, %1, %3\n\ts_nop 0\n\tglobal_load_lds_dwordx4 %0, %2"
               :: "v"(voff + (u32)MOFF), "s"(lds), "s"(sbase), "n"(LOFF) : "memory", "m0", "scc");
}
DEV u32 lds_addr(const char* p) { return (u32)(size_t)(LDSP const char*)p; }
#define SCHED() __builtin_amdgcn_sched_barrier(0)
template <int N> DEV void wait_vm() { asm volatile("s_waitcnt vmcnt(%0)" ::"n"(N) : "memory"); }
template <int N> DEV void wait_vm_lgkm() { asm volatile("s_waitcnt vmcnt(%0) lgkmcnt(0)" ::"n"(N) : "memory"); }
DEV void bar() { __builtin_amdgcn_s_barrier(); }
DEV void lds_bar() { asm volatile("s_waitcnt lgkmcnt(0)" ::: "memory"); __builtin_amdgcn_s_barrier(); }

#define STAMP(i)

typedef __amdgpu_buffer_rsrc_t rsrc_t;
DEV void st16_dev(rsrc_t r, u32 off, u32x4 v) { __builtin_amdgcn_raw_buffer_store_b128(v, r, off, 0, 16); }
DEV void st8_dev(rsrc_t r, u32 off, u32x2 v) { __builtin_amdgcn_raw_buffer_store_b64(v, r, off, 0, 16); }
DEV u32x4 ld16_dev(rsrc_t r, u32 off) { return __builtin_amdgcn_raw_buffer_load_b128(r, off, 0, 16); }
DEV u32x2 ld8_dev(rsrc_t r, u32 off) { return __builtin_amdgcn_raw_buffer_load_b64(r, off, 0, 16); }

constexpr int SPIN_MAX = 1 << 18;
DEV void poll256(rsrc_t r, u32 base, u32x2 tag, int lane) {
  for (int s = 0; s < SPIN_MAX; s++) {
    u32x4 a = ld16_dev(r, base + lane * 32), b = ld16_dev(r, base + lane * 32 + 16);
    bool ok = a[0] == tag[0] && a[1] == tag[1] && a[2] == tag[0] && a[3] == tag[1] &&
              b[0] == tag[0] && b[1] == tag[1] && b[2] == tag[0] && b[3] == tag[1];
    if (__all(ok)) break;
    __builtin_amdgcn_s_sleep(2);
  }
}
DEV void polln(rsrc_t r, u32 base, int lo, int hi, u32x2 tag, int lane) {
  const int i = min(lo + lane, hi - 1);
  for (int s = 0; s < SPIN_MAX; s++) {
    u32x2 a = ld8_dev(r, base + i * 8);
    if (__all(a[0] == tag[0] && a[1] == tag[1])) break;
    __builtin_amdgcn_s_sleep(2);
  }
}

DEV u32x2 act4(f32x4 a, f32x4 cs, f32x4 rt) {
  const float c0 = -2.0f * 0.7978845608028654f * 1.4426950408889634f;
  const float c1 = c0 * 0.044715f;
  const f32x2 rs = {rt[0], rt[1]}, t = {rt[2], rt[3]};
  f32x2 v0 = f32x2{a[0], a[1]} * rs + f32x2{cs[0], cs[1]} * t, v1 = f32x2{a[2], a[3]} * rs + f32x2{cs[2], cs[3]} * t;
  f32x4 v = {v0[0], v0[1], v1[0], v1[1]};
  u32 p0 = pkbf(v[0], v[1]), p1 = pkbf(v[2], v[3]);
  f32x4 x = {bflo(p0), bfhi(p0), bflo(p1), bfhi(p1)};
  f32x4 z = x * (x * x * c1 + c0);
  f32x4 e = {__builtin_amdgcn_exp2f(z[0]), __builtin_amdgcn_exp2f(z[1]), __builtin_amdgcn_exp2f(z[2]), __builtin_amdgcn_exp2f(z[3])};
  e += 1.0f;
  f32x4 r = {__builtin_amdgcn_rcpf(e[0]), __builtin_amdgcn_rcpf(e[1]), __builtin_amdgcn_rcpf(e[2]), __builtin_amdgcn_rcpf(e[3])};
  x *= r;
  return u32x2{pkbf(x[0], x[1]), pkbf(x[2], x[3])};
}

constexpr int LDS_ALL = 163840;
constexpr int KG_ROW = 528;
constexpr int KG_CH = 73 * KG_ROW;
constexpr int P0_KT = 77312;
constexpr int P0_LN = 101888;
constexpr int P0_MS = 119296;
constexpr int CV_COPY = 4160, CV_KP = 4 * CV_COPY, CV_CH = CV_KP + 4608;
constexpr int CV_UI = 2 * CV_CH;
constexpr int CV_RED = CV_UI + 16384;
constexpr int CV_STG = 0;
constexpr int GA_A = 0, GA_TAB = 65536, GA_B = 98304;
constexpr int G3_ST = 73728, G3_TAB = 90112;
constexpr int G4_B = 65536;

struct KTbl {
  u16 v[2048];
  constexpr KTbl() : v() {
    for (int pos = 0; pos < 2048; pos++) {
      int l = (pos & ~7) + 7 - (pos & 7);
      int sh = l < 256 ? 0 : l < 512 ? 1 : l < 1024 ? 2 : 3;
      int lv = sh ? (((l + (1 << (sh - 1)) - 1 + ((l >> sh) & 1)) >> sh) << sh) : l;
      int s = lv < 256 ? 0 : lv < 512 ? 1 : lv < 1024 ? 2 : 3;
      int rr = 8 * s + ((lv >> 7) << (3 - s)) + ((lv & 7) >> s);
      v[pos] = (u16)((rr * 16 + ((lv >> 3) & 15)) * 16);
    }
  }
};
__device__ __attribute__((aligned(16))) constexpr KTbl KTBL{};

struct cf { float x, y; };
DEV cf cmul(cf a, cf b) { return {a.x * b.x - a.y * b.y, a.x * b.y + a.y * b.x}; }
DEV cf cscale(cf a, float s) { return {a.x * s, a.y * s}; }
DEV cf cexpk(float tl2, float rh, float rl, float s) {
  float mag = __builtin_amdgcn_exp2f(tl2 * s);
  float p = rh * s;
  float f = __builtin_fmaf(rl, s, p - __builtin_rintf(p));
  return {mag * __builtin_amdgcn_cosf(f), mag * __builtin_amdgcn_sinf(f)};
}
DEV void st_x(char* p, cf a) { *(u32*)p = pkh(a.x, a.y); }
DEV void st_y(char* p, cf a) {
  u32 hi = pkh(a.x, a.y);
  f32x2 r = f32x2{a.x, a.y} - hup(hi);
  *(u32*)p = hi;
  *(u32*)(p + 16 * KG_ROW) = pkh(r[0], r[1]);
}
DEV float pow2_scale(float mx, u32 target, float& inv) {
  u32 eb = (__float_as_uint(mx) >> 23) & 0xffu;
  u32 se = (eb == 0 || eb == 255) ? 127u : min(254u + target - eb, 253u);
  inv = __uint_as_float((254u - se) << 23);
  return __uint_as_float(se << 23);
}

DEV void conv_load1(const char* pa, const char* pb, int b0, int i, u32x4 (&A)[8], u32x4 (&B)[8]) {
  u32x2 lo = *(const volatile LDSP u32x2*)(pa + 64 * b0 + 512 * i), hi = *(const volatile LDSP u32x2*)(pa + 64 * b0 + 512 * i + 8);
  A[i] = u32x4{lo[0], lo[1], hi[0], hi[1]};
  B[i] = *(const u32x4*)(pb - 64 * b0 + 512 * i);
}
DEV void conv_step(const char* pa, const char* pb, int nb, const u32x4 (&A)[8], const u32x4 (&B)[8], u32x4 (&An)[8], u32x4 (&Bn)[8], f32x4 (&acc)[8]) {
#pragma unroll
  for (int i = 0; i < 8; i++) {
    if (nb < 4 && i < 4) { conv_load1(pa, pb, nb, 2 * i, An, Bn); conv_load1(pa, pb, nb, 2 * i + 1, An, Bn); }
    SCHED();
#pragma unroll
    for (int t = i; t < 8; t++) { if (nb == 1 && i == 0) mmah0(acc[t], A[i], B[t - i]); else mmah(acc[t], A[i], B[t - i]); }
    SCHED();
  }
}

DEV u32 kn_voff(int ld, int w, int lane) {
  int kr = lane >> 3, pos = lane & 7;
  int f2 = ((lane >> 4) & 1) | ((w & 1) << 1);
  return (u32)((8 * w + kr) * ld * 2 + ((pos ^ (f2 << 1)) << 4));
}
DEV u32 mk_voff(int ld, int lane) { int r = lane >> 3, pos = lane & 7; return (u32)(r * ld * 2 + ((pos ^ r) << 4)); }
DEV int kn_base(int lane) {
  int g = lane >> 4, q = (lane & 15) >> 2, pp = lane & 3;
  int f2 = ((q >> 1) & 1) | ((g & 1) << 1);
  return (8 * g + q) * 128 + (f2 << 5) + pp * 8;
}
DEV u32x4 kn_frag(const char* p) { u32x2 a = tr_read(p), b = tr_read(p + 512); return u32x4{a[0], a[1], b[0], b[1]}; }
DEV int mk_base(int lane, int ks) { int r16 = lane & 15, g = lane >> 4; return r16 * 128 + (((4 * ks + g) ^ (r16 & 7)) << 4); }
constexpr u32 ONES = 0x3f803f80u;
DEV u32x4 pair_swap(u32 p01, u32 p23, u32 q01, u32 q23) {
  auto r0 = __builtin_amdgcn_permlane16_swap(p01, q01, false, false);
  auto r1 = __builtin_amdgcn_permlane16_swap(p23, q23, false, false);
  return u32x4{r0[0], r1[0], r0[1], r1[1]};
}
template <int LDW> DEV void w64_issue(const u16* wsrc, u32 dst, int w, int lane) {
  const u32 vo = kn_voff(LDW, w, lane);
  const u32 d = dst + w * 1024;
  glds<0 * 8192, 0 * 128 * LDW>(vo, wsrc, d); glds<1 * 8192, 1 * 128 * LDW>(vo, wsrc, d);
  glds<2 * 8192, 2 * 128 * LDW>(vo, wsrc, d); glds<3 * 8192, 3 * 128 * LDW>(vo, wsrc, d);
  glds<4 * 8192, 4 * 128 * LDW>(vo, wsrc, d); glds<5 * 8192, 5 * 128 * LDW>(vo, wsrc, d);
  glds<6 * 8192, 6 * 128 * LDW>(vo, wsrc, d); glds<7 * 8192, 7 * 128 * LDW>(vo, wsrc, d);
}
DEV void w64_regs(const char* img, int wn, int lane, u32x4 (&B)[16][2]) {
  const char* p0 = img + (kn_base(lane) ^ ((2 * wn) << 5));
  const char* p1 = img + (kn_base(lane) ^ ((2 * wn + 1) << 5));
#pragma unroll
  for (int s = 0; s < 16; s++) { B[s][0] = kn_frag(p0 + s * 4096); B[s][1] = kn_frag(p1 + s * 4096); }
}
template <int LDW> DEV void w64_issue4(const u16* wsrc, u32 dst, int w, int lane) {
  w64_issue<LDW>(wsrc, dst, w - 4, lane); w64_issue<LDW>(wsrc, dst, w, lane);
}
template <int H> DEV void w3_issue(const u16* sb, u32 lds0, int w, int lane) {
  constexpr int CB3 = 128 * F_;
  const u32 vb = kn_voff(F_, w, lane), dw = lds0 + GA_B + w * 1024;
  glds<0, (4 * H) * CB3>(vb, sb, dw); glds<8192, (4 * H) * CB3 + 128>(vb, sb, dw);
  glds<16384, (4 * H + 1) * CB3>(vb, sb, dw); glds<24576, (4 * H + 1) * CB3 + 128>(vb, sb, dw);
  glds<32768, (4 * H + 2) * CB3>(vb, sb, dw); glds<40960, (4 * H + 2) * CB3 + 128>(vb, sb, dw);
  glds<49152, (4 * H + 3) * CB3>(vb, sb, dw); glds<57344, (4 * H + 3) * CB3 + 128>(vb, sb, dw);
}

__global__ __launch_bounds__(512, 2) void k_fused(Params p) {
  __shared__ __attribute__((aligned(16))) char smem[LDS_ALL];
  const int bid = blockIdx.x;
  const int tid = threadIdx.x, w = __builtin_amdgcn_readfirstlane(tid >> 6), lane = tid & 63;
  const int r16 = lane & 15, g = lane >> 4;
  const u32 lds0 = lds_addr(smem);
  const rsrc_t wsr = __builtin_amdgcn_make_buffer_rsrc(p.ws, 0, 0x4000000, 0x00020000);
  STAMP(0);
  const int cp = (bid & 7) * 32 + (bid >> 3);
  const int pm = (bid & 7) * 4 + (bid >> 6), pn = (bid >> 3) & 7;
  const int row0 = pm * 64, col0 = pn * 64;
  u32x2 tag;
  {
    u32x2 ep = ld8_dev(wsr, FL_EPOCH);
    u32x4 xv = *(const u32x4*)(p.x + (bid * 8 + w) * D_ + lane * 8);
    const int chl = w >> 2, role = (w >> 1) & 1, n = tid & 127;
    const int ch = cp * 2 + chl;
    const int pi = ch * NS_ + n;
    u32 r_are = p.are[pi], r_aim = p.aim[pi], r_bre = p.bre[pi], r_bim = p.bim[pi], r_cre = p.cre[pi], r_cim = p.cim[pi];
    u32 r_ldt = p.logdt[ch];
    u32x4 kidx = *(const u32x4*)(KTBL.v + (tid & 255) * 8);
    {
      u64 e = ((u64)ep[1] << 32 | ep[0]) + 1;
      tag = u32x2{(u32)__builtin_amdgcn_readfirstlane((u32)e), (u32)__builtin_amdgcn_readfirstlane((u32)(e >> 32))};
    }
    char* su = smem + P0_LN;
    float* ms = (float*)(smem + P0_MS);
    {
      f32x2 v[4];
#pragma unroll
      for (int i = 0; i < 4; i++) v[i] = bfup(xv[i]);
      f32x2 s2 = (v[0] + v[1]) + (v[2] + v[3]);
      float mean = wave_sum(s2[0] + s2[1]) * (1.0f / D_);
      f32x2 q2 = {0, 0};
#pragma unroll
      for (int i = 0; i < 4; i++) { f32x2 d = v[i] - mean; q2 += d * d; }
      float var = wave_sum(q2[0] + q2[1]) * (1.0f / D_);
      float m = rbf(mean);
      float inv = __builtin_amdgcn_rcpf(rbf(__builtin_amdgcn_sqrtf(rbf(rbf(var) + 1e-5f))));
      u32x4 uo;
#pragma unroll
      for (int i = 0; i < 4; i++) {
        f32x2 u = bfup(pkbf2(bfup(pkbf2(v[i] - m)) * inv));
        uo[i] = pkh(u[0], u[1]);
      }
      *(u32x4*)(su + w * 1088 + lane * 16) = uo;
      *(u32x4*)(su + 8 * 1088 + w * 1088 + lane * 16) = xv;
    }
    STAMP(1);
    float dt = rbf(expf(bf2f(r_ldt)));
    float ar = dt * bf2f(r_are), ai = dt * bf2f(r_aim);
    cf CB = cmul(cf{bf2f(r_cre), bf2f(r_cim)}, cf{dt * bf2f(r_bre), dt * bf2f(r_bim)});
    float tl2 = ar * 1.4426950408889634f;
    const float I2PH = 0.15915494309189535f;
    const float I2PL = (float)(0.15915494309189533576888 - (double)0.15915494309189535f);
    float rh = ai * I2PH;
    float rl = __builtin_fmaf(ai, I2PH, -rh) + ai * I2PL;
    {
      float ax = fabsf(CB.x), ay = fabsf(CB.y);
      float mx = wave_max(fmaxf(ax, ay)), bs = wave_sum(ax + ay), isx;
      float sx = pow2_scale(mx, 13, isx);
      CB = cscale(CB, sx);
      if (role == 0 && lane == 0) { ms[chl * 2 + (w & 1)] = bs; ms[4 + chl * 2 + (w & 1)] = isx; }
    }
    lds_bar();
    {
      const int q4 = (lane & 15) >> 2, pp = lane & 3;
      const char* a = su + q4 * 1088 + (64 * w + 16 * g + 4 * pp) * 2;
      u32x2 u0 = tr_read(a), u1 = tr_read(a + 4 * 1088), x0 = tr_read(a + 8 * 1088), x1 = tr_read(a + 12 * 1088);
      const u32 dst = WS_U8 + bid * 16384 + tid * 32;
      st16_dev(wsr, dst, u32x4{u0[0], u0[1], u1[0], u1[1]});
      st16_dev(wsr, dst + 16, u32x4{x0[0], x0[1], x1[0], x1[1]});
    }
    STAMP(2);
    char* img = smem + chl * KG_CH;
    {
      char* wp = img + n * 4;
      if (role == 0) {
        cf e3 = cexpk(tl2, -rh, -rl, 8.f), e4 = cexpk(tl2, -rh, -rl, 16.f), e5 = cexpk(tl2, -rh, -rl, 32.f), e6 = cexpk(tl2, -rh, -rl, 64.f);
        cf y[8];
        y[0] = {16384.f, 0.f}; y[1] = cscale(e3, 16384.f); y[2] = cscale(e4, 16384.f); y[3] = cmul(y[2], e3);
        y[4] = cscale(e5, 16384.f); y[5] = cmul(y[4], e3); y[6] = cmul(y[4], e4); y[7] = cmul(y[6], e3);
#pragma unroll
        for (int j = 0; j < 8; j++) st_y(wp + (41 + j) * KG_ROW, y[j]);
#pragma unroll
        for (int j = 0; j < 8; j++) st_y(wp + (49 + j) * KG_ROW, j ? cmul(y[j], e6) : cscale(e6, 16384.f));
        cf e7 = cexpk(tl2, rh, rl, 128.f), e8 = cexpk(tl2, rh, rl, 256.f), e9 = cexpk(tl2, rh, rl, 512.f);
        cf e10 = cexpk(tl2, rh, rl, 1024.f), e11 = cexpk(tl2, rh, rl, 2048.f);
        cf g8 = cmul(CB, e10), g12 = cmul(g8, e9), g16 = cmul(CB, e11);
        cf g10 = cmul(g8, e8), g14 = cmul(g12, e8);
        st_x(wp + 32 * KG_ROW, g8); st_x(wp + 33 * KG_ROW, cmul(g8, e7));
        st_x(wp + 34 * KG_ROW, g10); st_x(wp + 35 * KG_ROW, cmul(g10, e7));
        st_x(wp + 36 * KG_ROW, g12); st_x(wp + 37 * KG_ROW, cmul(g12, e7));
        st_x(wp + 38 * KG_ROW, g14); st_x(wp + 39 * KG_ROW, cmul(g14, e7));
        st_x(wp + 40 * KG_ROW, g16);
      } else {
        cf e0 = cexpk(tl2, rh, rl, 1.f), e1 = cexpk(tl2, rh, rl, 2.f), e2 = cexpk(tl2, rh, rl, 4.f);
        cf e7 = cexpk(tl2, rh, rl, 128.f), e8 = cexpk(tl2, rh, rl, 256.f), e9 = cexpk(tl2, rh, rl, 512.f);
        cf eb[8];
        eb[1] = e0; eb[2] = e1; eb[3] = cmul(e1, e0); eb[4] = e2; eb[5] = cmul(e2, e0); eb[6] = cmul(e2, e1); eb[7] = cmul(eb[6], e0);
        cf gq[8];
        gq[0] = CB; gq[1] = cmul(CB, e7); gq[2] = cmul(CB, e8); gq[3] = cmul(gq[2], e7);
        gq[4] = cmul(CB, e9); gq[5] = cmul(gq[4], e7); gq[6] = cmul(gq[4], e8); gq[7] = cmul(gq[6], e7);
#pragma unroll
        for (int q = 0; q < 2; q++)
#pragma unroll
          for (int b = 0; b < 8; b++) st_x(wp + (q * 8 + b) * KG_ROW, b ? cmul(gq[q], eb[b]) : gq[q]);
#pragma unroll
        for (int q = 2; q < 4; q++)
#pragma unroll
          for (int b = 0; b < 4; b++) st_x(wp + (16 + (q - 2) * 4 + b) * KG_ROW, b ? cmul(gq[q], eb[2 * b]) : gq[q]);
#pragma unroll
        for (int q = 4; q < 8; q++)
#pragma unroll
          for (int b = 0; b < 2; b++) st_x(wp + (24 + (q - 4) * 2 + b) * KG_ROW, b ? cmul(gq[q], e2) : gq[q]);
      }
    }
    STAMP(3);
    wait_vm_lgkm<0>(); bar();
    if (tid < 16) st8_dev(wsr, FL_1 + tid * 2048 + bid * 8, tag);
    char* kt = smem + P0_KT + chl * 12288;
    float kinv;
    const float kscale = pow2_scale(ms[chl * 2] + ms[chl * 2 + 1], 14, kinv);
    {
      const int wv = w & 3;
      const char* pa = img + r16 * KG_ROW + wv * 128 + g * 16;
      u32x4 a[3][2], yh[2], yl[2];
#pragma unroll
      for (int s = 0; s < 2; s++) {
#pragma unroll
        for (int t = 0; t < 3; t++) a[t][s] = *(const u32x4*)(pa + t * 16 * KG_ROW + s * 64);
        yh[s] = *(const u32x4*)(pa + 41 * KG_ROW + s * 64);
        yl[s] = *(const u32x4*)(pa + 57 * KG_ROW + s * 64);
      }
      const float f = ms[4 + chl * 2 + (wv >> 1)] * (kscale * (1.0f / 16384.f));
      char* kw = kt + (4 * g * 16 + r16) * 16 + wv * 4;
      f32x4 c[3];
#pragma unroll
      for (int t = 0; t < 3; t++) { mmah0(c[t], a[t][0], yl[0]); mmah(c[t], a[t][1], yl[1]); }
#pragma unroll
      for (int t = 0; t < 3; t++) { mmah(c[t], a[t][0], yh[0]); mmah(c[t], a[t][1], yh[1]); }
      mfence(); pin(c[0]); pin(c[1]); pin(c[2]);
#pragma unroll
      for (int t = 0; t < 3; t++)
#pragma unroll
        for (int r = 0; r < 4; r++) *(float*)(kw + (t * 16 + r) * 256) = c[t][r] * f;
    }
    lds_bar();
    STAMP(4);
    {
      u32x4 o;
#pragma unroll
      for (int i = 0; i < 4; i++) {
        f32x4 a = *(const f32x4*)(kt + (kidx[i] & 0xffffu)), b = *(const f32x4*)(kt + (kidx[i] >> 16));
        o[i] = pkh((a[0] + a[1]) + (a[2] + a[3]), (b[0] + b[1]) + (b[2] + b[3]));
      }
      const int e = tid & 255;
      char* base = smem + chl * CV_CH;
      *(u32x4*)(base + CV_KP + (30 + e) * 16) = o;
      if (e < 30) *(u32x4*)(base + CV_KP + e * 16) = u32x4{0, 0, 0, 0};
      else if (e < 34) *(u32x4*)(base + (e - 30) * CV_COPY) = u32x4{0, 0, 0, 0};
      else if (e == 34) *(float*)(base + CV_KP + 4576) = kinv;
    }
  }
  STAMP(5);
  if (w == 0) {
    poll256(wsr, FL_1 + (bid & 15) * 2048, tag, lane);
    if (bid == 0 && lane == 0) st8_dev(wsr, FL_EPOCH, tag);
  }
  bar();
  STAMP(6);
  {
    {
      const int chl = w >> 2, q = tid & 255;
      char* base = smem + chl * CV_CH;
      {
        const char* sa = p.ws + WS_U8 + (size_t)w * 16 * 16384 + cp * 64;
        const u32 vo = (u32)((lane >> 2) * 16384 + (lane & 3) * 16);
        glds<CV_UI, 0>(vo, sa, lds0 + w * 1024); glds<CV_UI + 8192, 128 * 16384>(vo, sa, lds0 + w * 1024);
      }
      wait_vm<0>(); bar();
      const char* ui = smem + CV_UI + q * 64 + chl * 32;
      u32x4 cur = *(const u32x4*)ui, prv = *(const u32x4*)(ui - 64);
      if (q == 0) prv = u32x4{0, 0, 0, 0};
      u32 wd[8] = {prv[0], prv[1], prv[2], prv[3], cur[0], cur[1], cur[2], cur[3]};
      u32 o[5];
#pragma unroll
      for (int k = 0; k < 5; k++) o[k] = __builtin_amdgcn_alignbit(wd[k + 1], wd[k], 16);
      char* d = base + (q + 1) * 16;
      *(u32x4*)(d) = u32x4{o[0], o[1], o[2], o[3]};
      *(u32x4*)(d + CV_COPY) = u32x4{wd[1], wd[2], wd[3], wd[4]};
      *(u32x4*)(d + 2 * CV_COPY) = u32x4{o[1], o[2], o[3], o[4]};
      *(u32x4*)(d + 3 * CV_COPY) = u32x4{wd[2], wd[3], wd[4], wd[5]};
      if (q == 255) {
        u32 o4 = __builtin_amdgcn_alignbit(cur[1], cur[0], 16), o5 = __builtin_amdgcn_alignbit(cur[2], cur[1], 16);
        u32 o6 = __builtin_amdgcn_alignbit(cur[3], cur[2], 16), o7 = cur[3] >> 16;
        char* d2 = base + 257 * 16;
        *(u32x4*)(d2) = u32x4{o4, o5, o6, o7};
        *(u32x4*)(d2 + CV_COPY) = u32x4{cur[1], cur[2], cur[3], 0};
        *(u32x4*)(d2 + 2 * CV_COPY) = u32x4{o5, o6, o7, 0};
        *(u32x4*)(d2 + 3 * CV_COPY) = u32x4{cur[2], cur[3], 0, 0};
      }
    }
    const int chl = (w >> 1) & 1, hh = w & 1;
    const float dsk = bf2f(p.dskip[cp * 2 + chl]);
    const char* base = smem + chl * CV_CH;
    const int j2 = 2 * (hh + 2 * (w >> 2));
    char* red = smem + CV_RED + chl * 16384 + lane * 16;
    lds_bar();
    STAMP(7);
    f32x4 f0 = {0, 0, 0, 0}, f1 = {0, 0, 0, 0};
    if (w < 4) {
      f32x4 acc[8];
      const char* pa = base + (r16 & 3) * CV_COPY + 48 - 16 * g + 2 * (r16 & 12) + 256 * hh;
      const char* pb = base + CV_KP + 448 + 32 * r16 + 16 * g - 256 * hh;
      u32x4 A0[8], B0[8], A1[8], B1[8];
#pragma unroll
      for (int i = 0; i < 8; i++) conv_load1(pa, pb, 0, i, A0, B0);
      conv_step(pa, pb, 1, A0, B0, A1, B1, acc);
      conv_step(pa, pb, 2, A1, B1, A0, B0, acc);
      conv_step(pa, pb, 3, A0, B0, A1, B1, acc);
      conv_step(pa, pb, 4, A1, B1, A0, B0, acc);
      mfence();
#pragma unroll
      for (int i = 0; i < 8; i++) pin(acc[i]);
      STAMP(27);
      char* d = red + hh * 8192;
      if (hh) { *(f32x4*)(d) = acc[0]; *(f32x4*)(d + 1024) = acc[1]; f0 = acc[2]; f1 = acc[3]; }
      else { *(f32x4*)(d + 2048) = acc[2]; *(f32x4*)(d + 3072) = acc[3]; f0 = acc[0]; f1 = acc[1]; }
#pragma unroll
      for (int t = 4; t < 8; t++) *(f32x4*)(d + t * 1024) = acc[t];
    } else {
      w64_issue<D_>(p.w1 + col0, lds0 + GA_B, w - 4, lane);
      w64_issue<D_>(p.w1 + col0, lds0 + GA_B, w, lane);
    }
    const float kinv = *(const float*)(base + CV_KP + 4576);
    const int l0 = 256 * j2 + 16 * r16 + 4 * g;
    u32x2 xx[2];
    u32 dd[2][2];
#pragma unroll
    for (int k = 0; k < 2; k++) {
      const char* ui = smem + CV_UI + ((l0 + 256 * k) >> 3) * 64 + chl * 32 + (l0 & 4) * 2;
      u32x2 uu = *(const u32x2*)ui;
      xx[k] = *(const u32x2*)(ui + 16);
      dd[k][0] = pkbf2(hup(uu[0]) * dsk); dd[k][1] = pkbf2(hup(uu[1]) * dsk);
    }
    wait_vm_lgkm<0>(); bar();
    STAMP(8);
    if (w < 4) {
      f0 += *(const f32x4*)(red + (hh ^ 1) * 8192 + j2 * 1024);
      f1 += *(const f32x4*)(red + (hh ^ 1) * 8192 + j2 * 1024 + 1024);
    } else {
      f0 = *(const f32x4*)(red + j2 * 1024) + *(const f32x4*)(red + 8192 + j2 * 1024);
      f1 = *(const f32x4*)(red + j2 * 1024 + 1024) + *(const f32x4*)(red + 8192 + j2 * 1024 + 1024);
    }
    char* stg = smem + CV_STG;
#pragma unroll
    for (int k = 0; k < 2; k++) {
      f32x4 f = k ? f1 : f0;
      u32 y01 = pkbf2(f32x2{f[0], f[1]} * kinv), y23 = pkbf2(f32x2{f[2], f[3]} * kinv);
      u32 s01 = pkbf2(bfup(y01) + bfup(dd[k][0])), s23 = pkbf2(bfup(y23) + bfup(dd[k][1]));
      u32 h01 = pkbf2(bfup(xx[k][0]) + bfup(s01)), h23 = pkbf2(bfup(xx[k][1]) + bfup(s23));
      *(u32x2*)(stg + (16 * (j2 + k) + r16) * 80 + chl * 32 + g * 8) = u32x2{h01, h23};
    }
    lds_bar();
    {
      const int lb = tid >> 2, pc = tid & 3;
      st16_dev(wsr, WS_HT + cp * 8192 + tid * 16, *(const u32x4*)(stg + lb * 80 + pc * 16));
    }
  }
  const int mt = w & 3, wn = w >> 2;
  const int rt = (pm >> 1) * 8 + (pm & 1) * 2 + (mt >> 1) * 4 + (mt & 1);
  const int pc4 = (g & 1) * 2 + (g >> 1);
  const u32 boff = (u32)((rt * 16 + pn * 2 + wn) * 1024 + r16 * 64 + pc4 * 16);
  u32x4 ONES4 = {ONES, ONES, ONES, ONES};
  asm volatile("s_nop 1" : "+v"(ONES4));
  const u32 dw = lds0 + w * 1024;
  const u32 vbD = kn_voff(D_, w, lane);
  u32x4 hres, xres;
  u32x4 B[16][2];
  constexpr int CBD = 128 * D_, CB3 = 128 * F_;
  const int idx3 = bid >> 3;
  const bool has3 = idx3 < 28;
  const int pm3 = (bid & 7) * 2 + (idx3 >= 14 ? 1 : 0), pn3 = idx3 >= 14 ? idx3 - 14 : idx3;
  const u16* sb3 = p.w3 + (has3 ? pn3 * 128 : 0);
  const u32 vbF = kn_voff(F_, w, lane);
  const u32 vbF4 = kn_voff(F_, w - 4, lane);
  const int wm3 = w & 1, wn3 = w >> 1;
  const int ob0 = (kn_base(lane) + (wn3 >> 1) * 8192) ^ ((2 * (wn3 & 1)) << 5), ob1 = ob0 ^ 32;
  f32x4 cw0, cw1;
  u32x4 Bt[2][8];
  const char* pb0 = smem + GA_B + (kn_base(lane) ^ ((2 * wn) << 5));
  const char* pb1 = smem + GA_B + (kn_base(lane) ^ ((2 * wn + 1) << 5));
  auto ldB = [&](int j) {
#pragma unroll
    for (int i = 0; i < 4; i++) { Bt[j & 1][2 * i] = kn_frag(pb0 + (4 * j + i) * 4096); Bt[j & 1][2 * i + 1] = kn_frag(pb1 + (4 * j + i) * 4096); }
    SCHED();
  };
  auto ldB3 = [&](int s0, int n, int o) {
#pragma unroll
    for (int s = 0; s < 8; s++) if (s < n) {
      B[s0 + s][0] = kn_frag(smem + o + (s >> 1) * 16384 + (s & 1) * 4096 + ob0);
      B[s0 + s][1] = kn_frag(smem + o + (s >> 1) * 16384 + (s & 1) * 4096 + ob1);
    }
#pragma unroll
    for (int s = 0; s < 8; s++) if (s < n) { mmaz(s0 + s == 0, cw0, B[s0 + s][0], ONES4); mmaz(s0 + s == 0, cw1, B[s0 + s][1], ONES4); }
  };
#define W3C(c, dst) glds<(dst), (c) * CB3>(vbF, sb3, dw); glds<(dst) + 8192, (c) * CB3 + 128>(vbF, sb3, dw);
#define W3C4(c, dst) W3C(c, dst) glds<(dst), (c) * CB3>(vbF4, sb3, dw - 4096); glds<(dst) + 8192, (c) * CB3 + 128>(vbF4, sb3, dw - 4096);
  {
    f32x4 cv0, cv1;
    {
#pragma unroll
      for (int i = 0; i < 4; i++) { Bt[0][2 * i] = kn_frag(pb0 + (4 * mt + i) * 4096); Bt[0][2 * i + 1] = kn_frag(pb1 + (4 * mt + i) * 4096); }
#pragma unroll
      for (int i = 0; i < 4; i++) { mmaz(i == 0, cv0, Bt[0][2 * i], ONES4); mmaz(i == 0, cv1, Bt[0][2 * i + 1], ONES4); }
      mfence(); pin(cv0); pin(cv1);
      char* ct = smem + 65536 + wn * 128 + g * 16;
      if (r16 == 0) { *(f32x4*)(ct + mt * 256) = cv0; *(f32x4*)(ct + mt * 256 + 64) = cv1; }
      lds_bar();
      cv0 = (*(const f32x4*)(ct) + *(const f32x4*)(ct + 256)) + (*(const f32x4*)(ct + 512) + *(const f32x4*)(ct + 768));
      cv1 = (*(const f32x4*)(ct + 64) + *(const f32x4*)(ct + 320)) + (*(const f32x4*)(ct + 576) + *(const f32x4*)(ct + 832));
    }
    wait_vm_lgkm<0>(); bar();
    if (tid < 16) st8_dev(wsr, FL_2 + tid * 2048 + bid * 8, tag);
    STAMP(9);
    if (w >= 4) { W3C4(0, 65536) W3C4(1, 81920) }
    ldB(0); ldB(1);
    if (w == 0) poll256(wsr, FL_2 + (bid & 15) * 2048, tag, lane);
    bar();
    STAMP(10);
    {
      const char* sa = p.ws + WS_HT + rt * 64 + (size_t)wn * 262144;
      const int j2 = lane ^ (((lane >> 4) & 1) << 3);
      const u32 vo = (u32)((j2 >> 2) * 8192 + (j2 & 3) * 16);
      const u32 d = lds0 + GA_A + mt * 16384 + wn * 2048;
      glds<0, 0>(vo, sa, d); glds<1024, 131072>(vo, sa, d); glds<4096, 4 * 131072>(vo, sa, d); glds<5120, 5 * 131072>(vo, sa, d);
      glds<8192, 8 * 131072>(vo, sa, d); glds<9216, 9 * 131072>(vo, sa, d); glds<12288, 12 * 131072>(vo, sa, d); glds<13312, 13 * 131072>(vo, sa, d);
    }
    const char* aw = smem + GA_A + mt * 16384;
    const int ab = g * 256 + ((g & 1) << 7) + ((lane & 15) >> 2) * 32 + (lane & 3) * 8;
    const char* pa0 = aw + ab;
    const char* pa1 = aw + (ab ^ 128);
    f32x4 acc0, acc1, sacc, gacc;
    u32x4 a[2][4];
    auto ld = [&](int j) {
#pragma unroll
      for (int i = 0; i < 4; i++) {
        u32x2 x0 = tr_read(pa0 + (4 * j + i) * 1024), x1 = tr_read(pa1 + (4 * j + i) * 1024);
        a[j & 1][i] = u32x4{x0[0], x0[1], x1[0], x1[1]};
      }
      SCHED();
    };
    auto mm = [&](int j) {
#pragma unroll
      for (int i = 0; i < 4; i++) {
        mmaz(j + i == 0, sacc, ONES4, a[j & 1][i]);
        mmaz(j + i == 0, gacc, a[j & 1][i], a[j & 1][i]);
        mmaz(j + i == 0, acc0, Bt[j & 1][2 * i], a[j & 1][i]);
        mmaz(j + i == 0, acc1, Bt[j & 1][2 * i + 1], a[j & 1][i]);
      }
      SCHED();
    };
    SCHED();
    wait_vm<6>(); bar(); ld(0);
    STAMP(23);
    wait_vm<4>(); bar(); ld(1); mm(0); ldB(2);
    wait_vm<2>(); bar(); ld(2); mm(1); ldB(3);
    wait_vm<0>(); bar(); ld(3);
    STAMP(24);
    const int ab2 = pc4 * 256 + ((pc4 & 1) << 7) + ((lane & 15) >> 2) * 32 + (lane & 3) * 8;
    const char* hs = aw + (2 * pn + wn) * 1024;
    u32x2 hx0 = tr_read(hs + ab2), hx1 = tr_read(hs + (ab2 ^ 128));
    SCHED(); mm(2); mm(3);
    mfence(); pin(acc0); pin(acc1); pin(sacc); pin(gacc);
    STAMP(11);
    float dg = (r16 & 2) ? ((r16 & 1) ? gacc[3] : gacc[2]) : ((r16 & 1) ? gacc[1] : gacc[0]);
    f32x2 sa2 = {sacc[0], gsum(g == (r16 >> 2) ? dg : 0.f)};
    float mean1 = sa2[0] * (1.0f / D_);
    float var1 = fmaxf(sa2[1] * (1.0f / D_) - mean1 * mean1, 0.f);
    float m1 = rbf(mean1);
    float sd1 = rbf(__builtin_amdgcn_sqrtf(rbf(rbf(var1) + 1e-5f)));
    float i1 = __builtin_amdgcn_rcpf(sd1);
    float m2 = rbf((mean1 - m1) * i1);
    float sd2 = rbf(__builtin_amdgcn_sqrtf(rbf(rbf(var1 * i1 * i1) + 1e-5f)));
    float rs = i1 * __builtin_amdgcn_rcpf(sd2), tt = -(m1 + m2 * sd1) * rs;
    {
      f32x4 rt4 = {rs, rs, tt, tt};
      pin(rt4);
      u32x2 o0 = act4(acc0, cv0, rt4), o1 = act4(acc1, cv1, rt4);
      st16_dev(wsr, WS_H1 + 16384 + boff, pair_swap(o0[0], o0[1], o1[0], o1[1]));
    }
    if (tid < 16) {
      const u32 o = (u32)((pn * 2 + (tid >> 3)) * 1024 + (tid & 7) * 16);
      if (pm == 0) st16_dev(wsr, WS_H1 + 14 * 64 + o, u32x4{0, 0, 0, 0});
      if (pm == 31) st16_dev(wsr, WS_H1 + 129 * 16384 + o, u32x4{0, 0, 0, 0});
    }
    hres = u32x4{hx0[0], hx0[1], hx1[0], hx1[1]};
#pragma unroll
    for (int i = 0; i < 4; i++) xres[i] = pkbf2(bfup(pkbf2(bfup(hres[i]) - m1)) * i1);
    SCHED();
    ldB3(0, 4, 65536);
    wait_vm_lgkm<0>(); bar();
    if (tid < 8) st8_dev(wsr, FL_3 + tid * 2048 + (pm * 8 + pn) * 8, tag);
    if (w >= 4) w64_issue4<D_>(p.w2 + col0, lds0 + GA_B, w, lane);
  }
  STAMP(12);
  const int ar = lane >> 3, asp = (lane & 7) ^ ar;
  const u32 apo = (u32)((asp >> 2) * 1024 + (asp & 3) * 16);
  {
    u32 vo[8];
#pragma unroll
    for (int k = 0; k < 8; k++) {
      const int sh = (k >> 1) == 0 ? -1 : (k >> 1) == 1 ? 1 : (k >> 1) == 2 ? -2 : 2;
      const int row = 16 + ar + (k & 1) * 8 + sh;
      vo[k] = (u32)((row >> 4) * 16384 + (row & 15) * 64) + apo;
    }
    if (w == 0) polln(wsr, FL_3 + pn * 2048, max((pm & ~1) - 1, 0) * 8, min((pm & ~1) + 3, 32) * 8, tag, lane);
    bar();
    STAMP(13);
    {
      const char* sa = p.ws + WS_H1 + (size_t)rt * 16384 + wn * 2048;
      const u32 d = lds0 + GA_A + mt * 16384 + wn * 2048;
      glds<0, 0>(vo[0], sa, d); glds<1024, 0>(vo[1], sa, d); glds<4096, 4096>(vo[2], sa, d); glds<5120, 4096>(vo[3], sa, d);
      glds<8192, 8192>(vo[4], sa, d); glds<9216, 8192>(vo[5], sa, d); glds<12288, 12288>(vo[6], sa, d); glds<13312, 12288>(vo[7], sa, d);
    }
    W3C(2, 65536) W3C(3, 81920)
    wait_vm<12>(); bar();
    ldB(0); ldB(1);
    const char* pa0 = smem + GA_A + mt * 16384 + mk_base(lane, 0);
    const char* pa1 = smem + GA_A + mt * 16384 + mk_base(lane, 1);
    f32x4 acc0, acc1;
    u32x4 a[2][4];
    auto ld = [&](int j) {
      a[j & 1][0] = *(const u32x4*)(pa0 + j * 4096); a[j & 1][1] = *(const u32x4*)(pa1 + j * 4096);
      a[j & 1][2] = *(const u32x4*)(pa0 + j * 4096 + 2048); a[j & 1][3] = *(const u32x4*)(pa1 + j * 4096 + 2048);
      SCHED();
    };
    auto mm = [&](int j) {
#pragma unroll
      for (int i = 0; i < 4; i++) {
        mmaz(j + i == 0, acc0, Bt[j & 1][2 * i], a[j & 1][i]);
        mmaz(j + i == 0, acc1, Bt[j & 1][2 * i + 1], a[j & 1][i]);
      }
      SCHED();
    };
    SCHED();
    wait_vm<10>(); bar(); ld(0);
    STAMP(25);
    wait_vm<8>(); bar(); ld(1); mm(0); ldB(2);
    wait_vm<6>(); bar(); ld(2); mm(1); ldB(3);
    wait_vm<4>(); bar(); ld(3);
    STAMP(26);
    mm(2); mm(3);
    mfence(); pin(acc0); pin(acc1);
    STAMP(14);
    u32x4 v = pair_swap(pkbf(acc0[0], acc0[1]), pkbf(acc0[2], acc0[3]), pkbf(acc1[0], acc1[1]), pkbf(acc1[2], acc1[3]));
#pragma unroll
    for (int i = 0; i < 4; i++) xres[i] = pkbf2(bfup(xres[i]) + bfup(v[i]));
    st16_dev(wsr, WS_X1 + boff, xres);
    float s1 = 0, s2 = 0;
#pragma unroll
    for (int i = 0; i < 4; i++) { s1 = dot2(xres[i], ONES, s1); s2 = dot2(xres[i], xres[i], s2); }
    s1 = gsum(s1); s2 = gsum(s2);
    if (g == 0) st8_dev(wsr, WS_ST + (rt * 16 + pn * 2 + wn) * 128 + r16 * 8, u32x2{__float_as_uint(s1), __float_as_uint(s2)});
    wait_vm<2>(); bar();
    ldB3(4, 4, 65536);
    wait_vm_lgkm<0>(); bar();
    if (tid < 8) st8_dev(wsr, FL_4 + tid * 2048 + (pm * 8 + pn) * 8, tag);
  }
  STAMP(15);
  const u16* sb4 = p.w4 + col0;
#define G4_R1A(i) glds<GA_B + (i) * 8192, (((i) >> 1) * 7 + ((i) & 1)) * CBD>(vbD, sb4, dw);
#define G4_R1A4(i) G4_R1A(i) glds<GA_B + (i) * 8192, (((i) >> 1) * 7 + ((i) & 1)) * CBD>(vbD4, sb4, dw - 4096);
#define G4_R1A_ALL4 G4_R1A4(0) G4_R1A4(1) G4_R1A4(2) G4_R1A4(3) G4_R1A4(4) G4_R1A4(5) G4_R1A4(6) G4_R1A4(7)
  const u32 vbD4 = kn_voff(D_, w - 4, lane);
#define G4_R1B(i) glds<(i) * 8192, (((i) >> 1) * 7 + 2 + ((i) & 1)) * CBD>(vbD, sb4, dw);
#define G4_R2POS(i) ((i) < 4 ? 32768 + (i) * 8192 : GA_B + ((i) - 4) * 8192)
#define G4_R2(i) glds<G4_R2POS(i), (((i) / 3) * 7 + 4 + ((i) % 3)) * CBD>(vbD, sb4, dw);
  if (has3) {
    const int wm = wm3;
    if (w >= 4) { W3C4(4, GA_B) W3C4(5, GA_B + 16384) W3C4(6, GA_B + 32768) W3C4(7, GA_B + 49152) }
    if (w == 0) polln(wsr, FL_4 + pn * 2048, pm3 * 16, pm3 * 16 + 16, tag, lane);
    bar();
    STAMP(16);
    const char* sa = p.ws + WS_X1 + (size_t)(pm3 * 8 + (w >> 1)) * 16384 + (w & 1) * 512;
    const u32 va = (u32)(ar * 64) + apo;
#define G3_A(i) glds<(((i) + 4) % 9) * 8192, ((i) >> 3) * 65536 + ((i) & 7) * 2048>(va, sa, dw);
    {
      const char* ss = p.ws + WS_ST + (size_t)pm3 * 16384 + w * 1024;
      glds<G3_ST, 0>(lane * 16, ss, dw); glds<G3_ST + 8192, 8192>(lane * 16, ss, dw);
    }
    G3_A(0) G3_A(1) G3_A(2) G3_A(3) G3_A(4) G3_A(5) G3_A(6) G3_A(7) G3_A(8)
    wait_vm<9>(); bar();
    ldB3(8, 8, GA_B);
    {
      const char* sst = smem + G3_ST + (tid >> 6) * 2048 + (tid & 3) * 512 + ((tid >> 2) & 15) * 8;
      f32x2 st = (*(const f32x2*)(sst) + *(const f32x2*)(sst + 128)) + (*(const f32x2*)(sst + 256) + *(const f32x2*)(sst + 384));
      float t1 = st[0] + dppf<0xB1>(st[0]), t2 = st[1] + dppf<0xB1>(st[1]);
      t1 += dppf<0x4E>(t1); t2 += dppf<0x4E>(t2);
      float mean = t1 * (1.0f / D_);
      float var = fmaxf(t2 * (1.0f / D_) - mean * mean, 0.f);
      float rs = __builtin_amdgcn_rcpf(rbf(__builtin_amdgcn_sqrtf(rbf(rbf(var) + 1e-5f))));
      if ((tid & 3) == 0) { float tt = -rbf(mean) * rs; *(f32x4*)(smem + G3_TAB + (tid >> 2) * 16) = f32x4{rs, rs, tt, tt}; }
    }
    const f32x4& cv0 = cw0;
    const f32x4& cv1 = cw1;
    float* rowt = (float*)(smem + G3_TAB);
    const int oa0 = mk_base(lane, 0) + wm * 4096, oa1 = mk_base(lane, 1) + wm * 4096;
    f32x4 acc[2][2][2];
    u32x4 a[2][4];
    auto ld = [&](int slot, u32x4 (&d)[4]) {
      const char* s = smem + slot * 8192;
      d[0] = *(const u32x4*)(s + oa0); d[1] = *(const u32x4*)(s + oa0 + 2048);
      d[2] = *(const u32x4*)(s + oa1); d[3] = *(const u32x4*)(s + oa1 + 2048);
      SCHED();
    };
    auto m1 = [&](int ps, int b, int i, const u32x4 (&d)[4]) {
      mmaz(b == 0 && i < 4, acc[ps][(i >> 1) & 1][i & 1], B[2 * b + (i >> 2)][(i >> 1) & 1], d[2 * (i >> 2) + (i & 1)]);
      SCHED();
    };
    auto mm = [&](int ps, int b, const u32x4 (&d)[4]) {
#pragma unroll
      for (int i = 0; i < 8; i++) m1(ps, b, i, d);
    };
    const u32 hoff = WS_HID + (u32)(((pm3 * 8 + 2 * wm) * 56 + pn3 * 4 + wn3) * 1024 + r16 * 64 + pc4 * 16);
    u32x2 o0;
    f32x4 rtv[2];
    auto ldrt = [&](int ps) {
      rtv[0] = *(const f32x4*)(rowt + (64 * ps + 32 * wm + r16) * 4);
      rtv[1] = *(const f32x4*)(rowt + (64 * ps + 32 * wm + 16 + r16) * 4);
    };
    auto ep = [&](int ps, int i) {
      const int mi = i >> 1;
      if ((i & 1) == 0) o0 = act4(acc[ps][0][mi], cv0, rtv[mi]);
      else {
        u32x2 o1 = act4(acc[ps][1][mi], cv1, rtv[mi]);
        st16_dev(wsr, hoff + (4 * ps + mi) * 57344, pair_swap(o0[0], o0[1], o1[0], o1[1]));
      }
    };
    float c0v = -2.0f * 0.7978845608028654f * 1.4426950408889634f;
    asm volatile("" : "+v"(c0v));
    auto mme = [&](int b, const u32x4 (&d)[4], int e) {
      const float c1 = -2.0f * 0.7978845608028654f * 1.4426950408889634f * 0.044715f;
      const int mi = e >> 1;
      const f32x4 aa = acc[0][e & 1][mi], cc = (e & 1) ? cv1 : cv0;
      float v[4], x[4], z[4];
      m1(1, b, 0, d);
#pragma unroll
      for (int q = 0; q < 4; q++) v[q] = smul(aa[q], rtv[mi][0]);
      SCHED(); m1(1, b, 1, d);
#pragma unroll
      for (int q = 0; q < 4; q++) v[q] = sfma(cc[q], rtv[mi][2], v[q]);
      u32 p0 = pkbf(v[0], v[1]), p1 = pkbf(v[2], v[3]);
      SCHED(); m1(1, b, 2, d);
      x[0] = bflo(p0); x[1] = bfhi(p0); x[2] = bflo(p1); x[3] = bfhi(p1);
      z[0] = smul(x[0], x[0]); z[1] = smul(x[1], x[1]);
      SCHED(); m1(1, b, 3, d);
      z[2] = smul(x[2], x[2]); z[3] = smul(x[3], x[3]);
#pragma unroll
      for (int q = 0; q < 4; q++) z[q] = sfmak(z[q], c1, c0v);
      SCHED(); m1(1, b, 4, d);
#pragma unroll
      for (int q = 0; q < 4; q++) z[q] = smul(z[q], x[q]);
      z[0] = __builtin_amdgcn_exp2f(z[0]); z[1] = __builtin_amdgcn_exp2f(z[1]);
      SCHED(); m1(1, b, 5, d);
      z[2] = __builtin_amdgcn_exp2f(z[2]); z[3] = __builtin_amdgcn_exp2f(z[3]);
#pragma unroll
      for (int q = 0; q < 4; q++) z[q] = sadd1(z[q]);
      SCHED(); m1(1, b, 6, d);
#pragma unroll
      for (int q = 0; q < 4; q++) z[q] = __builtin_amdgcn_rcpf(z[q]);
      SCHED(); m1(1, b, 7, d);
#pragma unroll
      for (int q = 0; q < 4; q++) x[q] = smul(x[q], z[q]);
      u32x2 o = {pkbf(x[0], x[1]), pkbf(x[2], x[3])};
      if (e & 1) st16_dev(wsr, hoff + mi * 57344, pair_swap(o0[0], o0[1], o[0], o[1])); else o0 = o;
      SCHED();
    };
    SCHED();
#define SL(i) (((i) + 4) % 9)
    wait_vm_lgkm<8>(); bar(); ld(SL(0), a[0]); G4_R1A(0)
    wait_vm_lgkm<8>(); bar(); ld(SL(1), a[1]); G4_R1A(1) mm(0, 0, a[0]); SCHED();
    wait_vm_lgkm<8>(); bar(); G3_A(9) ld(SL(2), a[0]); G4_R1A(2) mm(0, 1, a[1]); SCHED();
    wait_vm_lgkm<9>(); bar(); G3_A(10) ld(SL(3), a[1]); G4_R1A(3) mm(0, 2, a[0]); SCHED();
    wait_vm_lgkm<10>(); bar(); G3_A(11) ld(SL(4), a[0]); G4_R1A(4) mm(0, 3, a[1]); SCHED();
    wait_vm_lgkm<11>(); bar(); G3_A(12) ld(SL(5), a[1]); G4_R1A(5) mm(0, 4, a[0]); SCHED();
    wait_vm_lgkm<12>(); bar(); G3_A(13) ld(SL(6), a[0]); G4_R1A(6) mm(0, 5, a[1]); SCHED();
    wait_vm_lgkm<13>(); bar(); G3_A(14) ld(SL(7), a[1]); G4_R1A(7) mm(0, 6, a[0]); SCHED();
    wait_vm_lgkm<14>(); bar(); G3_A(15) ld(SL(8), a[0]); ldrt(0); mm(0, 7, a[1]); SCHED();
    STAMP(17);
    wait_vm_lgkm<12>(); bar(); ld(SL(9), a[1]); G4_R1B(2) mme(0, a[0], 0);
    wait_vm_lgkm<11>(); bar(); ld(SL(10), a[0]); G4_R1B(3) mme(1, a[1], 1);
    wait_vm_lgkm<11>(); bar(); ld(SL(11), a[1]); G4_R1B(4) mme(2, a[0], 2);
    wait_vm_lgkm<10>(); bar(); ld(SL(12), a[0]); G4_R1B(5) mme(3, a[1], 3);
    wait_vm_lgkm<10>(); bar(); ld(SL(13), a[1]); G4_R1B(6) mm(1, 4, a[0]); SCHED();
    wait_vm_lgkm<9>(); bar(); ld(SL(14), a[0]); G4_R1B(7) mm(1, 5, a[1]); SCHED();
    wait_vm_lgkm<2>(); bar();
    if (tid < 8) st8_dev(wsr, FL_5 + tid * 2048 + (pm3 * 14 + pn3) * 8, tag);
    ld(SL(15), a[1]); ldrt(1); mm(1, 6, a[0]); SCHED();
    lds_bar();
    G4_R1B(0) G4_R1B(1)
    mm(1, 7, a[1]);
    mfence();
#pragma unroll
    for (int i = 0; i < 4; i++) pin(acc[1][i >> 1][i & 1]);
    STAMP(18);
#pragma unroll
    for (int i = 0; i < 4; i++) ep(1, i);
    if (w == 0) polln(wsr, FL_5 + pn * 2048, (pm >> 1) * 14, (pm >> 1) * 14 + 14, tag, lane);
    wait_vm_lgkm<0>(); bar();
    if (tid < 8) st8_dev(wsr, FL_6 + tid * 2048 + (pm3 * 14 + pn3) * 8, tag);
  } else {
    G4_R1A(0) G4_R1A(1) G4_R1A(2) G4_R1A(3) G4_R1A(4) G4_R1A(5) G4_R1A(6) G4_R1A(7)
    G4_R1B(0) G4_R1B(1) G4_R1B(2) G4_R1B(3) G4_R1B(4) G4_R1B(5) G4_R1B(6) G4_R1B(7)
    if (w == 0) polln(wsr, FL_5 + pn * 2048, (pm >> 1) * 14, (pm >> 1) * 14 + 14, tag, lane);
    wait_vm<0>(); bar();
  }
  STAMP(19);
  {
    const int wm = mt >> 1, kq = (mt & 1) + 2 * wn;
    const int kb = kn_base(lane);
    u32x4(&B4)[8][4] = *reinterpret_cast<u32x4(*)[8][4]>(&B);
    const char* sa = p.ws + WS_HID + (size_t)((rt & ~1) * 56 + 14 * kq) * 1024;
    const u32 va = (u32)(ar * 64) + apo;
    const int ring = (wm ? 0 : 65536) + kq * 8192;
    const u32 da = lds0 + ring;
#define G4_A4(b) glds<((b) & 1) * 4096, (b) * 2048>(va, sa, da); glds<((b) & 1) * 4096 + 1024, (b) * 2048 + 512>(va, sa, da); \
                 glds<((b) & 1) * 4096 + 2048, (b) * 2048 + 57344>(va, sa, da); glds<((b) & 1) * 4096 + 3072, (b) * 2048 + 57344 + 512>(va, sa, da);
    if (wm == 0) { G4_A4(0) G4_A4(1) }
#pragma unroll
    for (int s = 0; s < 4; s++)
#pragma unroll
      for (int nt = 0; nt < 4; nt++) {
        B4[s][nt] = kn_frag(smem + GA_B + kq * 16384 + s * 4096 + (kb ^ (nt << 5)));
        B4[4 + s][nt] = kn_frag(smem + kq * 16384 + s * 4096 + (kb ^ (nt << 5)));
      }
    lds_bar();
    if (wm) {
      const u32 vb2 = kn_voff(D_, w - 2, lane), dw2 = dw - 2048;
#define G4_R2X(i) G4_R2(i) glds<G4_R2POS(i), (((i) / 3) * 7 + 4 + ((i) % 3)) * CBD>(vb2, sb4, dw2);
      polln(wsr, FL_6 + (pn ^ kq) * 2048, (pm >> 1) * 14, (pm >> 1) * 14 + 14, tag, lane);
      G4_A4(0) G4_A4(1)
      G4_R2X(0) G4_R2X(3) G4_R2X(6) G4_R2X(9) G4_R2X(1) G4_R2X(4) G4_R2X(7) G4_R2X(10) G4_R2X(2) G4_R2X(5) G4_R2X(8) G4_R2X(11)
    }
    STAMP(20);
    const char* pa0 = smem + ring + mk_base(lane, 0);
    const char* pa1 = smem + ring + mk_base(lane, 1);
    f32x4 acc[4][2];
    u32x4 a[4];
    auto ld = [&](int b) {
      a[0] = *(const u32x4*)(pa0 + (b & 1) * 4096); a[1] = *(const u32x4*)(pa0 + (b & 1) * 4096 + 2048);
      a[2] = *(const u32x4*)(pa1 + (b & 1) * 4096); a[3] = *(const u32x4*)(pa1 + (b & 1) * 4096 + 2048);
      asm volatile("s_waitcnt lgkmcnt(0)" ::: "memory");
      SCHED();
    };
    auto mm = [&](int s0, bool first = false) {
#pragma unroll
      for (int ks = 0; ks < 2; ks++)
#pragma unroll
        for (int nt = 0; nt < 4; nt++)
#pragma unroll
          for (int mi = 0; mi < 2; mi++) mmaz(first && ks == 0, acc[nt][mi], B4[s0 + ks][nt], a[2 * ks + mi]);
      SCHED();
    };
    auto ldb = [&](int j, int s0) {
      const int i = kq * 3 + j;
      const char* bl = smem + (i < 4 ? 32768 + i * 8192 : GA_B + (i - 4) * 8192);
#pragma unroll
      for (int ks = 0; ks < 2; ks++)
#pragma unroll
        for (int nt = 0; nt < 4; nt++) B4[s0 + ks][nt] = kn_frag(bl + ks * 4096 + (kb ^ (nt << 5)));
      SCHED();
    };
    if (wm) wait_vm<28>(); else wait_vm<4>();
    ld(0); G4_A4(2) mm(0, true);
    if (wm) wait_vm<28>(); else wait_vm<4>();
    ld(1); G4_A4(3) mm(2);
    wait_vm<4>();
    if (wm) bar();
    ld(2); G4_A4(4) mm(4);
    wait_vm<4>(); ld(3); G4_A4(5) mm(6);
    if (wm == 0) bar();
    ldb(0, 0); ldb(1, 2);
    wait_vm<4>(); ld(4); G4_A4(6) ldb(2, 4); mm(0);
    wait_vm<4>(); ld(5); mm(2);
    wait_vm<0>(); ld(6); mm(4);
    mfence();
#pragma unroll
    for (int i = 0; i < 8; i++) pin(acc[i >> 1][i & 1]);
    STAMP(21);
    {
      char* d = smem + ring + lane * 16;
#pragma unroll
      for (int k2 = 0; k2 < 4; k2++) {
        *(f32x4*)(d + k2 * 2048) = acc[2 * (k2 >> 1)][k2 & 1];
        *(f32x4*)(d + k2 * 2048 + 1024) = acc[2 * (k2 >> 1) + 1][k2 & 1];
      }
    }
    lds_bar();
    const char* s = smem + (wm ? 0 : 65536) + kq * 2048 + lane * 16;
    f32x4 f0 = (*(const f32x4*)(s) + *(const f32x4*)(s + 8192)) + (*(const f32x4*)(s + 16384) + *(const f32x4*)(s + 24576));
    f32x4 f1 = (*(const f32x4*)(s + 1024) + *(const f32x4*)(s + 9216)) + (*(const f32x4*)(s + 17408) + *(const f32x4*)(s + 25600));
    u32x4 v = pair_swap(pkbf(f0[0], f0[1]), pkbf(f0[2], f0[3]), pkbf(f1[0], f1[1]), pkbf(f1[2], f1[3]));
    u32x4 o;
#pragma unroll
    for (int i = 0; i < 4; i++) o[i] = pkbf2(bfup(hres[i]) + bfup(pkbf2(bfup(xres[i]) + bfup(v[i]))));
    *(u32x4*)(p.out + (size_t)(rt * 16 + r16) * D_ + col0 + 32 * wn + pc4 * 8) = o;
  }
  STAMP(22);
}

extern "C" void kernel_launch(void* const* d_in, const int* in_sizes, int n_in, void* d_out, int out_size, void* d_ws,
                              size_t ws_size, hipStream_t stream) {
  Params p;
  p.x = (const u16*)d_in[0]; p.are = (const u16*)d_in[1]; p.aim = (const u16*)d_in[2]; p.bre = (const u16*)d_in[3];
  p.bim = (const u16*)d_in[4]; p.cre = (const u16*)d_in[5]; p.cim = (const u16*)d_in[6]; p.logdt = (const u16*)d_in[7];
  p.dskip = (const u16*)d_in[8]; p.w1 = (const u16*)d_in[10]; p.w2 = (const u16*)d_in[11]; p.w3 = (const u16*)d_in[12];
  p.w4 = (const u16*)d_in[13]; p.out = (u16*)d_out; p.ws = (char*)d_ws;
  k_fused<<<256, 512, 0, stream>>>(p);
}
```

```cpp
#include <hip/hip_runtime.h>
#include <stdint.h>

typedef unsigned short u16;
typedef unsigned int u32;
typedef unsigned long long u64;
typedef __bf16 bf16x2 __attribute__((ext_vector_type(2)));
typedef __bf16 bf16x8 __attribute__((ext_vector_type(8)));
typedef _Float16 f16x2 __attribute__((ext_vector_type(2)));
typedef _Float16 f16x8 __attribute__((ext_vector_type(8)));
typedef float f32x2 __attribute__((ext_vector_type(2)));
typedef float f32x4 __attribute__((ext_vector_type(4)));
typedef short s16x4 __attribute__((ext_vector_type(4)));
typedef u32 u32x2 __attribute__((ext_vector_type(2)));
typedef u32 u32x4 __attribute__((ext_vector_type(4)));
#define LDSP __attribute__((address_space(3)))
#define DEV __device__ __forceinline__

constexpr int L_ = 2048, D_ = 512, NS_ = 128, F_ = 1792;
constexpr u32 WS_U8 = 0;
constexpr u32 WS_HT = 7u << 20;
constexpr u32 WS_H1 = 13u << 20;
constexpr u32 WS_X1 = 16u << 20;
constexpr u32 WS_ST = 18u << 20;
constexpr u32 WS_HID = 19u << 20;
constexpr u32 WS_FL = 28u << 20;
constexpr u32 FL_EPOCH = WS_FL, FL_1 = WS_FL + 4096, FL_2 = FL_1 + 32768, FL_3 = FL_2 + 32768, FL_4 = FL_3 + 16384, FL_5 = FL_4 + 16384, FL_6 = FL_5 + 16384;

struct Params {
  const u16 *x, *are, *aim, *bre, *bim, *cre, *cim, *logdt, *dskip, *w1, *w2, *w3, *w4;
  u16* out;
  char* ws;
};

DEV float bf2f(u32 v) { return __uint_as_float(v << 16); }
DEV float bflo(u32 v) { return __uint_as_float(v << 16); }
DEV float bfhi(u32 v) { return __uint_as_float(v & 0xffff0000u); }
DEV f32x2 bfup(u32 v) { return f32x2{bflo(v), bfhi(v)}; }
DEV u32 pkbf2(f32x2 v) { return __builtin_bit_cast(u32, __builtin_convertvector(v, bf16x2)); }
DEV u32 pkbf(float a, float b) { return pkbf2(f32x2{a, b}); }
DEV u32 pkh(float a, float b) { f32x2 v = {a, b}; return __builtin_bit_cast(u32, __builtin_convertvector(v, f16x2)); }
DEV float rbf(float a) { return bflo(pkbf(a, a)); }
DEV f32x2 hup(u32 v) { return __builtin_convertvector(__builtin_bit_cast(f16x2, v), f32x2); }

template <int CTRL> DEV float dppf(float v) {
  return __builtin_bit_cast(float, __builtin_amdgcn_update_dpp(0, __builtin_bit_cast(int, v), CTRL, 0xf, 0xf, true));
}
DEV float gsum(float v) {
  u32 a = __float_as_uint(v);
  auto r = __builtin_amdgcn_permlane16_swap(a, a, false, false);
  v = __uint_as_float(r[0]) + __uint_as_float(r[1]);
  a = __float_as_uint(v);
  auto q = __builtin_amdgcn_permlane32_swap(a, a, false, false);
  return __uint_as_float(q[0]) + __uint_as_float(q[1]);
}
DEV float gmax(float v) {
  u32 a = __float_as_uint(v);
  auto r = __builtin_amdgcn_permlane16_swap(a, a, false, false);
  v = fmaxf(__uint_as_float(r[0]), __uint_as_float(r[1]));
  a = __float_as_uint(v);
  auto q = __builtin_amdgcn_permlane32_swap(a, a, false, false);
  return fmaxf(__uint_as_float(q[0]), __uint_as_float(q[1]));
}
DEV float wave_sum(float v) {
  v += dppf<0xB1>(v); v += dppf<0x4E>(v); v += dppf<0x141>(v); v += dppf<0x140>(v);
  return gsum(v);
}
DEV float wave_max(float v) {
  v = fmaxf(v, dppf<0xB1>(v)); v = fmaxf(v, dppf<0x4E>(v)); v = fmaxf(v, dppf<0x141>(v)); v = fmaxf(v, dppf<0x140>(v));
  return gmax(v);
}
DEV u32x2 tr_read(const char* p) {
  return __builtin_bit_cast(u32x2, __builtin_amdgcn_ds_read_tr16_b64_v4i16((LDSP s16x4*)p));
}
DEV void mma(f32x4& c, u32x4 a, u32x4 b) { asm volatile("v_mfma_f32_16x16x32_bf16 %0, %1, %2, %0" : "+v"(c) : "v"(a), "v"(b)); }
DEV void mma0(f32x4& c, u32x4 a, u32x4 b) { asm volatile("v_mfma_f32_16x16x32_bf16 %0, %1, %2, 0" : "=&v"(c) : "v"(a), "v"(b)); }
DEV void mmah(f32x4& c, u32x4 a, u32x4 b) { asm volatile("v_mfma_f32_16x16x32_f16 %0, %1, %2, %0" : "+v"(c) : "v"(a), "v"(b)); }
DEV void mmah0(f32x4& c, u32x4 a, u32x4 b) { asm volatile("v_mfma_f32_16x16x32_f16 %0, %1, %2, 0" : "=&v"(c) : "v"(a), "v"(b)); }
DEV void mmaz(bool first, f32x4& c, u32x4 a, u32x4 b) { if (first) mma0(c, a, b); else mma(c, a, b); }
DEV void mfence() { asm volatile("s_nop 7" ::: "memory"); __builtin_amdgcn_sched_barrier(0); }
DEV float smul(float a, float b) { float r; asm("v_mul_f32 %0, %1, %2" : "=v"(r) : "v"(a), "v"(b)); return r; }
DEV float sfma(float a, float b, float c) { float r; asm("v_fma_f32 %0, %1, %2, %3" : "=v"(r) : "v"(a), "v"(b), "v"(c)); return r; }
DEV float sfmak(float a, float k1, float k0) { float r; asm("v_fma_f32 %0, %1, %2, %3" : "=v"(r) : "v"(a), "s"(k1), "v"(k0)); return r; }
DEV float sadd1(float a) { float r; asm("v_add_f32 %0, 1.0, %1" : "=v"(r) : "v"(a)); return r; }
DEV void pin(f32x4& c) { asm volatile("" : "+v"(c)); }
DEV float dot2(u32 a, u32 b, float c) {
  return __builtin_amdgcn_fdot2_f32_bf16(__builtin_bit_cast(bf16x2, a), __builtin_bit_cast(bf16x2, b), c, false);
}
template <int LOFF, int MOFF> DEV void glds(u32 voff, const void* sbase, u32 lds) {
  asm volatile("s_add_u32 m0, %1, %3\n\ts_nop 0\n\tglobal_load_lds_dwordx4 %0, %2"
               :: "v"(voff + (u32)MOFF), "s"(lds), "s"(sbase), "n"(LOFF) : "memory", "m0", "scc");
}
DEV void touch(u32& t, const void* sbase, u32 voff) {
  asm volatile("global_load_dword %0, %1, %2" : "+v"(t) : "v"(voff), "s"(sbase) : "memory");
}
DEV void keep(u32 t) { asm volatile("" :: "v"(t)); }
DEV u32 lds_addr(const char* p) { return (u32)(size_t)(LDSP const char*)p; }
#define SCHED() __builtin_amdgcn_sched_barrier(0)
template <int N> DEV void wait_vm() { asm volatile("s_waitcnt vmcnt(%0)" ::"n"(N) : "memory"); }
template <int N> DEV void wait_vm_lgkm() { asm volatile("s_waitcnt vmcnt(%0) lgkmcnt(0)" ::"n"(N) : "memory"); }
DEV void bar() { __builtin_amdgcn_s_barrier(); }
DEV void lds_bar() { asm volatile("s_waitcnt lgkmcnt(0)" ::: "memory"); __builtin_amdgcn_s_barrier(); }

#define STAMP(i)

typedef __amdgpu_buffer_rsrc_t rsrc_t;
DEV void st16_dev(rsrc_t r, u32 off, u32x4 v) { __builtin_amdgcn_raw_buffer_store_b128(v, r, off, 0, 16); }
DEV void st8_dev(rsrc_t r, u32 off, u32x2 v) { __builtin_amdgcn_raw_buffer_store_b64(v, r, off, 0, 16); }
DEV u32x4 ld16_dev(rsrc_t r, u32 off) { return __builtin_amdgcn_raw_buffer_load_b128(r, off, 0, 16); }
DEV u32x2 ld8_dev(rsrc_t r, u32 off) { return __builtin_amdgcn_raw_buffer_load_b64(r, off, 0, 16); }

constexpr int SPIN_MAX = 1 << 18;
DEV void poll256(rsrc_t r, u32 base, u32x2 tag, int lane) {
  for (int s = 0; s < SPIN_MAX; s++) {
    u32x4 a = ld16_dev(r, base + lane * 32), b = ld16_dev(r, base + lane * 32 + 16);
    bool ok = a[0] == tag[0] && a[1] == tag[1] && a[2] == tag[0] && a[3] == tag[1] &&
              b[0] == tag[0] && b[1] == tag[1] && b[2] == tag[0] && b[3] == tag[1];
    if (__all(ok)) break;
    __builtin_amdgcn_s_sleep(2);
  }
}
DEV void polln(rsrc_t r, u32 base, int lo, int hi, u32x2 tag, int lane) {
  const int i = min(lo + lane, hi - 1);
  for (int s = 0; s < SPIN_MAX; s++) {
    u32x2 a = ld8_dev(r, base + i * 8);
    if (__all(a[0] == tag[0] && a[1] == tag[1])) break;
    __builtin_amdgcn_s_sleep(2);
  }
}

DEV u32x2 act4(f32x4 a, f32x4 cs, f32x4 rt) {
  const float c0 = -2.0f * 0.7978845608028654f * 1.4426950408889634f;
  const float c1 = c0 * 0.044715f;
  const f32x2 rs = {rt[0], rt[1]}, t = {rt[2], rt[3]};
  f32x2 v0 = f32x2{a[0], a[1]} * rs + f32x2{cs[0], cs[1]} * t, v1 = f32x2{a[2], a[3]} * rs + f32x2{cs[2], cs[3]} * t;
  f32x4 v = {v0[0], v0[1], v1[0], v1[1]};
  u32 p0 = pkbf(v[0], v[1]), p1 = pkbf(v[2], v[3]);
  f32x4 x = {bflo(p0), bfhi(p0), bflo(p1), bfhi(p1)};
  f32x4 z = x * (x * x * c1 + c0);
  f32x4 e = {__builtin_amdgcn_exp2f(z[0]), __builtin_amdgcn_exp2f(z[1]), __builtin_amdgcn_exp2f(z[2]), __builtin_amdgcn_exp2f(z[3])};
  e += 1.0f;
  f32x4 r = {__builtin_amdgcn_rcpf(e[0]), __builtin_amdgcn_rcpf(e[1]), __builtin_amdgcn_rcpf(e[2]), __builtin_amdgcn_rcpf(e[3])};
  x *= r;
  return u32x2{pkbf(x[0], x[1]), pkbf(x[2], x[3])};
}

constexpr int LDS_ALL = 163840;
constexpr int KG_ROW = 528;
constexpr int KG_CH = 73 * KG_ROW;
constexpr int P0_KT = 77312;
constexpr int P0_LN = 101888;
constexpr int P0_MS = 119296;
constexpr int CV_COPY = 4160, CV_KP = 4 * CV_COPY, CV_CH = CV_KP + 4608;
constexpr int CV_UI = 2 * CV_CH;
constexpr int CV_RED = CV_UI + 16384;
constexpr int CV_STG = 0;
constexpr int GA_A = 0, GA_TAB = 65536, GA_B = 98304;
constexpr int G3_ST = 73728, G3_TAB = 90112;
constexpr int G4_B = 65536;

struct KTbl {
  u16 v[2048];
  constexpr KTbl() : v() {
    for (int pos = 0; pos < 2048; pos++) {
      int l = (pos & ~7) + 7 - (pos & 7);
      int sh = l < 256 ? 0 : l < 512 ? 1 : l < 1024 ? 2 : 3;
      int lv = sh ? (((l + (1 << (sh - 1)) - 1 + ((l >> sh) & 1)) >> sh) << sh) : l;
      int s = lv < 256 ? 0 : lv < 512 ? 1 : lv < 1024 ? 2 : 3;
      int rr = 8 * s + ((lv >> 7) << (3 - s)) + ((lv & 7) >> s);
      v[pos] = (u16)((rr * 16 + ((lv >> 3) & 15)) * 16);
    }
  }
};
__device__ __attribute__((aligned(16))) constexpr KTbl KTBL{};

struct cf { float x, y; };
DEV cf cmul(cf a, cf b) { return {a.x * b.x - a.y * b.y, a.x * b.y + a.y * b.x}; }
DEV cf cscale(cf a, float s) { return {a.x * s, a.y * s}; }
DEV cf cexpk(float tl2, float rh, float rl, float s) {
  float mag = __builtin_amdgcn_exp2f(tl2 * s);
  float p = rh * s;
  float f = __builtin_fmaf(rl, s, p - __builtin_rintf(p));
  return {mag * __builtin_amdgcn_cosf(f), mag * __builtin_amdgcn_sinf(f)};
}
DEV void st_x(char* p, cf a) { *(u32*)p = pkh(a.x, a.y); }
DEV void st_y(char* p, cf a) {
  u32 hi = pkh(a.x, a.y);
  f32x2 r = f32x2{a.x, a.y} - hup(hi);
  *(u32*)p = hi;
  *(u32*)(p + 16 * KG_ROW) = pkh(r[0], r[1]);
}
DEV float pow2_scale(float mx, u32 target, float& inv) {
  u32 eb = (__float_as_uint(mx) >> 23) & 0xffu;
  u32 se = (eb == 0 || eb == 255) ? 127u : min(254u + target - eb, 253u);
  inv = __uint_as_float((254u - se) << 23);
  return __uint_as_float(se << 23);
}

DEV void conv_load1(const char* pa, const char* pb, int b0, int i, u32x4 (&A)[8], u32x4 (&B)[8]) {
  u32x2 lo = *(const volatile LDSP u32x2*)(pa + 64 * b0 + 512 * i), hi = *(const volatile LDSP u32x2*)(pa + 64 * b0 + 512 * i + 8);
  A[i] = u32x4{lo[0], lo[1], hi[0], hi[1]};
  B[i] = *(const u32x4*)(pb - 64 * b0 + 512 * i);
}
DEV void conv_step(const char* pa, const char* pb, int nb, const u32x4 (&A)[8], const u32x4 (&B)[8], u32x4 (&An)[8], u32x4 (&Bn)[8], f32x4 (&acc)[8]) {
  u32x2 lo[8], hi[8];
  int c = 0;
#pragma unroll
  for (int i = 0; i < 8; i++) {
#pragma unroll
    for (int t = i; t < 8; t++) {
      if (nb == 1 && i == 0) mmah0(acc[t], A[i], B[t - i]); else mmah(acc[t], A[i], B[t - i]);
      if (nb < 4 && c < 24) {
        const int f = c / 3, k = c % 3;
        if (k == 0) Bn[f] = *(const u32x4*)(pb - 64 * nb + 512 * f);
        else if (k == 1) lo[f] = *(const volatile LDSP u32x2*)(pa + 64 * nb + 512 * f);
        else hi[f] = *(const volatile LDSP u32x2*)(pa + 64 * nb + 512 * f + 8);
      }
      c++;
      SCHED();
    }
  }
  if (nb < 4) {
#pragma unroll
    for (int f = 0; f < 8; f++) An[f] = u32x4{lo[f][0], lo[f][1], hi[f][0], hi[f][1]};
  }
}

DEV u32 kn_voff(int ld, int w, int lane) {
  int kr = lane >> 3, pos = lane & 7;
  int f2 = ((lane >> 4) & 1) | ((w & 1) << 1);
  return (u32)((8 * w + kr) * ld * 2 + ((pos ^ (f2 << 1)) << 4));
}
DEV u32 mk_voff(int ld, int lane) { int r = lane >> 3, pos = lane & 7; return (u32)(r * ld * 2 + ((pos ^ r) << 4)); }
DEV int kn_base(int lane) {
  int g = lane >> 4, q = (lane & 15) >> 2, pp = lane & 3;
  int f2 = ((q >> 1) & 1) | ((g & 1) << 1);
  return (8 * g + q) * 128 + (f2 << 5) + pp * 8;
}
DEV u32x4 kn_frag(const char* p) { u32x2 a = tr_read(p), b = tr_read(p + 512); return u32x4{a[0], a[1], b[0], b[1]}; }
DEV int mk_base(int lane, int ks) { int r16 = lane & 15, g = lane >> 4; return r16 * 128 + (((4 * ks + g) ^ (r16 & 7)) << 4); }
constexpr u32 ONES = 0x3f803f80u;
DEV u32x4 pair_swap(u32 p01, u32 p23, u32 q01, u32 q23) {
  auto r0 = __builtin_amdgcn_permlane16_swap(p01, q01, false, false);
  auto r1 = __builtin_amdgcn_permlane16_swap(p23, q23, false, false);
  return u32x4{r0[0], r1[0], r0[1], r1[1]};
}
template <int LDW> DEV void w64_issue(const u16* wsrc, u32 dst, int w, int lane) {
  const u32 vo = kn_voff(LDW, w, lane);
  const u32 d = dst + w * 1024;
  glds<0 * 8192, 0 * 128 * LDW>(vo, wsrc, d); glds<1 * 8192, 1 * 128 * LDW>(vo, wsrc, d);
  glds<2 * 8192, 2 * 128 * LDW>(vo, wsrc, d); glds<3 * 8192, 3 * 128 * LDW>(vo, wsrc, d);
  glds<4 * 8192, 4 * 128 * LDW>(vo, wsrc, d); glds<5 * 8192, 5 * 128 * LDW>(vo, wsrc, d);
  glds<6 * 8192, 6 * 128 * LDW>(vo, wsrc, d); glds<7 * 8192, 7 * 128 * LDW>(vo, wsrc, d);
}
DEV void w64_regs(const char* img, int wn, int lane, u32x4 (&B)[16][2]) {
  const char* p0 = img + (kn_base(lane) ^ ((2 * wn) << 5));
  const char* p1 = img + (kn_base(lane) ^ ((2 * wn + 1) << 5));
#pragma unroll
  for (int s = 0; s < 16; s++) { B[s][0] = kn_frag(p0 + s * 4096); B[s][1] = kn_frag(p1 + s * 4096); }
}
template <int LDW> DEV void w64_issue4(const u16* wsrc, u32 dst, int w, int lane) {
  w64_issue<LDW>(wsrc, dst, w - 4, lane); w64_issue<LDW>(wsrc, dst, w, lane);
}
template <int H> DEV void w3_issue(const u16* sb, u32 lds0, int w, int lane) {
  constexpr int CB3 = 128 * F_;
  const u32 vb = kn_voff(F_, w, lane), dw = lds0 + GA_B + w * 1024;
  glds<0, (4 * H) * CB3>(vb, sb, dw); glds<8192, (4 * H) * CB3 + 128>(vb, sb, dw);
  glds<16384, (4 * H + 1) * CB3>(vb, sb, dw); glds<24576, (4 * H + 1) * CB3 + 128>(vb, sb, dw);
  glds<32768, (4 * H + 2) * CB3>(vb, sb, dw); glds<40960, (4 * H + 2) * CB3 + 128>(vb, sb, dw);
  glds<49152, (4 * H + 3) * CB3>(vb, sb, dw); glds<57344, (4 * H + 3) * CB3 + 128>(vb, sb, dw);
}

__global__ __launch_bounds__(512, 2) void k_fused(Params p) {
  __shared__ __attribute__((aligned(16))) char smem[LDS_ALL];
  const int bid = blockIdx.x;
  const int tid = threadIdx.x, w = __builtin_amdgcn_readfirstlane(tid >> 6), lane = tid & 63;
  const int r16 = lane & 15, g = lane >> 4;
  const u32 lds0 = lds_addr(smem);
  const rsrc_t wsr = __builtin_amdgcn_make_buffer_rsrc(p.ws, 0, 0x4000000, 0x00020000);
  STAMP(0);
  const int cp = (bid & 7) * 32 + (bid >> 3);
  const int pm = (bid & 7) * 4 + (bid >> 6), pn = (bid >> 3) & 7;
  const int row0 = pm * 64, col0 = pn * 64;
  u32x2 tag;
  u32 TKW = 0;
  {
    u32x2 ep = ld8_dev(wsr, FL_EPOCH);
    u32x4 xv = *(const u32x4*)(p.x + (bid * 8 + w) * D_ + lane * 8);
    const int chl = w >> 2, role = (w >> 1) & 1, n = tid & 127;
    const int ch = cp * 2 + chl;
    const int pi = ch * NS_ + n;
    u32 r_are = p.are[pi], r_aim = p.aim[pi], r_bre = p.bre[pi], r_bim = p.bim[pi], r_cre = p.cre[pi], r_cim = p.cim[pi];
    u32 r_ldt = p.logdt[ch];
    u32x4 kidx = *(const u32x4*)(KTBL.v + (tid & 255) * 8);
    {
      u64 e = ((u64)ep[1] << 32 | ep[0]) + 1;
      tag = u32x2{(u32)__builtin_amdgcn_readfirstlane((u32)e), (u32)__builtin_amdgcn_readfirstlane((u32)(e >> 32))};
    }
    char* su = smem + P0_LN;
    float* ms = (float*)(smem + P0_MS);
    {
      f32x2 v[4];
#pragma unroll
      for (int i = 0; i < 4; i++) v[i] = bfup(xv[i]);
      f32x2 s2 = (v[0] + v[1]) + (v[2] + v[3]);
      float mean = wave_sum(s2[0] + s2[1]) * (1.0f / D_);
      f32x2 q2 = {0, 0};
#pragma unroll
      for (int i = 0; i < 4; i++) { f32x2 d = v[i] - mean; q2 += d * d; }
      float var = wave_sum(q2[0] + q2[1]) * (1.0f / D_);
      float m = rbf(mean);
      float inv = __builtin_amdgcn_rcpf(rbf(__builtin_amdgcn_sqrtf(rbf(rbf(var) + 1e-5f))));
      u32x4 uo;
#pragma unroll
      for (int i = 0; i < 4; i++) {
        f32x2 u = bfup(pkbf2(bfup(pkbf2(v[i] - m)) * inv));
        uo[i] = pkh(u[0], u[1]);
      }
      *(u32x4*)(su + w * 1088 + lane * 16) = uo;
      *(u32x4*)(su + 8 * 1088 + w * 1088 + lane * 16) = xv;
    }
    STAMP(1);
    float dt = rbf(expf(bf2f(r_ldt)));
    float ar = dt * bf2f(r_are), ai = dt * bf2f(r_aim);
    cf CB = cmul(cf{bf2f(r_cre), bf2f(r_cim)}, cf{dt * bf2f(r_bre), dt * bf2f(r_bim)});
    float tl2 = ar * 1.4426950408889634f;
    const float I2PH = 0.15915494309189535f;
    const float I2PL = (float)(0.15915494309189533576888 - (double)0.15915494309189535f);
    float rh = ai * I2PH;
    float rl = __builtin_fmaf(ai, I2PH, -rh) + ai * I2PL;
    {
      float ax = fabsf(CB.x), ay = fabsf(CB.y);
      float mx = wave_max(fmaxf(ax, ay)), bs = wave_sum(ax + ay), isx;
      float sx = pow2_scale(mx, 13, isx);
      CB = cscale(CB, sx);
      if (role == 0 && lane == 0) { ms[chl * 2 + (w & 1)] = bs; ms[4 + chl * 2 + (w & 1)] = isx; }
    }
    lds_bar();
    {
      const int q4 = (lane & 15) >> 2, pp = lane & 3;
      const char* a = su + q4 * 1088 + (64 * w + 16 * g + 4 * pp) * 2;
      u32x2 u0 = tr_read(a), u1 = tr_read(a + 4 * 1088), x0 = tr_read(a + 8 * 1088), x1 = tr_read(a + 12 * 1088);
      const u32 dst = WS_U8 + bid * 16384 + tid * 32;
      st16_dev(wsr, dst, u32x4{u0[0], u0[1], u1[0], u1[1]});
      st16_dev(wsr, dst + 16, u32x4{x0[0], x0[1], x1[0], x1[1]});
    }
    {
      u64 pc; u32 len;
      asm volatile("s_getpc_b64 %0\n.Lk_here:\n\ts_mov_b32 %1, .Lk_end-.Lk_here" : "=s"(pc), "=s"(len));
      const void* wb = w == 0 ? (const void*)p.w3 : w == 1 ? (const void*)p.w4 : w == 2 ? (const void*)p.w1 : w == 3 ? (const void*)p.w2 : (const void*)pc;
      const u32 oc = min((u32)((bid >> 3) * 24 + (w & 3) * 6 + min(lane, 5)) * 64, len);
      touch(TKW, wb, w < 2 ? (u32)(bid * 56 + min(lane, 55)) * 128 : w < 4 ? (u32)(bid * 16 + (lane & 15)) * 128 : oc);
    }
    char* kt = smem + P0_KT + chl * 12288;
    float kinv;
    const float kscale = pow2_scale(ms[chl * 2] + ms[chl * 2 + 1], 14, kinv);
    float fsc = ms[4 + chl * 2 + ((w & 3) >> 1)] * (kscale * (1.0f / 16384.f));
    asm volatile("" : "+v"(fsc), "+v"(kinv));
    STAMP(2);
    char* img = smem + chl * KG_CH;
    {
      char* wp = img + n * 4;
      if (role == 0) {
        cf e3 = cexpk(tl2, -rh, -rl, 8.f), e4 = cexpk(tl2, -rh, -rl, 16.f), e5 = cexpk(tl2, -rh, -rl, 32.f), e6 = cexpk(tl2, -rh, -rl, 64.f);
        cf y[8];
        y[0] = {16384.f, 0.f}; y[1] = cscale(e3, 16384.f); y[2] = cscale(e4, 16384.f); y[3] = cmul(y[2], e3);
        y[4] = cscale(e5, 16384.f); y[5] = cmul(y[4], e3); y[6] = cmul(y[4], e4); y[7] = cmul(y[6], e3);
#pragma unroll
        for (int j = 0; j < 8; j++) st_y(wp + (41 + j) * KG_ROW, y[j]);
#pragma unroll
        for (int j = 0; j < 8; j++) st_y(wp + (49 + j) * KG_ROW, j ? cmul(y[j], e6) : cscale(e6, 16384.f));
        SCHED(); wait_vm<1>(); bar();
        if (tid < 16) st8_dev(wsr, FL_1 + tid * 2048 + bid * 8, tag);
        cf e7 = cexpk(tl2, rh, rl, 128.f), e8 = cexpk(tl2, rh, rl, 256.f), e9 = cexpk(tl2, rh, rl, 512.f);
        cf e10 = cexpk(tl2, rh, rl, 1024.f), e11 = cexpk(tl2, rh, rl, 2048.f);
        cf g8 = cmul(CB, e10), g12 = cmul(g8, e9), g16 = cmul(CB, e11);
        cf g10 = cmul(g8, e8), g14 = cmul(g12, e8);
        st_x(wp + 32 * KG_ROW, g8); st_x(wp + 33 * KG_ROW, cmul(g8, e7));
        st_x(wp + 34 * KG_ROW, g10); st_x(wp + 35 * KG_ROW, cmul(g10, e7));
        st_x(wp + 36 * KG_ROW, g12); st_x(wp + 37 * KG_ROW, cmul(g12, e7));
        st_x(wp + 38 * KG_ROW, g14); st_x(wp + 39 * KG_ROW, cmul(g14, e7));
        st_x(wp + 40 * KG_ROW, g16);
      } else {
        cf e0 = cexpk(tl2, rh, rl, 1.f), e1 = cexpk(tl2, rh, rl, 2.f), e2 = cexpk(tl2, rh, rl, 4.f);
        cf e7 = cexpk(tl2, rh, rl, 128.f), e8 = cexpk(tl2, rh, rl, 256.f), e9 = cexpk(tl2, rh, rl, 512.f);
        cf eb[8];
        eb[1] = e0; eb[2] = e1; eb[3] = cmul(e1, e0); eb[4] = e2; eb[5] = cmul(e2, e0); eb[6] = cmul(e2, e1); eb[7] = cmul(eb[6], e0);
        cf gq[8];
        gq[0] = CB; gq[1] = cmul(CB, e7); gq[2] = cmul(CB, e8); gq[3] = cmul(gq[2], e7);
        gq[4] = cmul(CB, e9); gq[5] = cmul(gq[4], e7); gq[6] = cmul(gq[4], e8); gq[7] = cmul(gq[6], e7);
#pragma unroll
        for (int q = 0; q < 2; q++)
#pragma unroll
          for (int b = 0; b < 8; b++) st_x(wp + (q * 8 + b) * KG_ROW, b ? cmul(gq[q], eb[b]) : gq[q]);
        SCHED(); wait_vm<1>(); bar();
#pragma unroll
        for (int q = 2; q < 4; q++)
#pragma unroll
          for (int b = 0; b < 4; b++) st_x(wp + (16 + (q - 2) * 4 + b) * KG_ROW, b ? cmul(gq[q], eb[2 * b]) : gq[q]);
#pragma unroll
        for (int q = 4; q < 8; q++)
#pragma unroll
          for (int b = 0; b < 2; b++) st_x(wp + (24 + (q - 4) * 2 + b) * KG_ROW, b ? cmul(gq[q], e2) : gq[q]);
      }
    }
    STAMP(3);
    lds_bar();
    {
      const int wv = w & 3;
      const char* pa = img + r16 * KG_ROW + wv * 128 + g * 16;
      u32x4 a[3][2], yh[2], yl[2];
#pragma unroll
      for (int s = 0; s < 2; s++) {
#pragma unroll
        for (int t = 0; t < 3; t++) a[t][s] = *(const u32x4*)(pa + t * 16 * KG_ROW + s * 64);
        yh[s] = *(const u32x4*)(pa + 41 * KG_ROW + s * 64);
        yl[s] = *(const u32x4*)(pa + 57 * KG_ROW + s * 64);
      }
      const float f = fsc;
      char* kw = kt + (4 * g * 16 + r16) * 16 + ((wv + (r16 >> 3) + 2 * (g & 1)) & 3) * 4;
      f32x4 c[3];
#pragma unroll
      for (int t = 0; t < 3; t++) { mmah0(c[t], a[t][0], yl[0]); mmah(c[t], a[t][1], yl[1]); }
#pragma unroll
      for (int t = 0; t < 3; t++) { mmah(c[t], a[t][0], yh[0]); mmah(c[t], a[t][1], yh[1]); }
      mfence(); pin(c[0]); pin(c[1]); pin(c[2]);
#pragma unroll
      for (int t = 0; t < 3; t++)
#pragma unroll
        for (int r = 0; r < 4; r++) *(float*)(kw + (t * 16 + r) * 256) = c[t][r] * f;
    }
    if (w == 0) {
      poll256(wsr, FL_1 + (bid & 15) * 2048, tag, lane);
      if (bid == 0 && lane == 0) st8_dev(wsr, FL_EPOCH, tag);
    }
    lds_bar();
    STAMP(4);
    {
      const char* sa = p.ws + WS_U8 + (size_t)w * 16 * 16384 + cp * 64;
      const u32 vo = (u32)((lane >> 2) * 16384 + (lane & 3) * 16);
      glds<CV_UI, 0>(vo, sa, lds0 + w * 1024); glds<CV_UI + 8192, 128 * 16384>(vo, sa, lds0 + w * 1024);
    }
    {
      u32x4 o;
#pragma unroll
      for (int i = 0; i < 4; i++) {
        f32x4 a = *(const f32x4*)(kt + (kidx[i] & 0xffffu)), b = *(const f32x4*)(kt + (kidx[i] >> 16));
        o[i] = pkh((a[0] + a[1]) + (a[2] + a[3]), (b[0] + b[1]) + (b[2] + b[3]));
      }
      const int e = tid & 255;
      char* base = smem + chl * CV_CH;
      *(u32x4*)(base + CV_KP + (30 + e) * 16) = o;
      if (e < 30) *(u32x4*)(base + CV_KP + e * 16) = u32x4{0, 0, 0, 0};
      else if (e < 34) *(u32x4*)(base + (e - 30) * CV_COPY) = u32x4{0, 0, 0, 0};
      else if (e == 34) *(float*)(base + CV_KP + 4576) = kinv;
    }
  }
  STAMP(5);
  STAMP(6);
  {
    {
      const int chl = w >> 2, q = tid & 255;
      char* base = smem + chl * CV_CH;
      wait_vm<0>(); keep(TKW); bar();
      const char* ui = smem + CV_UI + q * 64 + chl * 32;
      u32x4 cur = *(const u32x4*)ui, prv = *(const u32x4*)(ui - 64);
      if (q == 0) prv = u32x4{0, 0, 0, 0};
      u32 wd[8] = {prv[0], prv[1], prv[2], prv[3], cur[0], cur[1], cur[2], cur[3]};
      u32 o[5];
#pragma unroll
      for (int k = 0; k < 5; k++) o[k] = __builtin_amdgcn_alignbit(wd[k + 1], wd[k], 16);
      char* d = base + (q + 1) * 16;
      *(u32x4*)(d) = u32x4{o[0], o[1], o[2], o[3]};
      *(u32x4*)(d + CV_COPY) = u32x4{wd[1], wd[2], wd[3], wd[4]};
      *(u32x4*)(d + 2 * CV_COPY) = u32x4{o[1], o[2], o[3], o[4]};
      *(u32x4*)(d + 3 * CV_COPY) = u32x4{wd[2], wd[3], wd[4], wd[5]};
      if (q == 255) {
        u32 o4 = __builtin_amdgcn_alignbit(cur[1], cur[0], 16), o5 = __builtin_amdgcn_alignbit(cur[2], cur[1], 16);
        u32 o6 = __builtin_amdgcn_alignbit(cur[3], cur[2], 16), o7 = cur[3] >> 16;
        char* d2 = base + 257 * 16;
        *(u32x4*)(d2) = u32x4{o4, o5, o6, o7};
        *(u32x4*)(d2 + CV_COPY) = u32x4{cur[1], cur[2], cur[3], 0};
        *(u32x4*)(d2 + 2 * CV_COPY) = u32x4{o5, o6, o7, 0};
        *(u32x4*)(d2 + 3 * CV_COPY) = u32x4{cur[2], cur[3], 0, 0};
      }
    }
    const int chl = (w >> 1) & 1, hh = w & 1;
    const float dsk = bf2f(p.dskip[cp * 2 + chl]);
    const char* base = smem + chl * CV_CH;
    const int j2 = 2 * (hh + 2 * (w >> 2));
    char* red = smem + CV_RED + chl * 16384 + lane * 16;
    lds_bar();
    STAMP(7);
    f32x4 f0 = {0, 0, 0, 0}, f1 = {0, 0, 0, 0};
    if (w < 4) {
      f32x4 acc[8];
      const char* pa = base + (r16 & 3) * CV_COPY + 48 - 16 * g + 2 * (r16 & 12) + 256 * hh;
      const char* pb = base + CV_KP + 448 + 32 * r16 + 16 * g - 256 * hh;
      u32x4 A0[8], B0[8], A1[8], B1[8];
#pragma unroll
      for (int i = 0; i < 8; i++) conv_load1(pa, pb, 0, i, A0, B0);
      conv_step(pa, pb, 1, A0, B0, A1, B1, acc);
      conv_step(pa, pb, 2, A1, B1, A0, B0, acc);
      conv_step(pa, pb, 3, A0, B0, A1, B1, acc);
      conv_step(pa, pb, 4, A1, B1, A0, B0, acc);
      mfence();
#pragma unroll
      for (int i = 0; i < 8; i++) pin(acc[i]);
      STAMP(27);
      char* d = red + hh * 8192;
      if (hh) { *(f32x4*)(d) = acc[0]; *(f32x4*)(d + 1024) = acc[1]; f0 = acc[2]; f1 = acc[3]; }
      else { *(f32x4*)(d + 2048) = acc[2]; *(f32x4*)(d + 3072) = acc[3]; f0 = acc[0]; f1 = acc[1]; }
#pragma unroll
      for (int t = 4; t < 8; t++) *(f32x4*)(d + t * 1024) = acc[t];
    } else {
      w64_issue<D_>(p.w1 + col0, lds0 + GA_B, w - 4, lane);
      w64_issue<D_>(p.w1 + col0, lds0 + GA_B, w, lane);
      wait_vm<0>();
    }
    const float kinv = *(const float*)(base + CV_KP + 4576);
    const int l0 = 256 * j2 + 16 * r16 + 4 * g;
    u32x2 xx[2];
    u32 dd[2][2];
#pragma unroll
    for (int k = 0; k < 2; k++) {
      const char* ui = smem + CV_UI + ((l0 + 256 * k) >> 3) * 64 + chl * 32 + (l0 & 4) * 2;
      u32x2 uu = *(const u32x2*)ui;
      xx[k] = *(const u32x2*)(ui + 16);
      dd[k][0] = pkbf2(hup(uu[0]) * dsk); dd[k][1] = pkbf2(hup(uu[1]) * dsk);
    }
    lds_bar();
    STAMP(8);
    if (w < 4) {
      f0 += *(const f32x4*)(red + (hh ^ 1) * 8192 + j2 * 1024);
      f1 += *(const f32x4*)(red + (hh ^ 1) * 8192 + j2 * 1024 + 1024);
    } else {
      f0 = *(const f32x4*)(red + j2 * 1024) + *(const f32x4*)(red + 8192 + j2 * 1024);
      f1 = *(const f32x4*)(red + j2 * 1024 + 1024) + *(const f32x4*)(red + 8192 + j2 * 1024 + 1024);
    }
    char* stg = smem + CV_STG;
#pragma unroll
    for (int k = 0; k < 2; k++) {
      f32x4 f = k ? f1 : f0;
      u32 y01 = pkbf2(f32x2{f[0], f[1]} * kinv), y23 = pkbf2(f32x2{f[2], f[3]} * kinv);
      u32 s01 = pkbf2(bfup(y01) + bfup(dd[k][0])), s23 = pkbf2(bfup(y23) + bfup(dd[k][1]));
      u32 h01 = pkbf2(bfup(xx[k][0]) + bfup(s01)), h23 = pkbf2(bfup(xx[k][1]) + bfup(s23));
      *(u32x2*)(stg + (16 * (j2 + k) + r16) * 80 + chl * 32 + g * 8) = u32x2{h01, h23};
    }
    lds_bar();
    {
      const int lb = tid >> 2, pc = tid & 3;
      st16_dev(wsr, WS_HT + cp * 8192 + tid * 16, *(const u32x4*)(stg + lb * 80 + pc * 16));
    }
  }
  const int mt = w & 3, wn = w >> 2;
  const int rt = (pm >> 1) * 8 + (pm & 1) * 2 + (mt >> 1) * 4 + (mt & 1);
  const int pc4 = (g & 1) * 2 + (g >> 1);
  const u32 boff = (u32)((rt * 16 + pn * 2 + wn) * 1024 + r16 * 64 + pc4 * 16);
  u32x4 ONES4 = {ONES, ONES, ONES, ONES};
  asm volatile("s_nop 1" : "+v"(ONES4));
  const u32 dw = lds0 + w * 1024;
  const u32 vbD = kn_voff(D_, w, lane);
  u32x4 hres, xres;
  u32x4 B[16][2];
  constexpr int CBD = 128 * D_, CB3 = 128 * F_;
  const int idx3 = bid >> 3;
  const bool has3 = idx3 < 28;
  const int pm3 = (bid & 7) * 2 + (idx3 >= 14 ? 1 : 0), pn3 = idx3 >= 14 ? idx3 - 14 : idx3;
  const u16* sb3 = p.w3 + (has3 ? pn3 * 128 : 0);
  const u32 vbF = kn_voff(F_, w, lane);
  const u32 vbF4 = kn_voff(F_, w - 4, lane);
  const int wm3 = w & 1, wn3 = w >> 1;
  const int ob0 = (kn_base(lane) + (wn3 >> 1) * 8192) ^ ((2 * (wn3 & 1)) << 5), ob1 = ob0 ^ 32;
  f32x4 cw0, cw1;
  u32x4 Bt[2][8];
  const char* pb0 = smem + GA_B + (kn_base(lane) ^ ((2 * wn) << 5));
  const char* pb1 = smem + GA_B + (kn_base(lane) ^ ((2 * wn + 1) << 5));
  auto ldB = [&](int j) {
#pragma unroll
    for (int i = 0; i < 4; i++) { Bt[j & 1][2 * i] = kn_frag(pb0 + (4 * j + i) * 4096); Bt[j & 1][2 * i + 1] = kn_frag(pb1 + (4 * j + i) * 4096); }
    SCHED();
  };
  auto ldB3 = [&](int s0, int n, int o) {
#pragma unroll
    for (int s = 0; s < 8; s++) if (s < n) {
      B[s0 + s][0] = kn_frag(smem + o + (s >> 1) * 16384 + (s & 1) * 4096 + ob0);
      B[s0 + s][1] = kn_frag(smem + o + (s >> 1) * 16384 + (s & 1) * 4096 + ob1);
    }
#pragma unroll
    for (int s = 0; s < 8; s++) if (s < n) { mmaz(s0 + s == 0, cw0, B[s0 + s][0], ONES4); mmaz(s0 + s == 0, cw1, B[s0 + s][1], ONES4); }
  };
#define W2C(c) glds<GA_B + (c) * 8192, (c) * CBD>(vbD, p.w2 + col0, dw);
#define W3C(c, dst) glds<(dst), (c) * CB3>(vbF, sb3, dw); glds<(dst) + 8192, (c) * CB3 + 128>(vbF, sb3, dw);
#define W3C4(c, dst) W3C(c, dst) glds<(dst), (c) * CB3>(vbF4, sb3, dw - 4096); glds<(dst) + 8192, (c) * CB3 + 128>(vbF4, sb3, dw - 4096);
  {
    f32x4 cv0, cv1;
    {
#pragma unroll
      for (int i = 0; i < 4; i++) { Bt[0][2 * i] = kn_frag(pb0 + (4 * mt + i) * 4096); Bt[0][2 * i + 1] = kn_frag(pb1 + (4 * mt + i) * 4096); }
#pragma unroll
      for (int i = 0; i < 4; i++) { mmaz(i == 0, cv0, Bt[0][2 * i], ONES4); mmaz(i == 0, cv1, Bt[0][2 * i + 1], ONES4); }
      mfence(); pin(cv0); pin(cv1);
      char* ct = smem + 65536 + wn * 128 + g * 16;
      if (r16 == 0) { *(f32x4*)(ct + mt * 256) = cv0; *(f32x4*)(ct + mt * 256 + 64) = cv1; }
      lds_bar();
      cv0 = (*(const f32x4*)(ct) + *(const f32x4*)(ct + 256)) + (*(const f32x4*)(ct + 512) + *(const f32x4*)(ct + 768));
      cv1 = (*(const f32x4*)(ct + 64) + *(const f32x4*)(ct + 320)) + (*(const f32x4*)(ct + 576) + *(const f32x4*)(ct + 832));
    }
    wait_vm_lgkm<0>(); bar();
    if (tid < 16) st8_dev(wsr, FL_2 + tid * 2048 + bid * 8, tag);
    STAMP(9);
    if (w >= 4) { W3C4(0, 65536) W3C4(1, 81920) }
    ldB(0); ldB(1);
    if (w == 0) poll256(wsr, FL_2 + (bid & 15) * 2048, tag, lane);
    bar();
    STAMP(10);
    {
      const char* sa = p.ws + WS_HT + rt * 64 + (size_t)wn * 262144;
      const int j2 = lane ^ (((lane >> 4) & 1) << 3);
      const u32 vo = (u32)((j2 >> 2) * 8192 + (j2 & 3) * 16);
      const u32 d = lds0 + GA_A + mt * 16384 + wn * 2048;
      glds<0, 0>(vo, sa, d); glds<1024, 131072>(vo, sa, d); glds<4096, 4 * 131072>(vo, sa, d); glds<5120, 5 * 131072>(vo, sa, d);
      glds<8192, 8 * 131072>(vo, sa, d); glds<9216, 9 * 131072>(vo, sa, d); glds<12288, 12 * 131072>(vo, sa, d); glds<13312, 13 * 131072>(vo, sa, d);
    }
    const char* aw = smem + GA_A + mt * 16384;
    const int ab = g * 256 + ((g & 1) << 7) + ((lane & 15) >> 2) * 32 + (lane & 3) * 8;
    const char* pa0 = aw + ab;
    const char* pa1 = aw + (ab ^ 128);
    f32x4 acc0, acc1, sacc, gacc;
    u32x4 a[2][4];
    auto ld = [&](int j) {
#pragma unroll
      for (int i = 0; i < 4; i++) {
        u32x2 x0 = tr_read(pa0 + (4 * j + i) * 1024), x1 = tr_read(pa1 + (4 * j + i) * 1024);
        a[j & 1][i] = u32x4{x0[0], x0[1], x1[0], x1[1]};
      }
      SCHED();
    };
    auto mm = [&](int j) {
#pragma unroll
      for (int i = 0; i < 4; i++) {
        mmaz(j + i == 0, sacc, ONES4, a[j & 1][i]);
        mmaz(j + i == 0, gacc, a[j & 1][i], a[j & 1][i]);
        mmaz(j + i == 0, acc0, Bt[j & 1][2 * i], a[j & 1][i]);
        mmaz(j + i == 0, acc1, Bt[j & 1][2 * i + 1], a[j & 1][i]);
      }
      SCHED();
    };
    SCHED();
    wait_vm<6>(); bar(); ld(0);
    STAMP(23);
    wait_vm<4>(); bar(); ld(1); mm(0); ldB(2);
    wait_vm<2>(); bar(); ld(2); mm(1); ldB(3);
    wait_vm_lgkm<0>(); bar(); ld(3);
    W2C(0) W2C(1)
    STAMP(24);
    const int ab2 = pc4 * 256 + ((pc4 & 1) << 7) + ((lane & 15) >> 2) * 32 + (lane & 3) * 8;
    const char* hs = aw + (2 * pn + wn) * 1024;
    u32x2 hx0 = tr_read(hs + ab2), hx1 = tr_read(hs + (ab2 ^ 128));
    SCHED(); mm(2); W2C(2) W2C(3) mm(3);
    W2C(4) W2C(5)
    mfence(); pin(acc0); pin(acc1); pin(sacc); pin(gacc);
    STAMP(11);
    float dg = (r16 & 2) ? ((r16 & 1) ? gacc[3] : gacc[2]) : ((r16 & 1) ? gacc[1] : gacc[0]);
    f32x2 sa2 = {sacc[0], gsum(g == (r16 >> 2) ? dg : 0.f)};
    float mean1 = sa2[0] * (1.0f / D_);
    float var1 = fmaxf(sa2[1] * (1.0f / D_) - mean1 * mean1, 0.f);
    float m1 = rbf(mean1);
    float sd1 = rbf(__builtin_amdgcn_sqrtf(rbf(rbf(var1) + 1e-5f)));
    float i1 = __builtin_amdgcn_rcpf(sd1);
    float m2 = rbf((mean1 - m1) * i1);
    float sd2 = rbf(__builtin_amdgcn_sqrtf(rbf(rbf(var1 * i1 * i1) + 1e-5f)));
    float rs = i1 * __builtin_amdgcn_rcpf(sd2), tt = -(m1 + m2 * sd1) * rs;
    {
      f32x4 rt4 = {rs, rs, tt, tt};
      pin(rt4);
      W2C(6) W2C(7)
      u32x2 o0 = act4(acc0, cv0, rt4), o1 = act4(acc1, cv1, rt4);
      st16_dev(wsr, WS_H1 + 16384 + boff, pair_swap(o0[0], o0[1], o1[0], o1[1]));
    }
    if (tid < 16) {
      const u32 o = (u32)((pn * 2 + (tid >> 3)) * 1024 + (tid & 7) * 16);
      if (pm == 0) st16_dev(wsr, WS_H1 + 14 * 64 + o, u32x4{0, 0, 0, 0});
      if (pm == 31) st16_dev(wsr, WS_H1 + 129 * 16384 + o, u32x4{0, 0, 0, 0});
    }
    hres = u32x4{hx0[0], hx0[1], hx1[0], hx1[1]};
#pragma unroll
    for (int i = 0; i < 4; i++) xres[i] = pkbf2(bfup(pkbf2(bfup(hres[i]) - m1)) * i1);
    wait_vm_lgkm<0>(); bar();
    if (tid < 8) st8_dev(wsr, FL_3 + tid * 2048 + (pm * 8 + pn) * 8, tag);
    ldB3(0, 4, 65536);
    ldB(0); ldB(1);
  }
  STAMP(12);
  const int ar = lane >> 3, asp = (lane & 7) ^ ar;
  const u32 apo = (u32)((asp >> 2) * 1024 + (asp & 3) * 16);
  {
    u32 vo[8];
#pragma unroll
    for (int k = 0; k < 8; k++) {
      const int sh = (k >> 1) == 0 ? -1 : (k >> 1) == 1 ? 1 : (k >> 1) == 2 ? -2 : 2;
      const int row = 16 + ar + (k & 1) * 8 + sh;
      vo[k] = (u32)((row >> 4) * 16384 + (row & 15) * 64) + apo;
    }
    if (w == 0) polln(wsr, FL_3 + pn * 2048, max((pm & ~1) - 1, 0) * 8, min((pm & ~1) + 3, 32) * 8, tag, lane);
    lds_bar();
    STAMP(13);
    {
      const char* sa = p.ws + WS_H1 + (size_t)rt * 16384 + wn * 2048;
      const u32 d = lds0 + GA_A + mt * 16384 + wn * 2048;
      glds<0, 0>(vo[0], sa, d); glds<1024, 0>(vo[1], sa, d); glds<4096, 4096>(vo[2], sa, d); glds<5120, 4096>(vo[3], sa, d);
      glds<8192, 8192>(vo[4], sa, d); glds<9216, 8192>(vo[5], sa, d); glds<12288, 12288>(vo[6], sa, d); glds<13312, 12288>(vo[7], sa, d);
    }

    const char* pa0 = smem + GA_A + mt * 16384 + mk_base(lane, 0);
    const char* pa1 = smem + GA_A + mt * 16384 + mk_base(lane, 1);
    f32x4 acc0, acc1;
    u32x4 a[2][4];
    auto ld = [&](int j) {
      a[j & 1][0] = *(const u32x4*)(pa0 + j * 4096); a[j & 1][1] = *(const u32x4*)(pa1 + j * 4096);
      a[j & 1][2] = *(const u32x4*)(pa0 + j * 4096 + 2048); a[j & 1][3] = *(const u32x4*)(pa1 + j * 4096 + 2048);
      SCHED();
    };
    auto mm = [&](int j) {
#pragma unroll
      for (int i = 0; i < 4; i++) {
        mmaz(j + i == 0, acc0, Bt[j & 1][2 * i], a[j & 1][i]);
        mmaz(j + i == 0, acc1, Bt[j & 1][2 * i + 1], a[j & 1][i]);
      }
      SCHED();
    };
    SCHED();
    wait_vm<6>(); bar(); ld(0);
    STAMP(25);
    wait_vm<4>(); bar(); ld(1); mm(0); ldB(2);
    wait_vm<2>(); bar(); ld(2); mm(1); ldB(3);
    wait_vm_lgkm<0>(); bar(); ld(3);
    W3C(2, 65536)
    STAMP(26);
    mm(2); W3C(3, 81920) mm(3);
    W3C(4, GA_B)
    mfence(); pin(acc0); pin(acc1);
    STAMP(14);
    u32x4 v = pair_swap(pkbf(acc0[0], acc0[1]), pkbf(acc0[2], acc0[3]), pkbf(acc1[0], acc1[1]), pkbf(acc1[2], acc1[3]));
#pragma unroll
    for (int i = 0; i < 4; i++) xres[i] = pkbf2(bfup(xres[i]) + bfup(v[i]));
    st16_dev(wsr, WS_X1 + boff, xres);
    float s1 = 0, s2 = 0;
#pragma unroll
    for (int i = 0; i < 4; i++) { s1 = dot2(xres[i], ONES, s1); s2 = dot2(xres[i], xres[i], s2); }
    s1 = gsum(s1); s2 = gsum(s2);
    if (g == 0) st8_dev(wsr, WS_ST + (rt * 16 + pn * 2 + wn) * 128 + r16 * 8, u32x2{__float_as_uint(s1), __float_as_uint(s2)});
    W3C(5, GA_B + 16384) W3C(6, GA_B + 32768)
    wait_vm_lgkm<4>(); bar();
    if (tid < 8) st8_dev(wsr, FL_4 + tid * 2048 + (pm * 8 + pn) * 8, tag);
    W3C(7, GA_B + 49152)
  }
  STAMP(15);
  const u16* sb4 = p.w4 + col0;
#define G4_R1A(i) glds<GA_B + (i) * 8192, (((i) >> 1) * 7 + ((i) & 1)) * CBD>(vbD, sb4, dw);
#define G4_R1A4(i) G4_R1A(i) glds<GA_B + (i) * 8192, (((i) >> 1) * 7 + ((i) & 1)) * CBD>(vbD4, sb4, dw - 4096);
#define G4_R1A_ALL4 G4_R1A4(0) G4_R1A4(1) G4_R1A4(2) G4_R1A4(3) G4_R1A4(4) G4_R1A4(5) G4_R1A4(6) G4_R1A4(7)
  const u32 vbD4 = kn_voff(D_, w - 4, lane);
#define G4_R1B(i) glds<(i) * 8192, (((i) >> 1) * 7 + 2 + ((i) & 1)) * CBD>(vbD, sb4, dw);
#define G4_R2POS(i) ((i) < 4 ? 32768 + (i) * 8192 : GA_B + ((i) - 4) * 8192)
#define G4_R2(i) glds<G4_R2POS(i), (((i) / 3) * 7 + 4 + ((i) % 3)) * CBD>(vbD, sb4, dw);
  if (has3) {
    const int wm = wm3;
    ldB3(4, 4, 65536);
    ldB3(8, 2, GA_B);
    wait_vm<0>(); bar();
    ldB3(10, 6, GA_B + 16384);
    if (w == 0) polln(wsr, FL_4 + pn * 2048, pm3 * 16, pm3 * 16 + 16, tag, lane);
    lds_bar();
    STAMP(16);
    const char* sa = p.ws + WS_X1 + (size_t)(pm3 * 8 + (w >> 1)) * 16384 + (w & 1) * 512;
    const u32 va = (u32)(ar * 64) + apo;
#define G3_A(i) glds<(((i) + 4) % 9) * 8192, ((i) >> 3) * 65536 + ((i) & 7) * 2048>(va, sa, dw);
    {
      const char* ss = p.ws + WS_ST + (size_t)pm3 * 16384 + w * 1024;
      glds<G3_ST, 0>(lane * 16, ss, dw); glds<G3_ST + 8192, 8192>(lane * 16, ss, dw);
    }
    G3_A(0) G3_A(1) G3_A(2) G3_A(3) G3_A(4) G3_A(5) G3_A(6) G3_A(7) G3_A(8)
    wait_vm<9>(); bar();
    {
      const char* sst = smem + G3_ST + (tid >> 6) * 2048 + (tid & 3) * 512 + ((tid >> 2) & 15) * 8;
      f32x2 st = (*(const f32x2*)(sst) + *(const f32x2*)(sst + 128)) + (*(const f32x2*)(sst + 256) + *(const f32x2*)(sst + 384));
      float t1 = st[0] + dppf<0xB1>(st[0]), t2 = st[1] + dppf<0xB1>(st[1]);
      t1 += dppf<0x4E>(t1); t2 += dppf<0x4E>(t2);
      float mean = t1 * (1.0f / D_);
      float var = fmaxf(t2 * (1.0f / D_) - mean * mean, 0.f);
      float rs = __builtin_amdgcn_rcpf(rbf(__builtin_amdgcn_sqrtf(rbf(rbf(var) + 1e-5f))));
      if ((tid & 3) == 0) { float tt = -rbf(mean) * rs; *(f32x4*)(smem + G3_TAB + (tid >> 2) * 16) = f32x4{rs, rs, tt, tt}; }
    }
    const f32x4& cv0 = cw0;
    const f32x4& cv1 = cw1;
    float* rowt = (float*)(smem + G3_TAB);
    const int oa0 = mk_base(lane, 0) + wm * 4096, oa1 = mk_base(lane, 1) + wm * 4096;
    f32x4 acc[2][2][2];
    u32x4 a[2][4];
    auto ld = [&](int slot, u32x4 (&d)[4]) {
      const char* s = smem + slot * 8192;
      d[0] = *(const u32x4*)(s + oa0); d[1] = *(const u32x4*)(s + oa0 + 2048);
      d[2] = *(const u32x4*)(s + oa1); d[3] = *(const u32x4*)(s + oa1 + 2048);
      SCHED();
    };
    auto m1 = [&](int ps, int b, int i, const u32x4 (&d)[4]) {
      mmaz(b == 0 && i < 4, acc[ps][(i >> 1) & 1][i & 1], B[2 * b + (i >> 2)][(i >> 1) & 1], d[2 * (i >> 2) + (i & 1)]);
      SCHED();
    };
    auto mm = [&](int ps, int b, const u32x4 (&d)[4]) {
#pragma unroll
      for (int i = 0; i < 8; i++) m1(ps, b, i, d);
    };
    const u32 hoff = WS_HID + (u32)(((pm3 * 8 + 2 * wm) * 56 + pn3 * 4 + wn3) * 1024 + r16 * 64 + pc4 * 16);
    u32x2 o0;
    f32x4 rtv[2];
    auto ldrt = [&](int ps) {
      rtv[0] = *(const f32x4*)(rowt + (64 * ps + 32 * wm + r16) * 4);
      rtv[1] = *(const f32x4*)(rowt + (64 * ps + 32 * wm + 16 + r16) * 4);
    };
    auto ep = [&](int ps, int i) {
      const int mi = i >> 1;
      if ((i & 1) == 0) o0 = act4(acc[ps][0][mi], cv0, rtv[mi]);
      else {
        u32x2 o1 = act4(acc[ps][1][mi], cv1, rtv[mi]);
        st16_dev(wsr, hoff + (4 * ps + mi) * 57344, pair_swap(o0[0], o0[1], o1[0], o1[1]));
      }
    };
    float c0v = -2.0f * 0.7978845608028654f * 1.4426950408889634f;
    asm volatile("" : "+v"(c0v));
    auto mme = [&](int b, const u32x4 (&d)[4], int e) {
      const float c1 = -2.0f * 0.7978845608028654f * 1.4426950408889634f * 0.044715f;
      const int mi = e >> 1;
      const f32x4 aa = acc[0][e & 1][mi], cc = (e & 1) ? cv1 : cv0;
      float v[4], x[4], z[4];
      m1(1, b, 0, d);
#pragma unroll
      for (int q = 0; q < 4; q++) v[q] = smul(aa[q], rtv[mi][0]);
      SCHED(); m1(1, b, 1, d);
#pragma unroll
      for (int q = 0; q < 4; q++) v[q] = sfma(cc[q], rtv[mi][2], v[q]);
      u32 p0 = pkbf(v[0], v[1]), p1 = pkbf(v[2], v[3]);
      SCHED(); m1(1, b, 2, d);
      x[0] = bflo(p0); x[1] = bfhi(p0); x[2] = bflo(p1); x[3] = bfhi(p1);
      z[0] = smul(x[0], x[0]); z[1] = smul(x[1], x[1]);
      SCHED(); m1(1, b, 3, d);
      z[2] = smul(x[2], x[2]); z[3] = smul(x[3], x[3]);
#pragma unroll
      for (int q = 0; q < 4; q++) z[q] = sfmak(z[q], c1, c0v);
      SCHED(); m1(1, b, 4, d);
#pragma unroll
      for (int q = 0; q < 4; q++) z[q] = smul(z[q], x[q]);
      z[0] = __builtin_amdgcn_exp2f(z[0]); z[1] = __builtin_amdgcn_exp2f(z[1]);
      SCHED(); m1(1, b, 5, d);
      z[2] = __builtin_amdgcn_exp2f(z[2]); z[3] = __builtin_amdgcn_exp2f(z[3]);
#pragma unroll
      for (int q = 0; q < 4; q++) z[q] = sadd1(z[q]);
      SCHED(); m1(1, b, 6, d);
#pragma unroll
      for (int q = 0; q < 4; q++) z[q] = __builtin_amdgcn_rcpf(z[q]);
      SCHED(); m1(1, b, 7, d);
#pragma unroll
      for (int q = 0; q < 4; q++) x[q] = smul(x[q], z[q]);
      u32x2 o = {pkbf(x[0], x[1]), pkbf(x[2], x[3])};
      if (e & 1) st16_dev(wsr, hoff + mi * 57344, pair_swap(o0[0], o0[1], o[0], o[1])); else o0 = o;
      SCHED();
    };
    SCHED();
#define SL(i) (((i) + 4) % 9)
    wait_vm_lgkm<8>(); bar(); ld(SL(0), a[0]); G4_R1A(0)
    wait_vm_lgkm<8>(); bar(); ld(SL(1), a[1]); G4_R1A(1) mm(0, 0, a[0]); SCHED();
    wait_vm_lgkm<8>(); bar(); G3_A(9) ld(SL(2), a[0]); G4_R1A(2) mm(0, 1, a[1]); SCHED();
    wait_vm_lgkm<9>(); bar(); G3_A(10) ld(SL(3), a[1]); G4_R1A(3) mm(0, 2, a[0]); SCHED();
    wait_vm_lgkm<10>(); bar(); G3_A(11) ld(SL(4), a[0]); G4_R1A(4) mm(0, 3, a[1]); SCHED();
    wait_vm_lgkm<11>(); bar(); G3_A(12) ld(SL(5), a[1]); G4_R1A(5) mm(0, 4, a[0]); SCHED();
    wait_vm_lgkm<12>(); bar(); G3_A(13) ld(SL(6), a[0]); G4_R1A(6) mm(0, 5, a[1]); SCHED();
    wait_vm_lgkm<13>(); bar(); G3_A(14) ld(SL(7), a[1]); G4_R1A(7) mm(0, 6, a[0]); SCHED();
    wait_vm_lgkm<14>(); bar(); G3_A(15) ld(SL(8), a[0]); ldrt(0); mm(0, 7, a[1]); SCHED();
    STAMP(17);
    wait_vm_lgkm<12>(); bar(); ld(SL(9), a[1]); G4_R1B(2) mme(0, a[0], 0);
    wait_vm_lgkm<11>(); bar(); ld(SL(10), a[0]); G4_R1B(3) mme(1, a[1], 1);
    wait_vm_lgkm<11>(); bar(); ld(SL(11), a[1]); G4_R1B(4) mme(2, a[0], 2);
    wait_vm_lgkm<10>(); bar(); ld(SL(12), a[0]); G4_R1B(5) mme(3, a[1], 3);
    wait_vm_lgkm<10>(); bar(); ld(SL(13), a[1]); G4_R1B(6) mm(1, 4, a[0]); SCHED();
    wait_vm_lgkm<9>(); bar(); ld(SL(14), a[0]); G4_R1B(7) mm(1, 5, a[1]); SCHED();
    wait_vm_lgkm<1>(); bar();
    if (tid < 8) st8_dev(wsr, FL_5 + tid * 2048 + (pm3 * 14 + pn3) * 8, tag);
    ld(SL(15), a[1]); ldrt(1); mm(1, 6, a[0]); SCHED();
    lds_bar();
    G4_R1B(0) G4_R1B(1)
    mm(1, 7, a[1]);
    mfence();
#pragma unroll
    for (int i = 0; i < 4; i++) pin(acc[1][i >> 1][i & 1]);
    STAMP(18);
#pragma unroll
    for (int i = 0; i < 4; i++) ep(1, i);
    if (w == 0) polln(wsr, FL_5 + pn * 2048, (pm >> 1) * 14, (pm >> 1) * 14 + 14, tag, lane);
    wait_vm_lgkm<0>(); bar();
    if (tid < 8) st8_dev(wsr, FL_6 + tid * 2048 + (pm3 * 14 + pn3) * 8, tag);
  } else {
    wait_vm<0>(); bar();
    G4_R1A(0) G4_R1A(1) G4_R1A(2) G4_R1A(3) G4_R1A(4) G4_R1A(5) G4_R1A(6) G4_R1A(7)
    G4_R1B(0) G4_R1B(1) G4_R1B(2) G4_R1B(3) G4_R1B(4) G4_R1B(5) G4_R1B(6) G4_R1B(7)
    if (w == 0) polln(wsr, FL_5 + pn * 2048, (pm >> 1) * 14, (pm >> 1) * 14 + 14, tag, lane);
    wait_vm<0>(); bar();
  }
  STAMP(19);
  {
    const int wm = mt >> 1, kq = (mt & 1) + 2 * wn;
    const int kb = kn_base(lane);
    u32x4(&B4)[8][4] = *reinterpret_cast<u32x4(*)[8][4]>(&B);
    const char* sa = p.ws + WS_HID + (size_t)((rt & ~1) * 56 + 14 * kq) * 1024;
    const u32 va = (u32)(ar * 64) + apo;
    const int ring = (wm ? 0 : 65536) + kq * 8192;
    const u32 da = lds0 + ring;
#define G4_A4(b) glds<((b) & 1) * 4096, (b) * 2048>(va, sa, da); glds<((b) & 1) * 4096 + 1024, (b) * 2048 + 512>(va, sa, da); \
                 glds<((b) & 1) * 4096 + 2048, (b) * 2048 + 57344>(va, sa, da); glds<((b) & 1) * 4096 + 3072, (b) * 2048 + 57344 + 512>(va, sa, da);
    if (wm == 0) { G4_A4(0) G4_A4(1) }
#pragma unroll
    for (int s = 0; s < 4; s++)
#pragma unroll
      for (int nt = 0; nt < 4; nt++) {
        B4[s][nt] = kn_frag(smem + GA_B + kq * 16384 + s * 4096 + (kb ^ (nt << 5)));
        B4[4 + s][nt] = kn_frag(smem + kq * 16384 + s * 4096 + (kb ^ (nt << 5)));
      }
    lds_bar();
    if (wm) {
      const u32 vb2 = kn_voff(D_, w - 2, lane), dw2 = dw - 2048;
#define G4_R2X(i) G4_R2(i) glds<G4_R2POS(i), (((i) / 3) * 7 + 4 + ((i) % 3)) * CBD>(vb2, sb4, dw2);
      G4_R2X(0) G4_R2X(3) G4_R2X(6) G4_R2X(9) G4_R2X(1) G4_R2X(4) G4_R2X(7) G4_R2X(10) G4_R2X(2) G4_R2X(5) G4_R2X(8) G4_R2X(11)
      polln(wsr, FL_6 + (pn ^ kq) * 2048, (pm >> 1) * 14, (pm >> 1) * 14 + 14, tag, lane);
      G4_A4(0) G4_A4(1)
      wait_vm<8>(); bar();
    }
    STAMP(20);
    const char* pa0 = smem + ring + mk_base(lane, 0);
    const char* pa1 = smem + ring + mk_base(lane, 1);
    f32x4 acc[4][2];
    u32x4 a[4];
    auto ld = [&](int b) {
      a[0] = *(const u32x4*)(pa0 + (b & 1) * 4096); a[1] = *(const u32x4*)(pa0 + (b & 1) * 4096 + 2048);
      a[2] = *(const u32x4*)(pa1 + (b & 1) * 4096); a[3] = *(const u32x4*)(pa1 + (b & 1) * 4096 + 2048);
      asm volatile("s_waitcnt lgkmcnt(0)" ::: "memory");
      SCHED();
    };
    auto mm = [&](int s0, bool first = false) {
#pragma unroll
      for (int ks = 0; ks < 2; ks++)
#pragma unroll
        for (int nt = 0; nt < 4; nt++)
#pragma unroll
          for (int mi = 0; mi < 2; mi++) mmaz(first && ks == 0, acc[nt][mi], B4[s0 + ks][nt], a[2 * ks + mi]);
      SCHED();
    };
    auto ldb = [&](int j, int s0) {
      const int i = kq * 3 + j;
      const char* bl = smem + (i < 4 ? 32768 + i * 8192 : GA_B + (i - 4) * 8192);
#pragma unroll
      for (int ks = 0; ks < 2; ks++)
#pragma unroll
        for (int nt = 0; nt < 4; nt++) B4[s0 + ks][nt] = kn_frag(bl + ks * 4096 + (kb ^ (nt << 5)));
      SCHED();
    };
    wait_vm<4>(); ld(0); G4_A4(2) mm(0, true);
    wait_vm<4>(); ld(1); G4_A4(3) mm(2);
    wait_vm<4>(); ld(2); G4_A4(4) mm(4);
    wait_vm<4>(); ld(3); G4_A4(5) mm(6);
    if (wm == 0) bar();
    ldb(0, 0); ldb(1, 2);
    wait_vm<4>(); ld(4); G4_A4(6) ldb(2, 4); mm(0);
    wait_vm<4>(); ld(5); mm(2);
    wait_vm<0>(); ld(6); mm(4);
    mfence();
#pragma unroll
    for (int i = 0; i < 8; i++) pin(acc[i >> 1][i & 1]);
    STAMP(21);
    {
      char* d = smem + ring + lane * 16;
#pragma unroll
      for (int k2 = 0; k2 < 4; k2++) {
        *(f32x4*)(d + k2 * 2048) = acc[2 * (k2 >> 1)][k2 & 1];
        *(f32x4*)(d + k2 * 2048 + 1024) = acc[2 * (k2 >> 1) + 1][k2 & 1];
      }
    }
    lds_bar();
    const char* s = smem + (wm ? 0 : 65536) + kq * 2048 + lane * 16;
    f32x4 f0 = (*(const f32x4*)(s) + *(const f32x4*)(s + 8192)) + (*(const f32x4*)(s + 16384) + *(const f32x4*)(s + 24576));
    f32x4 f1 = (*(const f32x4*)(s + 1024) + *(const f32x4*)(s + 9216)) + (*(const f32x4*)(s + 17408) + *(const f32x4*)(s + 25600));
    u32x4 v = pair_swap(pkbf(f0[0], f0[1]), pkbf(f0[2], f0[3]), pkbf(f1[0], f1[1]), pkbf(f1[2], f1[3]));
    u32x4 o;
#pragma unroll
    for (int i = 0; i < 4; i++) o[i] = pkbf2(bfup(hres[i]) + bfup(pkbf2(bfup(xres[i]) + bfup(v[i]))));
    *(u32x4*)(p.out + (size_t)(rt * 16 + r16) * D_ + col0 + 32 * wn + pc4 * 8) = o;
  }
  STAMP(22);
  asm volatile(".Lk_end:");
}

extern "C" void kernel_launch(void* const* d_in, const int* in_sizes, int n_in, void* d_out, int out_size, void* d_ws,
                              size_t ws_size, hipStream_t stream) {
  Params p;
  p.x = (const u16*)d_in[0]; p.are = (const u16*)d_in[1]; p.aim = (const u16*)d_in[2]; p.bre = (const u16*)d_in[3];
  p.bim = (const u16*)d_in[4]; p.cre = (const u16*)d_in[5]; p.cim = (const u16*)d_in[6]; p.logdt = (const u16*)d_in[7];
  p.dskip = (const u16*)d_in[8]; p.w1 = (const u16*)d_in[10]; p.w2 = (const u16*)d_in[11]; p.w3 = (const u16*)d_in[12];
  p.w4 = (const u16*)d_in[13]; p.out = (u16*)d_out; p.ws = (char*)d_ws;
  k_fused<<<256, 512, 0, stream>>>(p);
}
```

```cpp
#include <hip/hip_runtime.h>
#include <stdint.h>

typedef unsigned short u16;
typedef unsigned int u32;
typedef unsigned long long u64;
typedef __bf16 bf16x2 __attribute__((ext_vector_type(2)));
typedef __bf16 bf16x8 __attribute__((ext_vector_type(8)));
typedef _Float16 f16x2 __attribute__((ext_vector_type(2)));
typedef _Float16 f16x8 __attribute__((ext_vector_type(8)));
typedef float f32x2 __attribute__((ext_vector_type(2)));
typedef float f32x4 __attribute__((ext_vector_type(4)));
typedef short s16x4 __attribute__((ext_vector_type(4)));
typedef u32 u32x2 __attribute__((ext_vector_type(2)));
typedef u32 u32x4 __attribute__((ext_vector_type(4)));
#define LDSP __attribute__((address_space(3)))
#define DEV __device__ __forceinline__

constexpr int L_ = 2048, D_ = 512, NS_ = 128, F_ = 1792;
constexpr u32 WS_U8 = 0;
constexpr u32 WS_HT = 7u << 20;
constexpr u32 WS_H1 = 13u << 20;
constexpr u32 WS_X1 = 16u << 20;
constexpr u32 WS_ST = 18u << 20;
constexpr u32 WS_HID = 19u << 20;
constexpr u32 WS_FL = 28u << 20;
constexpr u32 FL_EPOCH = WS_FL, FL_1 = WS_FL + 4096, FL_2 = FL_1 + 32768, FL_3 = FL_2 + 32768, FL_4 = FL_3 + 16384, FL_5 = FL_4 + 16384, FL_6 = FL_5 + 16384;

struct Params {
  const u16 *x, *are, *aim, *bre, *bim, *cre, *cim, *logdt, *dskip, *w1, *w2, *w3, *w4;
  u16* out;
  char* ws;
};

DEV float bf2f(u32 v) { return __uint_as_float(v << 16); }
DEV float bflo(u32 v) { return __uint_as_float(v << 16); }
DEV float bfhi(u32 v) { return __uint_as_float(v & 0xffff0000u); }
DEV f32x2 bfup(u32 v) { return f32x2{bflo(v), bfhi(v)}; }
DEV u32 pkbf2(f32x2 v) { return __builtin_bit_cast(u32, __builtin_convertvector(v, bf16x2)); }
DEV u32 pkbf(float a, float b) { return pkbf2(f32x2{a, b}); }
DEV u32 pkh(float a, float b) { f32x2 v = {a, b}; return __builtin_bit_cast(u32, __builtin_convertvector(v, f16x2)); }
DEV float rbf(float a) { return bflo(pkbf(a, a)); }
DEV f32x2 hup(u32 v) { return __builtin_convertvector(__builtin_bit_cast(f16x2, v), f32x2); }

template <int CTRL> DEV float dppf(float v) {
  return __builtin_bit_cast(float, __builtin_amdgcn_update_dpp(0, __builtin_bit_cast(int, v), CTRL, 0xf, 0xf, true));
}
DEV float gsum(float v) {
  u32 a = __float_as_uint(v);
  auto r = __builtin_amdgcn_permlane16_swap(a, a, false, false);
  v = __uint_as_float(r[0]) + __uint_as_float(r[1]);
  a = __float_as_uint(v);
  auto q = __builtin_amdgcn_permlane32_swap(a, a, false, false);
  return __uint_as_float(q[0]) + __uint_as_float(q[1]);
}
DEV float gmax(float v) {
  u32 a = __float_as_uint(v);
  auto r = __builtin_amdgcn_permlane16_swap(a, a, false, false);
  v = fmaxf(__uint_as_float(r[0]), __uint_as_float(r[1]));
  a = __float_as_uint(v);
  auto q = __builtin_amdgcn_permlane32_swap(a, a, false, false);
  return fmaxf(__uint_as_float(q[0]), __uint_as_float(q[1]));
}
DEV float wave_sum(float v) {
  v += dppf<0xB1>(v); v += dppf<0x4E>(v); v += dppf<0x141>(v); v += dppf<0x140>(v);
  return gsum(v);
}
DEV float wave_max(float v) {
  v = fmaxf(v, dppf<0xB1>(v)); v = fmaxf(v, dppf<0x4E>(v)); v = fmaxf(v, dppf<0x141>(v)); v = fmaxf(v, dppf<0x140>(v));
  return gmax(v);
}
DEV u32x2 tr_read(const char* p) {
  return __builtin_bit_cast(u32x2, __builtin_amdgcn_ds_read_tr16_b64_v4i16((LDSP s16x4*)p));
}
DEV void mma(f32x4& c, u32x4 a, u32x4 b) { asm volatile("v_mfma_f32_16x16x32_bf16 %0, %1, %2, %0" : "+v"(c) : "v"(a), "v"(b)); }
DEV void mma0(f32x4& c, u32x4 a, u32x4 b) { asm volatile("v_mfma_f32_16x16x32_bf16 %0, %1, %2, 0" : "=&v"(c) : "v"(a), "v"(b)); }
DEV void mmah(f32x4& c, u32x4 a, u32x4 b) { asm volatile("v_mfma_f32_16x16x32_f16 %0, %1, %2, %0" : "+v"(c) : "v"(a), "v"(b)); }
DEV void mmah0(f32x4& c, u32x4 a, u32x4 b) { asm volatile("v_mfma_f32_16x16x32_f16 %0, %1, %2, 0" : "=&v"(c) : "v"(a), "v"(b)); }
DEV void mmaz(bool first, f32x4& c, u32x4 a, u32x4 b) { if (first) mma0(c, a, b); else mma(c, a, b); }
DEV void mfence() { asm volatile("s_nop 7" ::: "memory"); __builtin_amdgcn_sched_barrier(0); }
DEV float smul(float a, float b) { float r; asm("v_mul_f32 %0, %1, %2" : "=v"(r) : "v"(a), "v"(b)); return r; }
DEV float sfma(float a, float b, float c) { float r; asm("v_fma_f32 %0, %1, %2, %3" : "=v"(r) : "v"(a), "v"(b), "v"(c)); return r; }
DEV float sfmak(float a, float k1, float k0) { float r; asm("v_fma_f32 %0, %1, %2, %3" : "=v"(r) : "v"(a), "s"(k1), "v"(k0)); return r; }
DEV float sadd1(float a) { float r; asm("v_add_f32 %0, 1.0, %1" : "=v"(r) : "v"(a)); return r; }
DEV void pin(f32x4& c) { asm volatile("" : "+v"(c)); }
DEV float dot2(u32 a, u32 b, float c) {
  return __builtin_amdgcn_fdot2_f32_bf16(__builtin_bit_cast(bf16x2, a), __builtin_bit_cast(bf16x2, b), c, false);
}
template <int LOFF, int MOFF> DEV void glds(u32 voff, const void* sbase, u32 lds) {
  asm volatile("s_add_u32 m0, %1, %3\n\ts_nop 0\n\tglobal_load_lds_dwordx4 %0, %2"
               :: "v"(voff + (u32)MOFF), "s"(lds), "s"(sbase), "n"(LOFF) : "memory", "m0", "scc");
}
DEV void touch(u32& t, const void* sbase, u32 voff) {
  asm volatile("global_load_dword %0, %1, %2" : "+v"(t) : "v"(voff), "s"(sbase) : "memory");
}
DEV void keep(u32 t) { asm volatile("" :: "v"(t)); }
DEV u32 lds_addr(const char* p) { return (u32)(size_t)(LDSP const char*)p; }
#define SCHED() __builtin_amdgcn_sched_barrier(0)
template <int N> DEV void wait_vm() { asm volatile("s_waitcnt vmcnt(%0)" ::"n"(N) : "memory"); }
template <int N> DEV void wait_vm_lgkm() { asm volatile("s_waitcnt vmcnt(%0) lgkmcnt(0)" ::"n"(N) : "memory"); }
DEV void bar() { __builtin_amdgcn_s_barrier(); }
DEV void lds_bar() { asm volatile("s_waitcnt lgkmcnt(0)" ::: "memory"); __builtin_amdgcn_s_barrier(); }

#define STAMP(i)

typedef __amdgpu_buffer_rsrc_t rsrc_t;
DEV void st16_dev(rsrc_t r, u32 off, u32x4 v) { __builtin_amdgcn_raw_buffer_store_b128(v, r, off, 0, 16); }
DEV void st8_dev(rsrc_t r, u32 off, u32x2 v) { __builtin_amdgcn_raw_buffer_store_b64(v, r, off, 0, 16); }
DEV u32x4 ld16_dev(rsrc_t r, u32 off) { return __builtin_amdgcn_raw_buffer_load_b128(r, off, 0, 16); }
DEV u32x2 ld8_dev(rsrc_t r, u32 off) { return __builtin_amdgcn_raw_buffer_load_b64(r, off, 0, 16); }

constexpr int SPIN_MAX = 1 << 18;
DEV void poll256(rsrc_t r, u32 base, u32x2 tag, int lane) {
  for (int s = 0; s < SPIN_MAX; s++) {
    u32x4 a = ld16_dev(r, base + lane * 32), b = ld16_dev(r, base + lane * 32 + 16);
    bool ok = a[0] == tag[0] && a[1] == tag[1] && a[2] == tag[0] && a[3] == tag[1] &&
              b[0] == tag[0] && b[1] == tag[1] && b[2] == tag[0] && b[3] == tag[1];
    if (__all(ok)) break;
    __builtin_amdgcn_s_sleep(2);
  }
}
DEV void polln(rsrc_t r, u32 base, int lo, int hi, u32x2 tag, int lane) {
  const int i = min(lo + lane, hi - 1);
  for (int s = 0; s < SPIN_MAX; s++) {
    u32x2 a = ld8_dev(r, base + i * 8);
    if (__all(a[0] == tag[0] && a[1] == tag[1])) break;
    __builtin_amdgcn_s_sleep(2);
  }
}

DEV u32x2 act4(f32x4 a, f32x4 cs, f32x4 rt) {
  const float c0 = -2.0f * 0.7978845608028654f * 1.4426950408889634f;
  const float c1 = c0 * 0.044715f;
  const f32x2 rs = {rt[0], rt[1]}, t = {rt[2], rt[3]};
  f32x2 v0 = f32x2{a[0], a[1]} * rs + f32x2{cs[0], cs[1]} * t, v1 = f32x2{a[2], a[3]} * rs + f32x2{cs[2], cs[3]} * t;
  f32x4 v = {v0[0], v0[1], v1[0], v1[1]};
  u32 p0 = pkbf(v[0], v[1]), p1 = pkbf(v[2], v[3]);
  f32x4 x = {bflo(p0), bfhi(p0), bflo(p1), bfhi(p1)};
  f32x4 z = x * (x * x * c1 + c0);
  f32x4 e = {__builtin_amdgcn_exp2f(z[0]), __builtin_amdgcn_exp2f(z[1]), __builtin_amdgcn_exp2f(z[2]), __builtin_amdgcn_exp2f(z[3])};
  e += 1.0f;
  f32x4 r = {__builtin_amdgcn_rcpf(e[0]), __builtin_amdgcn_rcpf(e[1]), __builtin_amdgcn_rcpf(e[2]), __builtin_amdgcn_rcpf(e[3])};
  x *= r;
  return u32x2{pkbf(x[0], x[1]), pkbf(x[2], x[3])};
}

constexpr int LDS_ALL = 163840;
constexpr int KG_ROW = 528;
constexpr int KG_CH = 73 * KG_ROW;
constexpr int P0_KT = 77312;
constexpr int P0_LN = 101888;
constexpr int P0_MS = 119296;
constexpr int CV_COPY = 4160, CV_KP = 4 * CV_COPY, CV_CH = CV_KP + 4608;
constexpr int CV_UI = 2 * CV_CH;
constexpr int CV_RED = CV_UI + 16384;
constexpr int CV_STG = 0;
constexpr int GA_A = 0, GA_TAB = 65536, GA_B = 98304;
constexpr int G3_ST = 73728, G3_TAB = 90112;
constexpr int G4_B = 65536;

struct KTbl {
  u16 v[2048];
  constexpr KTbl() : v() {
    for (int pos = 0; pos < 2048; pos++) {
      int l = (pos & ~7) + 7 - (pos & 7);
      int sh = l < 256 ? 0 : l < 512 ? 1 : l < 1024 ? 2 : 3;
      int lv = sh ? (((l + (1 << (sh - 1)) - 1 + ((l >> sh) & 1)) >> sh) << sh) : l;
      int s = lv < 256 ? 0 : lv < 512 ? 1 : lv < 1024 ? 2 : 3;
      int rr = 8 * s + ((lv >> 7) << (3 - s)) + ((lv & 7) >> s);
      v[pos] = (u16)((rr * 16 + ((lv >> 3) & 15)) * 16);
    }
  }
};
__device__ __attribute__((aligned(16))) constexpr KTbl KTBL{};

struct cf { float x, y; };
DEV cf cmul(cf a, cf b) { return {a.x * b.x - a.y * b.y, a.x * b.y + a.y * b.x}; }
DEV cf cscale(cf a, float s) { return {a.x * s, a.y * s}; }
DEV cf cexpk(float tl2, float rh, float rl, float s) {
  float mag = __builtin_amdgcn_exp2f(tl2 * s);
  float p = rh * s;
  float f = __builtin_fmaf(rl, s, p - __builtin_rintf(p));
  return {mag * __builtin_amdgcn_cosf(f), mag * __builtin_amdgcn_sinf(f)};
}
DEV void st_x(char* p, cf a) { *(u32*)p = pkh(a.x, a.y); }
DEV void st_y(char* p, cf a) {
  u32 hi = pkh(a.x, a.y);
  f32x2 r = f32x2{a.x, a.y} - hup(hi);
  *(u32*)p = hi;
  *(u32*)(p + 16 * KG_ROW) = pkh(r[0], r[1]);
}
DEV float pow2_scale(float mx, u32 target, float& inv) {
  u32 eb = (__float_as_uint(mx) >> 23) & 0xffu;
  u32 se = (eb == 0 || eb == 255) ? 127u : min(254u + target - eb, 253u);
  inv = __uint_as_float((254u - se) << 23);
  return __uint_as_float(se << 23);
}

DEV void conv_load1(const char* pa, const char* pb, int b0, int i, u32x4 (&A)[8], u32x4 (&B)[8]) {
  u32x2 lo = *(const volatile LDSP u32x2*)(pa + 64 * b0 + 512 * i), hi = *(const volatile LDSP u32x2*)(pa + 64 * b0 + 512 * i + 8);
  A[i] = u32x4{lo[0], lo[1], hi[0], hi[1]};
  B[i] = *(const u32x4*)(pb - 64 * b0 + 512 * i);
}
DEV void conv_step(const char* pa, const char* pb, int nb, const u32x4 (&A)[8], const u32x4 (&B)[8], u32x4 (&An)[8], u32x4 (&Bn)[8], f32x4 (&acc)[8]) {
  u32x2 lo[8], hi[8];
  int c = 0;
#pragma unroll
  for (int i = 0; i < 8; i++) {
#pragma unroll
    for (int t = i; t < 8; t++) {
      if (nb == 1 && i == 0) mmah0(acc[t], A[i], B[t - i]); else mmah(acc[t], A[i], B[t - i]);
      if (nb < 4 && c < 24) {
        const int f = c / 3, k = c % 3;
        if (k == 0) Bn[f] = *(const u32x4*)(pb - 64 * nb + 512 * f);
        else if (k == 1) lo[f] = *(const volatile LDSP u32x2*)(pa + 64 * nb + 512 * f);
        else hi[f] = *(const volatile LDSP u32x2*)(pa + 64 * nb + 512 * f + 8);
      }
      c++;
      SCHED();
    }
  }
  if (nb < 4) {
#pragma unroll
    for (int f = 0; f < 8; f++) An[f] = u32x4{lo[f][0], lo[f][1], hi[f][0], hi[f][1]};
  }
}

DEV u32 kn_voff(int ld, int w, int lane) {
  int kr = lane >> 3, pos = lane & 7;
  int f2 = ((lane >> 4) & 1) | ((w & 1) << 1);
  return (u32)((8 * w + kr) * ld * 2 + ((pos ^ (f2 << 1)) << 4));
}
DEV u32 mk_voff(int ld, int lane) { int r = lane >> 3, pos = lane & 7; return (u32)(r * ld * 2 + ((pos ^ r) << 4)); }
DEV int kn_base(int lane) {
  int g = lane >> 4, q = (lane & 15) >> 2, pp = lane & 3;
  int f2 = ((q >> 1) & 1) | ((g & 1) << 1);
  return (8 * g + q) * 128 + (f2 << 5) + pp * 8;
}
DEV u32x4 kn_frag(const char* p) { u32x2 a = tr_read(p), b = tr_read(p + 512); return u32x4{a[0], a[1], b[0], b[1]}; }
DEV int mk_base(int lane, int ks) { int r16 = lane & 15, g = lane >> 4; return r16 * 128 + (((4 * ks + g) ^ (r16 & 7)) << 4); }
constexpr u32 ONES = 0x3f803f80u;
DEV u32x4 pair_swap(u32 p01, u32 p23, u32 q01, u32 q23) {
  auto r0 = __builtin_amdgcn_permlane16_swap(p01, q01, false, false);
  auto r1 = __builtin_amdgcn_permlane16_swap(p23, q23, false, false);
  return u32x4{r0[0], r1[0], r0[1], r1[1]};
}
template <int LDW> DEV void w64_issue(const u16* wsrc, u32 dst, int w, int lane) {
  const u32 vo = kn_voff(LDW, w, lane);
  const u32 d = dst + w * 1024;
  glds<0 * 8192, 0 * 128 * LDW>(vo, wsrc, d); glds<1 * 8192, 1 * 128 * LDW>(vo, wsrc, d);
  glds<2 * 8192, 2 * 128 * LDW>(vo, wsrc, d); glds<3 * 8192, 3 * 128 * LDW>(vo, wsrc, d);
  glds<4 * 8192, 4 * 128 * LDW>(vo, wsrc, d); glds<5 * 8192, 5 * 128 * LDW>(vo, wsrc, d);
  glds<6 * 8192, 6 * 128 * LDW>(vo, wsrc, d); glds<7 * 8192, 7 * 128 * LDW>(vo, wsrc, d);
}
DEV void w64_regs(const char* img, int wn, int lane, u32x4 (&B)[16][2]) {
  const char* p0 = img + (kn_base(lane) ^ ((2 * wn) << 5));
  const char* p1 = img + (kn_base(lane) ^ ((2 * wn + 1) << 5));
#pragma unroll
  for (int s = 0; s < 16; s++) { B[s][0] = kn_frag(p0 + s * 4096); B[s][1] = kn_frag(p1 + s * 4096); }
}
template <int LDW> DEV void w64_issue4(const u16* wsrc, u32 dst, int w, int lane) {
  w64_issue<LDW>(wsrc, dst, w - 4, lane); w64_issue<LDW>(wsrc, dst, w, lane);
}
template <int H> DEV void w3_issue(const u16* sb, u32 lds0, int w, int lane) {
  constexpr int CB3 = 128 * F_;
  const u32 vb = kn_voff(F_, w, lane), dw = lds0 + GA_B + w * 1024;
  glds<0, (4 * H) * CB3>(vb, sb, dw); glds<8192, (4 * H) * CB3 + 128>(vb, sb, dw);
  glds<16384, (4 * H + 1) * CB3>(vb, sb, dw); glds<24576, (4 * H + 1) * CB3 + 128>(vb, sb, dw);
  glds<32768, (4 * H + 2) * CB3>(vb, sb, dw); glds<40960, (4 * H + 2) * CB3 + 128>(vb, sb, dw);
  glds<49152, (4 * H + 3) * CB3>(vb, sb, dw); glds<57344, (4 * H + 3) * CB3 + 128>(vb, sb, dw);
}

__global__ __launch_bounds__(512, 2) void k_fused(Params p) {
  __shared__ __attribute__((aligned(16))) char smem[LDS_ALL];
  const int bid = blockIdx.x;
  const int tid = threadIdx.x, w = __builtin_amdgcn_readfirstlane(tid >> 6), lane = tid & 63;
  const int r16 = lane & 15, g = lane >> 4;
  const u32 lds0 = lds_addr(smem);
  const rsrc_t wsr = __builtin_amdgcn_make_buffer_rsrc(p.ws, 0, 0x4000000, 0x00020000);
  STAMP(0);
  const int cp = (bid & 7) * 32 + (bid >> 3);
  const int pm = (bid & 7) * 4 + (bid >> 6), pn = (bid >> 3) & 7;
  const int row0 = pm * 64, col0 = pn * 64;
  u32x2 tag;
  u32 TKW = 0;
  {
    u32x2 ep = ld8_dev(wsr, FL_EPOCH);
    u32x4 xv = *(const u32x4*)(p.x + (bid * 8 + w) * D_ + lane * 8);
    const int chl = w >> 2, role = (w >> 1) & 1, n = tid & 127;
    const int ch = cp * 2 + chl;
    const int pi = ch * NS_ + n;
    u32 r_are = p.are[pi], r_aim = p.aim[pi], r_bre = p.bre[pi], r_bim = p.bim[pi], r_cre = p.cre[pi], r_cim = p.cim[pi];
    u32 r_ldt = p.logdt[ch];
    u32x4 kidx = *(const u32x4*)(KTBL.v + (tid & 255) * 8);
    SCHED();
    u32 sk0, sk1, sk2;
    {
      u64 pc; u32 len;
      asm volatile("s_getpc_b64 %0\n.Lk_here:\n\ts_mov_b32 %1, .Lk_end-.Lk_here" : "=s"(pc), "=s"(len));
      const u32 o = (u32)((bid >> 3) * 24 + w * 3) * 64;
      asm volatile("s_load_dword %0, %3, %4\n\ts_load_dword %1, %3, %5\n\ts_load_dword %2, %3, %6"
                   : "=&s"(sk0), "=&s"(sk1), "=&s"(sk2) : "s"(pc), "s"(min(o, len)), "s"(min(o + 64, len)), "s"(min(o + 128, len)) : "memory");
    }
    {
      u64 e = ((u64)ep[1] << 32 | ep[0]) + 1;
      tag = u32x2{(u32)__builtin_amdgcn_readfirstlane((u32)e), (u32)__builtin_amdgcn_readfirstlane((u32)(e >> 32))};
    }
    char* su = smem + P0_LN;
    float* ms = (float*)(smem + P0_MS);
    {
      f32x2 v[4];
#pragma unroll
      for (int i = 0; i < 4; i++) v[i] = bfup(xv[i]);
      f32x2 s2 = (v[0] + v[1]) + (v[2] + v[3]);
      float mean = wave_sum(s2[0] + s2[1]) * (1.0f / D_);
      f32x2 q2 = {0, 0};
#pragma unroll
      for (int i = 0; i < 4; i++) { f32x2 d = v[i] - mean; q2 += d * d; }
      float var = wave_sum(q2[0] + q2[1]) * (1.0f / D_);
      float m = rbf(mean);
      float inv = __builtin_amdgcn_rcpf(rbf(__builtin_amdgcn_sqrtf(rbf(rbf(var) + 1e-5f))));
      u32x4 uo;
#pragma unroll
      for (int i = 0; i < 4; i++) {
        f32x2 u = bfup(pkbf2(bfup(pkbf2(v[i] - m)) * inv));
        uo[i] = pkh(u[0], u[1]);
      }
      u32x4 lo4, hi4;
#pragma unroll
      for (int i = 0; i < 2; i++) {
        lo4[2 * i] = __builtin_amdgcn_perm(xv[i], uo[i], 0x05040100u); lo4[2 * i + 1] = __builtin_amdgcn_perm(xv[i], uo[i], 0x07060302u);
        hi4[2 * i] = __builtin_amdgcn_perm(xv[2 + i], uo[2 + i], 0x05040100u); hi4[2 * i + 1] = __builtin_amdgcn_perm(xv[2 + i], uo[2 + i], 0x07060302u);
      }
      *(u32x4*)(su + w * 2112 + lane * 32) = lo4;
      *(u32x4*)(su + w * 2112 + lane * 32 + 16) = hi4;
    }
    asm volatile("" : "+v"(kidx));
    STAMP(1);
    lds_bar();
    asm volatile("" :: "s"(sk0), "s"(sk1), "s"(sk2));
    {
      const int q4 = (lane & 15) >> 2, pp = lane & 3;
      const char* a = su + q4 * 2112 + (128 * w + 16 * g + 4 * pp) * 2;
      u32x2 u0 = tr_read(a), u1 = tr_read(a + 4 * 2112), x0 = tr_read(a + 128), x1 = tr_read(a + 4 * 2112 + 128);
      const u32 dst = WS_U8 + bid * 16384 + w * 2048 + lane * 16;
      st16_dev(wsr, dst, u32x4{u0[0], u0[1], u1[0], u1[1]});
      st16_dev(wsr, dst + 1024, u32x4{x0[0], x0[1], x1[0], x1[1]});
    }
    float dt = rbf(expf(bf2f(r_ldt)));
    float ar = dt * bf2f(r_are), ai = dt * bf2f(r_aim);
    cf CB = cmul(cf{bf2f(r_cre), bf2f(r_cim)}, cf{dt * bf2f(r_bre), dt * bf2f(r_bim)});
    float tl2 = ar * 1.4426950408889634f;
    const float I2PH = 0.15915494309189535f;
    const float I2PL = (float)(0.15915494309189533576888 - (double)0.15915494309189535f);
    float rh = ai * I2PH;
    float rl = __builtin_fmaf(ai, I2PH, -rh) + ai * I2PL;
    {
      float ax = fabsf(CB.x), ay = fabsf(CB.y);
      float mx = wave_max(fmaxf(ax, ay)), bs = wave_sum(ax + ay), isx;
      float sx = pow2_scale(mx, 13, isx);
      CB = cscale(CB, sx);
      if (role == 0 && lane == 0) { ms[chl * 2 + (w & 1)] = bs; ms[4 + chl * 2 + (w & 1)] = isx; }
    }
    {
      const u16* wb = w == 0 ? p.w3 : w == 1 ? p.w4 : w == 2 ? p.w1 : p.w2;
      touch(TKW, wb, w < 2 ? (u32)(bid * 56 + min(lane, 55)) * 128 : (u32)(bid * 16 + (lane & 15)) * 128);
    }
    STAMP(2);
    char* img = smem + chl * KG_CH;
    {
      char* wp = img + n * 4;
      if (role == 0) {
        cf e3 = cexpk(tl2, -rh, -rl, 8.f), e4 = cexpk(tl2, -rh, -rl, 16.f), e5 = cexpk(tl2, -rh, -rl, 32.f), e6 = cexpk(tl2, -rh, -rl, 64.f);
        cf y[8];
        y[0] = {16384.f, 0.f}; y[1] = cscale(e3, 16384.f); y[2] = cscale(e4, 16384.f); y[3] = cmul(y[2], e3);
        y[4] = cscale(e5, 16384.f); y[5] = cmul(y[4], e3); y[6] = cmul(y[4], e4); y[7] = cmul(y[6], e3);
#pragma unroll
        for (int j = 0; j < 8; j++) st_y(wp + (41 + j) * KG_ROW, y[j]);
#pragma unroll
        for (int j = 0; j < 8; j++) st_y(wp + (49 + j) * KG_ROW, j ? cmul(y[j], e6) : cscale(e6, 16384.f));
        cf e7 = cexpk(tl2, rh, rl, 128.f), e8 = cexpk(tl2, rh, rl, 256.f), e9 = cexpk(tl2, rh, rl, 512.f);
        cf e10 = cexpk(tl2, rh, rl, 1024.f), e11 = cexpk(tl2, rh, rl, 2048.f);
        cf g8 = cmul(CB, e10), g12 = cmul(g8, e9), g16 = cmul(CB, e11);
        cf g10 = cmul(g8, e8), g14 = cmul(g12, e8);
        st_x(wp + 32 * KG_ROW, g8); st_x(wp + 33 * KG_ROW, cmul(g8, e7));
        st_x(wp + 34 * KG_ROW, g10); st_x(wp + 35 * KG_ROW, cmul(g10, e7));
        st_x(wp + 36 * KG_ROW, g12); st_x(wp + 37 * KG_ROW, cmul(g12, e7));
        st_x(wp + 38 * KG_ROW, g14); st_x(wp + 39 * KG_ROW, cmul(g14, e7));
        st_x(wp + 40 * KG_ROW, g16);
      } else {
        cf e0 = cexpk(tl2, rh, rl, 1.f), e1 = cexpk(tl2, rh, rl, 2.f), e2 = cexpk(tl2, rh, rl, 4.f);
        cf e7 = cexpk(tl2, rh, rl, 128.f), e8 = cexpk(tl2, rh, rl, 256.f), e9 = cexpk(tl2, rh, rl, 512.f);
        cf eb[8];
        eb[1] = e0; eb[2] = e1; eb[3] = cmul(e1, e0); eb[4] = e2; eb[5] = cmul(e2, e0); eb[6] = cmul(e2, e1); eb[7] = cmul(eb[6], e0);
        cf gq[8];
        gq[0] = CB; gq[1] = cmul(CB, e7); gq[2] = cmul(CB, e8); gq[3] = cmul(gq[2], e7);
        gq[4] = cmul(CB, e9); gq[5] = cmul(gq[4], e7); gq[6] = cmul(gq[4], e8); gq[7] = cmul(gq[6], e7);
#pragma unroll
        for (int q = 0; q < 2; q++)
#pragma unroll
          for (int b = 0; b < 8; b++) st_x(wp + (q * 8 + b) * KG_ROW, b ? cmul(gq[q], eb[b]) : gq[q]);
#pragma unroll
        for (int q = 2; q < 4; q++)
#pragma unroll
          for (int b = 0; b < 4; b++) st_x(wp + (16 + (q - 2) * 4 + b) * KG_ROW, b ? cmul(gq[q], eb[2 * b]) : gq[q]);
#pragma unroll
        for (int q = 4; q < 8; q++)
#pragma unroll
          for (int b = 0; b < 2; b++) st_x(wp + (24 + (q - 4) * 2 + b) * KG_ROW, b ? cmul(gq[q], e2) : gq[q]);
      }
    }
    STAMP(3);
    wait_vm_lgkm<1>(); bar();
    if (tid < 16) st8_dev(wsr, FL_1 + tid * 2048 + bid * 8, tag);
    char* kt = smem + P0_KT + chl * 12288;
    float kinv;
    const float kscale = pow2_scale(ms[chl * 2] + ms[chl * 2 + 1], 14, kinv);
    const float fsc = ms[4 + chl * 2 + ((w & 3) >> 1)] * (kscale * (1.0f / 16384.f));
    {
      const int wv = w & 3;
      const char* pa = img + r16 * KG_ROW + wv * 128 + g * 16;
      u32x4 a[3][2], yh[2], yl[2];
#pragma unroll
      for (int s = 0; s < 2; s++) {
#pragma unroll
        for (int t = 0; t < 3; t++) a[t][s] = *(const u32x4*)(pa + t * 16 * KG_ROW + s * 64);
        yh[s] = *(const u32x4*)(pa + 41 * KG_ROW + s * 64);
        yl[s] = *(const u32x4*)(pa + 57 * KG_ROW + s * 64);
      }
      const float f = fsc;
      char* kw = kt + (4 * g * 16 + r16) * 16 + ((wv + (r16 >> 3) + 2 * (g & 1)) & 3) * 4;
      f32x4 c[3];
#pragma unroll
      for (int t = 0; t < 3; t++) { mmah0(c[t], a[t][0], yl[0]); mmah(c[t], a[t][1], yl[1]); }
#pragma unroll
      for (int t = 0; t < 3; t++) { mmah(c[t], a[t][0], yh[0]); mmah(c[t], a[t][1], yh[1]); }
      mfence(); pin(c[0]); pin(c[1]); pin(c[2]);
#pragma unroll
      for (int t = 0; t < 3; t++)
#pragma unroll
        for (int r = 0; r < 4; r++) *(float*)(kw + (t * 16 + r) * 256) = c[t][r] * f;
    }
    if (w == 0) {
      poll256(wsr, FL_1 + (bid & 15) * 2048, tag, lane);
      if (bid == 0 && lane == 0) st8_dev(wsr, FL_EPOCH, tag);
    }
    lds_bar();
    STAMP(4);
    {
      const char* sa = p.ws + WS_U8 + (size_t)w * 16 * 16384 + cp * 64;
      const u32 vo = (u32)((lane >> 2) * 16384 + (lane & 3) * 16);
      glds<CV_UI, 0>(vo, sa, lds0 + w * 1024); glds<CV_UI + 8192, 128 * 16384>(vo, sa, lds0 + w * 1024);
    }
    {
      u32x4 o;
#pragma unroll
      for (int i = 0; i < 4; i++) {
        f32x4 a = *(const f32x4*)(kt + (kidx[i] & 0xffffu)), b = *(const f32x4*)(kt + (kidx[i] >> 16));
        o[i] = pkh((a[0] + a[1]) + (a[2] + a[3]), (b[0] + b[1]) + (b[2] + b[3]));
      }
      const int e = tid & 255;
      char* base = smem + chl * CV_CH;
      *(u32x4*)(base + CV_KP + (30 + e) * 16) = o;
      if (e < 30) *(u32x4*)(base + CV_KP + e * 16) = u32x4{0, 0, 0, 0};
      else if (e < 34) *(u32x4*)(base + (e - 30) * CV_COPY) = u32x4{0, 0, 0, 0};
      else if (e == 34) *(float*)(base + CV_KP + 4576) = kinv;
    }
  }
  STAMP(5);
  STAMP(6);
  {
    {
      const int chl = w >> 2, q = tid & 255;
      char* base = smem + chl * CV_CH;
      wait_vm<0>(); keep(TKW); bar();
      const char* ui = smem + CV_UI + q * 64 + chl * 32;
      u32x4 cur = *(const u32x4*)ui, prv = *(const u32x4*)(ui - 64);
      if (q == 0) prv = u32x4{0, 0, 0, 0};
      u32 wd[8] = {prv[0], prv[1], prv[2], prv[3], cur[0], cur[1], cur[2], cur[3]};
      u32 o[5];
#pragma unroll
      for (int k = 0; k < 5; k++) o[k] = __builtin_amdgcn_alignbit(wd[k + 1], wd[k], 16);
      char* d = base + (q + 1) * 16;
      *(u32x4*)(d) = u32x4{o[0], o[1], o[2], o[3]};
      *(u32x4*)(d + CV_COPY) = u32x4{wd[1], wd[2], wd[3], wd[4]};
      *(u32x4*)(d + 2 * CV_COPY) = u32x4{o[1], o[2], o[3], o[4]};
      *(u32x4*)(d + 3 * CV_COPY) = u32x4{wd[2], wd[3], wd[4], wd[5]};
      if (q == 255) {
        u32 o4 = __builtin_amdgcn_alignbit(cur[1], cur[0], 16), o5 = __builtin_amdgcn_alignbit(cur[2], cur[1], 16);
        u32 o6 = __builtin_amdgcn_alignbit(cur[3], cur[2], 16), o7 = cur[3] >> 16;
        char* d2 = base + 257 * 16;
        *(u32x4*)(d2) = u32x4{o4, o5, o6, o7};
        *(u32x4*)(d2 + CV_COPY) = u32x4{cur[1], cur[2], cur[3], 0};
        *(u32x4*)(d2 + 2 * CV_COPY) = u32x4{o5, o6, o7, 0};
        *(u32x4*)(d2 + 3 * CV_COPY) = u32x4{cur[2], cur[3], 0, 0};
      }
    }
    const int chl = (w >> 1) & 1, hh = w & 1;
    const float dsk = bf2f(p.dskip[cp * 2 + chl]);
    const char* base = smem + chl * CV_CH;
    const int j2 = 2 * (hh + 2 * (w >> 2));
    char* red = smem + CV_RED + chl * 16384 + lane * 16;
    lds_bar();
    STAMP(7);
    f32x4 f0 = {0, 0, 0, 0}, f1 = {0, 0, 0, 0};
    if (w < 4) {
      f32x4 acc[8];
      const char* pa = base + (r16 & 3) * CV_COPY + 48 - 16 * g + 2 * (r16 & 12) + 256 * hh;
      const char* pb = base + CV_KP + 448 + 32 * r16 + 16 * g - 256 * hh;
      u32x4 A0[8], B0[8], A1[8], B1[8];
#pragma unroll
      for (int i = 0; i < 8; i++) conv_load1(pa, pb, 0, i, A0, B0);
      conv_step(pa, pb, 1, A0, B0, A1, B1, acc);
      conv_step(pa, pb, 2, A1, B1, A0, B0, acc);
      conv_step(pa, pb, 3, A0, B0, A1, B1, acc);
      conv_step(pa, pb, 4, A1, B1, A0, B0, acc);
      mfence();
#pragma unroll
      for (int i = 0; i < 8; i++) pin(acc[i]);
      STAMP(27);
      char* d = red + hh * 8192;
      if (hh) { *(f32x4*)(d) = acc[0]; *(f32x4*)(d + 1024) = acc[1]; f0 = acc[2]; f1 = acc[3]; }
      else { *(f32x4*)(d + 2048) = acc[2]; *(f32x4*)(d + 3072) = acc[3]; f0 = acc[0]; f1 = acc[1]; }
#pragma unroll
      for (int t = 4; t < 8; t++) *(f32x4*)(d + t * 1024) = acc[t];
    } else {
      w64_issue<D_>(p.w1 + col0, lds0 + GA_B, w - 4, lane);
      w64_issue<D_>(p.w1 + col0, lds0 + GA_B, w, lane);
      wait_vm<0>();
    }
    const float kinv = *(const float*)(base + CV_KP + 4576);
    const int l0 = 256 * j2 + 16 * r16 + 4 * g;
    u32x2 xx[2];
    u32 dd[2][2];
#pragma unroll
    for (int k = 0; k < 2; k++) {
      const char* ui = smem + CV_UI + ((l0 + 256 * k) >> 3) * 64 + chl * 32 + (l0 & 4) * 2;
      u32x2 uu = *(const u32x2*)ui;
      xx[k] = *(const u32x2*)(ui + 16);
      dd[k][0] = pkbf2(hup(uu[0]) * dsk); dd[k][1] = pkbf2(hup(uu[1]) * dsk);
    }
    lds_bar();
    STAMP(8);
    if (w < 4) {
      f0 += *(const f32x4*)(red + (hh ^ 1) * 8192 + j2 * 1024);
      f1 += *(const f32x4*)(red + (hh ^ 1) * 8192 + j2 * 1024 + 1024);
    } else {
      f0 = *(const f32x4*)(red + j2 * 1024) + *(const f32x4*)(red + 8192 + j2 * 1024);
      f1 = *(const f32x4*)(red + j2 * 1024 + 1024) + *(const f32x4*)(red + 8192 + j2 * 1024 + 1024);
    }
    char* stg = smem + CV_STG;
#pragma unroll
    for (int k = 0; k < 2; k++) {
      f32x4 f = k ? f1 : f0;
      u32 y01 = pkbf2(f32x2{f[0], f[1]} * kinv), y23 = pkbf2(f32x2{f[2], f[3]} * kinv);
      u32 s01 = pkbf2(bfup(y01) + bfup(dd[k][0])), s23 = pkbf2(bfup(y23) + bfup(dd[k][1]));
      u32 h01 = pkbf2(bfup(xx[k][0]) + bfup(s01)), h23 = pkbf2(bfup(xx[k][1]) + bfup(s23));
      *(u32x2*)(stg + (16 * (j2 + k) + r16) * 80 + chl * 32 + g * 8) = u32x2{h01, h23};
    }
    lds_bar();
    {
      const int lb = tid >> 2, pc = tid & 3;
      st16_dev(wsr, WS_HT + cp * 8192 + tid * 16, *(const u32x4*)(stg + lb * 80 + pc * 16));
    }
  }
  const int mt = w & 3, wn = w >> 2;
  const int rt = (pm >> 1) * 8 + (pm & 1) * 2 + (mt >> 1) * 4 + (mt & 1);
  const int pc4 = (g & 1) * 2 + (g >> 1);
  const u32 boff = (u32)((rt * 16 + pn * 2 + wn) * 1024 + r16 * 64 + pc4 * 16);
  u32x4 ONES4 = {ONES, ONES, ONES, ONES};
  asm volatile("s_nop 1" : "+v"(ONES4));
  const u32 dw = lds0 + w * 1024;
  const u32 vbD = kn_voff(D_, w, lane);
  u32x4 hres, xres;
  u32x4 B[16][2];
  constexpr int CBD = 128 * D_, CB3 = 128 * F_;
  const int idx3 = bid >> 3;
  const bool has3 = idx3 < 28;
  const int pm3 = (bid & 7) * 2 + (idx3 >= 14 ? 1 : 0), pn3 = idx3 >= 14 ? idx3 - 14 : idx3;
  const u16* sb3 = p.w3 + (has3 ? pn3 * 128 : 0);
  const u32 vbF = kn_voff(F_, w, lane);
  const u32 vbF4 = kn_voff(F_, w - 4, lane);
  const int wm3 = w & 1, wn3 = w >> 1;
  const int ob0 = (kn_base(lane) + (wn3 >> 1) * 8192) ^ ((2 * (wn3 & 1)) << 5), ob1 = ob0 ^ 32;
  f32x4 cw0, cw1;
  u32x4 Bt[2][8];
  const char* pb0 = smem + GA_B + (kn_base(lane) ^ ((2 * wn) << 5));
  const char* pb1 = smem + GA_B + (kn_base(lane) ^ ((2 * wn + 1) << 5));
  auto ldB = [&](int j) {
#pragma unroll
    for (int i = 0; i < 4; i++) { Bt[j & 1][2 * i] = kn_frag(pb0 + (4 * j + i) * 4096); Bt[j & 1][2 * i + 1] = kn_frag(pb1 + (4 * j + i) * 4096); }
    SCHED();
  };
  auto ldB3 = [&](int s0, int n, int o) {
#pragma unroll
    for (int s = 0; s < 8; s++) if (s < n) {
      B[s0 + s][0] = kn_frag(smem + o + (s >> 1) * 16384 + (s & 1) * 4096 + ob0);
      B[s0 + s][1] = kn_frag(smem + o + (s >> 1) * 16384 + (s & 1) * 4096 + ob1);
    }
#pragma unroll
    for (int s = 0; s < 8; s++) if (s < n) { mmaz(s0 + s == 0, cw0, B[s0 + s][0], ONES4); mmaz(s0 + s == 0, cw1, B[s0 + s][1], ONES4); }
  };
#define W2C(c) glds<GA_B + (c) * 8192, (c) * CBD>(vbD, p.w2 + col0, dw);
#define W3C(c, dst) glds<(dst), (c) * CB3>(vbF, sb3, dw); glds<(dst) + 8192, (c) * CB3 + 128>(vbF, sb3, dw);
#define W3C4(c, dst) W3C(c, dst) glds<(dst), (c) * CB3>(vbF4, sb3, dw - 4096); glds<(dst) + 8192, (c) * CB3 + 128>(vbF4, sb3, dw - 4096);
  {
    f32x4 cv0, cv1;
    {
#pragma unroll
      for (int i = 0; i < 4; i++) { Bt[0][2 * i] = kn_frag(pb0 + (4 * mt + i) * 4096); Bt[0][2 * i + 1] = kn_frag(pb1 + (4 * mt + i) * 4096); }
#pragma unroll
      for (int i = 0; i < 4; i++) { mmaz(i == 0, cv0, Bt[0][2 * i], ONES4); mmaz(i == 0, cv1, Bt[0][2 * i + 1], ONES4); }
      mfence(); pin(cv0); pin(cv1);
      char* ct = smem + 65536 + wn * 128 + g * 16;
      if (r16 == 0) { *(f32x4*)(ct + mt * 256) = cv0; *(f32x4*)(ct + mt * 256 + 64) = cv1; }
      lds_bar();
      cv0 = (*(const f32x4*)(ct) + *(const f32x4*)(ct + 256)) + (*(const f32x4*)(ct + 512) + *(const f32x4*)(ct + 768));
      cv1 = (*(const f32x4*)(ct + 64) + *(const f32x4*)(ct + 320)) + (*(const f32x4*)(ct + 576) + *(const f32x4*)(ct + 832));
    }
    wait_vm_lgkm<0>(); bar();
    if (tid < 16) st8_dev(wsr, FL_2 + tid * 2048 + bid * 8, tag);
    STAMP(9);
    if (w >= 4) { W3C4(0, 65536) W3C4(1, 81920) }
    ldB(0); ldB(1);
    if (w == 0) poll256(wsr, FL_2 + (bid & 15) * 2048, tag, lane);
    bar();
    STAMP(10);
    {
      const char* sa = p.ws + WS_HT + rt * 64 + (size_t)wn * 262144;
      const int j2 = lane ^ (((lane >> 4) & 1) << 3);
      const u32 vo = (u32)((j2 >> 2) * 8192 + (j2 & 3) * 16);
      const u32 d = lds0 + GA_A + mt * 16384 + wn * 2048;
      glds<0, 0>(vo, sa, d); glds<1024, 131072>(vo, sa, d); glds<4096, 4 * 131072>(vo, sa, d); glds<5120, 5 * 131072>(vo, sa, d);
      glds<8192, 8 * 131072>(vo, sa, d); glds<9216, 9 * 131072>(vo, sa, d); glds<12288, 12 * 131072>(vo, sa, d); glds<13312, 13 * 131072>(vo, sa, d);
    }
    const char* aw = smem + GA_A + mt * 16384;
    const int ab = g * 256 + ((g & 1) << 7) + ((lane & 15) >> 2) * 32 + (lane & 3) * 8;
    const char* pa0 = aw + ab;
    const char* pa1 = aw + (ab ^ 128);
    f32x4 acc0, acc1, sacc, gacc;
    u32x4 a[2][4];
    auto ld = [&](int j) {
#pragma unroll
      for (int i = 0; i < 4; i++) {
        u32x2 x0 = tr_read(pa0 + (4 * j + i) * 1024), x1 = tr_read(pa1 + (4 * j + i) * 1024);
        a[j & 1][i] = u32x4{x0[0], x0[1], x1[0], x1[1]};
      }
      SCHED();
    };
    auto mm = [&](int j) {
#pragma unroll
      for (int i = 0; i < 4; i++) {
        mmaz(j + i == 0, sacc, ONES4, a[j & 1][i]);
        mmaz(j + i == 0, gacc, a[j & 1][i], a[j & 1][i]);
        mmaz(j + i == 0, acc0, Bt[j & 1][2 * i], a[j & 1][i]);
        mmaz(j + i == 0, acc1, Bt[j & 1][2 * i + 1], a[j & 1][i]);
      }
      SCHED();
    };
    SCHED();
    wait_vm<6>(); bar(); ld(0);
    STAMP(23);
    wait_vm<4>(); bar(); ld(1); mm(0); ldB(2);
    wait_vm<2>(); bar(); ld(2); mm(1); ldB(3);
    wait_vm_lgkm<0>(); bar(); ld(3);
    W2C(0) W2C(1)
    STAMP(24);
    const int ab2 = pc4 * 256 + ((pc4 & 1) << 7) + ((lane & 15) >> 2) * 32 + (lane & 3) * 8;
    const char* hs = aw + (2 * pn + wn) * 1024;
    u32x2 hx0 = tr_read(hs + ab2), hx1 = tr_read(hs + (ab2 ^ 128));
    SCHED(); mm(2); W2C(2) W2C(3) mm(3);
    W2C(4) W2C(5)
    mfence(); pin(acc0); pin(acc1); pin(sacc); pin(gacc);
    STAMP(11);
    float dg = (r16 & 2) ? ((r16 & 1) ? gacc[3] : gacc[2]) : ((r16 & 1) ? gacc[1] : gacc[0]);
    f32x2 sa2 = {sacc[0], gsum(g == (r16 >> 2) ? dg : 0.f)};
    float mean1 = sa2[0] * (1.0f / D_);
    float var1 = fmaxf(sa2[1] * (1.0f / D_) - mean1 * mean1, 0.f);
    float m1 = rbf(mean1);
    float sd1 = rbf(__builtin_amdgcn_sqrtf(rbf(rbf(var1) + 1e-5f)));
    float i1 = __builtin_amdgcn_rcpf(sd1);
    float m2 = rbf((mean1 - m1) * i1);
    float sd2 = rbf(__builtin_amdgcn_sqrtf(rbf(rbf(var1 * i1 * i1) + 1e-5f)));
    float rs = i1 * __builtin_amdgcn_rcpf(sd2), tt = -(m1 + m2 * sd1) * rs;
    {
      f32x4 rt4 = {rs, rs, tt, tt};
      pin(rt4);
      W2C(6) W2C(7)
      u32x2 o0 = act4(acc0, cv0, rt4), o1 = act4(acc1, cv1, rt4);
      st16_dev(wsr, WS_H1 + 16384 + boff, pair_swap(o0[0], o0[1], o1[0], o1[1]));
    }
    if (tid < 16) {
      const u32 o = (u32)((pn * 2 + (tid >> 3)) * 1024 + (tid & 7) * 16);
      if (pm == 0) st16_dev(wsr, WS_H1 + 14 * 64 + o, u32x4{0, 0, 0, 0});
      if (pm == 31) st16_dev(wsr, WS_H1 + 129 * 16384 + o, u32x4{0, 0, 0, 0});
    }
    hres = u32x4{hx0[0], hx0[1], hx1[0], hx1[1]};
#pragma unroll
    for (int i = 0; i < 4; i++) xres[i] = pkbf2(bfup(pkbf2(bfup(hres[i]) - m1)) * i1);
    wait_vm_lgkm<0>(); bar();
    if (tid < 8) st8_dev(wsr, FL_3 + tid * 2048 + (pm * 8 + pn) * 8, tag);
    ldB3(0, 4, 65536);
    ldB(0); ldB(1);
  }
  STAMP(12);
  const int ar = lane >> 3, asp = (lane & 7) ^ ar;
  const u32 apo = (u32)((asp >> 2) * 1024 + (asp & 3) * 16);
  {
    u32 vo[8];
#pragma unroll
    for (int k = 0; k < 8; k++) {
      const int sh = (k >> 1) == 0 ? -1 : (k >> 1) == 1 ? 1 : (k >> 1) == 2 ? -2 : 2;
      const int row = 16 + ar + (k & 1) * 8 + sh;
      vo[k] = (u32)((row >> 4) * 16384 + (row & 15) * 64) + apo;
    }
    if (w == 0) polln(wsr, FL_3 + pn * 2048, max((pm & ~1) - 1, 0) * 8, min((pm & ~1) + 3, 32) * 8, tag, lane);
    lds_bar();
    STAMP(13);
    {
      const char* sa = p.ws + WS_H1 + (size_t)rt * 16384 + wn * 2048;
      const u32 d = lds0 + GA_A + mt * 16384 + wn * 2048;
      glds<0, 0>(vo[0], sa, d); glds<1024, 0>(vo[1], sa, d); glds<4096, 4096>(vo[2], sa, d); glds<5120, 4096>(vo[3], sa, d);
      glds<8192, 8192>(vo[4], sa, d); glds<9216, 8192>(vo[5], sa, d); glds<12288, 12288>(vo[6], sa, d); glds<13312, 12288>(vo[7], sa, d);
    }

    const char* pa0 = smem + GA_A + mt * 16384 + mk_base(lane, 0);
    const char* pa1 = smem + GA_A + mt * 16384 + mk_base(lane, 1);
    f32x4 acc0, acc1;
    u32x4 a[2][4];
    auto ld = [&](int j) {
      a[j & 1][0] = *(const u32x4*)(pa0 + j * 4096); a[j & 1][1] = *(const u32x4*)(pa1 + j * 4096);
      a[j & 1][2] = *(const u32x4*)(pa0 + j * 4096 + 2048); a[j & 1][3] = *(const u32x4*)(pa1 + j * 4096 + 2048);
      SCHED();
    };
    auto mm = [&](int j) {
#pragma unroll
      for (int i = 0; i < 4; i++) {
        mmaz(j + i == 0, acc0, Bt[j & 1][2 * i], a[j & 1][i]);
        mmaz(j + i == 0, acc1, Bt[j & 1][2 * i + 1], a[j & 1][i]);
      }
      SCHED();
    };
    SCHED();
    wait_vm<6>(); bar(); ld(0);
    STAMP(25);
    wait_vm<4>(); bar(); ld(1); mm(0); ldB(2);
    wait_vm<2>(); bar(); ld(2); mm(1); ldB(3);
    wait_vm_lgkm<0>(); bar(); ld(3);
    W3C(2, 65536)
    STAMP(26);
    mm(2); W3C(3, 81920) mm(3);
    W3C(4, GA_B)
    mfence(); pin(acc0); pin(acc1);
    STAMP(14);
    u32x4 v = pair_swap(pkbf(acc0[0], acc0[1]), pkbf(acc0[2], acc0[3]), pkbf(acc1[0], acc1[1]), pkbf(acc1[2], acc1[3]));
#pragma unroll
    for (int i = 0; i < 4; i++) xres[i] = pkbf2(bfup(xres[i]) + bfup(v[i]));
    st16_dev(wsr, WS_X1 + boff, xres);
    float s1 = 0, s2 = 0;
#pragma unroll
    for (int i = 0; i < 4; i++) { s1 = dot2(xres[i], ONES, s1); s2 = dot2(xres[i], xres[i], s2); }
    s1 = gsum(s1); s2 = gsum(s2);
    if (g == 0) st8_dev(wsr, WS_ST + (rt * 16 + pn * 2 + wn) * 128 + r16 * 8, u32x2{__float_as_uint(s1), __float_as_uint(s2)});
    W3C(5, GA_B + 16384) W3C(6, GA_B + 32768)
    wait_vm_lgkm<4>(); bar();
    if (tid < 8) st8_dev(wsr, FL_4 + tid * 2048 + (pm * 8 + pn) * 8, tag);
    W3C(7, GA_B + 49152)
  }
  STAMP(15);
  const u16* sb4 = p.w4 + col0;
#define G4_R1A(i) glds<GA_B + (i) * 8192, (((i) >> 1) * 7 + ((i) & 1)) * CBD>(vbD, sb4, dw);
#define G4_R1A4(i) G4_R1A(i) glds<GA_B + (i) * 8192, (((i) >> 1) * 7 + ((i) & 1)) * CBD>(vbD4, sb4, dw - 4096);
#define G4_R1A_ALL4 G4_R1A4(0) G4_R1A4(1) G4_R1A4(2) G4_R1A4(3) G4_R1A4(4) G4_R1A4(5) G4_R1A4(6) G4_R1A4(7)
  const u32 vbD4 = kn_voff(D_, w - 4, lane);
#define G4_R1B(i) glds<(i) * 8192, (((i) >> 1) * 7 + 2 + ((i) & 1)) * CBD>(vbD, sb4, dw);
#define G4_R2POS(i) ((i) < 4 ? 32768 + (i) * 8192 : GA_B + ((i) - 4) * 8192)
#define G4_R2(i) glds<G4_R2POS(i), (((i) / 3) * 7 + 4 + ((i) % 3)) * CBD>(vbD, sb4, dw);
  if (has3) {
    const int wm = wm3;
    ldB3(4, 4, 65536);
    ldB3(8, 2, GA_B);
    wait_vm<0>(); bar();
    ldB3(10, 6, GA_B + 16384);
    if (w == 0) polln(wsr, FL_4 + pn * 2048, pm3 * 16, pm3 * 16 + 16, tag, lane);
    lds_bar();
    STAMP(16);
    const char* sa = p.ws + WS_X1 + (size_t)(pm3 * 8 + (w >> 1)) * 16384 + (w & 1) * 512;
    const u32 va = (u32)(ar * 64) + apo;
#define G3_A(i) glds<(((i) + 4) % 9) * 8192, ((i) >> 3) * 65536 + ((i) & 7) * 2048>(va, sa, dw);
    {
      const char* ss = p.ws + WS_ST + (size_t)pm3 * 16384 + w * 1024;
      glds<G3_ST, 0>(lane * 16, ss, dw); glds<G3_ST + 8192, 8192>(lane * 16, ss, dw);
    }
    G3_A(0) G3_A(1) G3_A(2) G3_A(3) G3_A(4) G3_A(5) G3_A(6) G3_A(7) G3_A(8)
    wait_vm<9>(); bar();
    {
      const char* sst = smem + G3_ST + (tid >> 6) * 2048 + (tid & 3) * 512 + ((tid >> 2) & 15) * 8;
      f32x2 st = (*(const f32x2*)(sst) + *(const f32x2*)(sst + 128)) + (*(const f32x2*)(sst + 256) + *(const f32x2*)(sst + 384));
      float t1 = st[0] + dppf<0xB1>(st[0]), t2 = st[1] + dppf<0xB1>(st[1]);
      t1 += dppf<0x4E>(t1); t2 += dppf<0x4E>(t2);
      float mean = t1 * (1.0f / D_);
      float var = fmaxf(t2 * (1.0f / D_) - mean * mean, 0.f);
      float rs = __builtin_amdgcn_rcpf(rbf(__builtin_amdgcn_sqrtf(rbf(rbf(var) + 1e-5f))));
      if ((tid & 3) == 0) { float tt = -rbf(mean) * rs; *(f32x4*)(smem + G3_TAB + (tid >> 2) * 16) = f32x4{rs, rs, tt, tt}; }
    }
    const f32x4& cv0 = cw0;
    const f32x4& cv1 = cw1;
    float* rowt = (float*)(smem + G3_TAB);
    const int oa0 = mk_base(lane, 0) + wm * 4096, oa1 = mk_base(lane, 1) + wm * 4096;
    f32x4 acc[2][2][2];
    u32x4 a[2][4];
    auto ld = [&](int slot, u32x4 (&d)[4]) {
      const char* s = smem + slot * 8192;
      d[0] = *(const u32x4*)(s + oa0); d[1] = *(const u32x4*)(s + oa0 + 2048);
      d[2] = *(const u32x4*)(s + oa1); d[3] = *(const u32x4*)(s + oa1 + 2048);
      SCHED();
    };
    auto m1 = [&](int ps, int b, int i, const u32x4 (&d)[4]) {
      mmaz(b == 0 && i < 4, acc[ps][(i >> 1) & 1][i & 1], B[2 * b + (i >> 2)][(i >> 1) & 1], d[2 * (i >> 2) + (i & 1)]);
      SCHED();
    };
    auto mm = [&](int ps, int b, const u32x4 (&d)[4]) {
#pragma unroll
      for (int i = 0; i < 8; i++) m1(ps, b, i, d);
    };
    const u32 hoff = WS_HID + (u32)(((pm3 * 8 + 2 * wm) * 56 + pn3 * 4 + wn3) * 1024 + r16 * 64 + pc4 * 16);
    u32x2 o0;
    f32x4 rtv[2];
    auto ldrt = [&](int ps) {
      rtv[0] = *(const f32x4*)(rowt + (64 * ps + 32 * wm + r16) * 4);
      rtv[1] = *(const f32x4*)(rowt + (64 * ps + 32 * wm + 16 + r16) * 4);
    };
    auto ep = [&](int ps, int i) {
      const int mi = i >> 1;
      if ((i & 1) == 0) o0 = act4(acc[ps][0][mi], cv0, rtv[mi]);
      else {
        u32x2 o1 = act4(acc[ps][1][mi], cv1, rtv[mi]);
        st16_dev(wsr, hoff + (4 * ps + mi) * 57344, pair_swap(o0[0], o0[1], o1[0], o1[1]));
      }
    };
    float c0v = -2.0f * 0.7978845608028654f * 1.4426950408889634f;
    asm volatile("" : "+v"(c0v));
    auto mme = [&](int b, const u32x4 (&d)[4], int e) {
      const float c1 = -2.0f * 0.7978845608028654f * 1.4426950408889634f * 0.044715f;
      const int mi = e >> 1;
      const f32x4 aa = acc[0][e & 1][mi], cc = (e & 1) ? cv1 : cv0;
      float v[4], x[4], z[4];
      m1(1, b, 0, d);
#pragma unroll
      for (int q = 0; q < 4; q++) v[q] = smul(aa[q], rtv[mi][0]);
      SCHED(); m1(1, b, 1, d);
#pragma unroll
      for (int q = 0; q < 4; q++) v[q] = sfma(cc[q], rtv[mi][2], v[q]);
      u32 p0 = pkbf(v[0], v[1]), p1 = pkbf(v[2], v[3]);
      SCHED(); m1(1, b, 2, d);
      x[0] = bflo(p0); x[1] = bfhi(p0); x[2] = bflo(p1); x[3] = bfhi(p1);
      z[0] = smul(x[0], x[0]); z[1] = smul(x[1], x[1]);
      SCHED(); m1(1, b, 3, d);
      z[2] = smul(x[2], x[2]); z[3] = smul(x[3], x[3]);
#pragma unroll
      for (int q = 0; q < 4; q++) z[q] = sfmak(z[q], c1, c0v);
      SCHED(); m1(1, b, 4, d);
#pragma unroll
      for (int q = 0; q < 4; q++) z[q] = smul(z[q], x[q]);
      z[0] = __builtin_amdgcn_exp2f(z[0]); z[1] = __builtin_amdgcn_exp2f(z[1]);
      SCHED(); m1(1, b, 5, d);
      z[2] = __builtin_amdgcn_exp2f(z[2]); z[3] = __builtin_amdgcn_exp2f(z[3]);
#pragma unroll
      for (int q = 0; q < 4; q++) z[q] = sadd1(z[q]);
      SCHED(); m1(1, b, 6, d);
#pragma unroll
      for (int q = 0; q < 4; q++) z[q] = __builtin_amdgcn_rcpf(z[q]);
      SCHED(); m1(1, b, 7, d);
#pragma unroll
      for (int q = 0; q < 4; q++) x[q] = smul(x[q], z[q]);
      u32x2 o = {pkbf(x[0], x[1]), pkbf(x[2], x[3])};
      if (e & 1) st16_dev(wsr, hoff + mi * 57344, pair_swap(o0[0], o0[1], o[0], o[1])); else o0 = o;
      SCHED();
    };
    SCHED();
#define SL(i) (((i) + 4) % 9)
    wait_vm_lgkm<8>(); bar(); ld(SL(0), a[0]); G4_R1A(0)
    wait_vm_lgkm<8>(); bar(); ld(SL(1), a[1]); G4_R1A(1) mm(0, 0, a[0]); SCHED();
    wait_vm_lgkm<8>(); bar(); G3_A(9) ld(SL(2), a[0]); G4_R1A(2) mm(0, 1, a[1]); SCHED();
    wait_vm_lgkm<9>(); bar(); G3_A(10) ld(SL(3), a[1]); G4_R1A(3) mm(0, 2, a[0]); SCHED();
    wait_vm_lgkm<10>(); bar(); G3_A(11) ld(SL(4), a[0]); G4_R1A(4) mm(0, 3, a[1]); SCHED();
    wait_vm_lgkm<11>(); bar(); G3_A(12) ld(SL(5), a[1]); G4_R1A(5) mm(0, 4, a[0]); SCHED();
    wait_vm_lgkm<12>(); bar(); G3_A(13) ld(SL(6), a[0]); G4_R1A(6) mm(0, 5, a[1]); SCHED();
    wait_vm_lgkm<13>(); bar(); G3_A(14) ld(SL(7), a[1]); G4_R1A(7) mm(0, 6, a[0]); SCHED();
    wait_vm_lgkm<14>(); bar(); G3_A(15) ld(SL(8), a[0]); ldrt(0); mm(0, 7, a[1]); SCHED();
    STAMP(17);
    wait_vm_lgkm<12>(); bar(); ld(SL(9), a[1]); G4_R1B(2) mme(0, a[0], 0);
    wait_vm_lgkm<11>(); bar(); ld(SL(10), a[0]); G4_R1B(3) mme(1, a[1], 1);
    wait_vm_lgkm<11>(); bar(); ld(SL(11), a[1]); G4_R1B(4) mme(2, a[0], 2);
    wait_vm_lgkm<10>(); bar(); ld(SL(12), a[0]); G4_R1B(5) mme(3, a[1], 3);
    wait_vm_lgkm<10>(); bar(); ld(SL(13), a[1]); G4_R1B(6) mm(1, 4, a[0]); SCHED();
    wait_vm_lgkm<9>(); bar(); ld(SL(14), a[0]); G4_R1B(7) mm(1, 5, a[1]); SCHED();
    wait_vm_lgkm<1>(); bar();
    if (tid < 8) st8_dev(wsr, FL_5 + tid * 2048 + (pm3 * 14 + pn3) * 8, tag);
    ld(SL(15), a[1]); ldrt(1); mm(1, 6, a[0]); SCHED();
    lds_bar();
    G4_R1B(0) G4_R1B(1)
    mm(1, 7, a[1]);
    mfence();
#pragma unroll
    for (int i = 0; i < 4; i++) pin(acc[1][i >> 1][i & 1]);
    STAMP(18);
#pragma unroll
    for (int i = 0; i < 4; i++) ep(1, i);
    if (w == 0) polln(wsr, FL_5 + pn * 2048, (pm >> 1) * 14, (pm >> 1) * 14 + 14, tag, lane);
    wait_vm_lgkm<0>(); bar();
    if (tid < 8) st8_dev(wsr, FL_6 + tid * 2048 + (pm3 * 14 + pn3) * 8, tag);
  } else {
    wait_vm<0>(); bar();
    G4_R1A(0) G4_R1A(1) G4_R1A(2) G4_R1A(3) G4_R1A(4) G4_R1A(5) G4_R1A(6) G4_R1A(7)
    G4_R1B(0) G4_R1B(1) G4_R1B(2) G4_R1B(3) G4_R1B(4) G4_R1B(5) G4_R1B(6) G4_R1B(7)
    if (w == 0) polln(wsr, FL_5 + pn * 2048, (pm >> 1) * 14, (pm >> 1) * 14 + 14, tag, lane);
    wait_vm<0>(); bar();
  }
  STAMP(19);
  {
    const int wm = mt >> 1, kq = (mt & 1) + 2 * wn;
    const int kb = kn_base(lane);
    u32x4(&B4)[8][4] = *reinterpret_cast<u32x4(*)[8][4]>(&B);
    const char* sa = p.ws + WS_HID + (size_t)((rt & ~1) * 56 + 14 * kq) * 1024;
    const u32 va = (u32)(ar * 64) + apo;
    const int ring = (wm ? 0 : 65536) + kq * 8192;
    const u32 da = lds0 + ring;
#define G4_A4(b) glds<((b) & 1) * 4096, (b) * 2048>(va, sa, da); glds<((b) & 1) * 4096 + 1024, (b) * 2048 + 512>(va, sa, da); \
                 glds<((b) & 1) * 4096 + 2048, (b) * 2048 + 57344>(va, sa, da); glds<((b) & 1) * 4096 + 3072, (b) * 2048 + 57344 + 512>(va, sa, da);
    if (wm == 0) { G4_A4(0) G4_A4(1) }
#pragma unroll
    for (int s = 0; s < 4; s++)
#pragma unroll
      for (int nt = 0; nt < 4; nt++) {
        B4[s][nt] = kn_frag(smem + GA_B + kq * 16384 + s * 4096 + (kb ^ (nt << 5)));
        B4[4 + s][nt] = kn_frag(smem + kq * 16384 + s * 4096 + (kb ^ (nt << 5)));
      }
    lds_bar();
    if (wm) {
      const u32 vb2 = kn_voff(D_, w - 2, lane), dw2 = dw - 2048;
#define G4_R2X(i) G4_R2(i) glds<G4_R2POS(i), (((i) / 3) * 7 + 4 + ((i) % 3)) * CBD>(vb2, sb4, dw2);
      G4_R2X(0) G4_R2X(3) G4_R2X(6) G4_R2X(9) G4_R2X(1) G4_R2X(4) G4_R2X(7) G4_R2X(10) G4_R2X(2) G4_R2X(5) G4_R2X(8) G4_R2X(11)
      polln(wsr, FL_6 + (pn ^ kq) * 2048, (pm >> 1) * 14, (pm >> 1) * 14 + 14, tag, lane);
      G4_A4(0) G4_A4(1)
      wait_vm<8>(); bar();
    }
    STAMP(20);
    const char* pa0 = smem + ring + mk_base(lane, 0);
    const char* pa1 = smem + ring + mk_base(lane, 1);
    f32x4 acc[4][2];
    u32x4 a[4];
    auto ld = [&](int b) {
      a[0] = *(const u32x4*)(pa0 + (b & 1) * 4096); a[1] = *(const u32x4*)(pa0 + (b & 1) * 4096 + 2048);
      a[2] = *(const u32x4*)(pa1 + (b & 1) * 4096); a[3] = *(const u32x4*)(pa1 + (b & 1) * 4096 + 2048);
      asm volatile("s_waitcnt lgkmcnt(0)" ::: "memory");
      SCHED();
    };
    auto mm = [&](int s0, bool first = false) {
#pragma unroll
      for (int ks = 0; ks < 2; ks++)
#pragma unroll
        for (int nt = 0; nt < 4; nt++)
#pragma unroll
          for (int mi = 0; mi < 2; mi++) mmaz(first && ks == 0, acc[nt][mi], B4[s0 + ks][nt], a[2 * ks + mi]);
      SCHED();
    };
    auto ldb = [&](int j, int s0) {
      const int i = kq * 3 + j;
      const char* bl = smem + (i < 4 ? 32768 + i * 8192 : GA_B + (i - 4) * 8192);
#pragma unroll
      for (int ks = 0; ks < 2; ks++)
#pragma unroll
        for (int nt = 0; nt < 4; nt++) B4[s0 + ks][nt] = kn_frag(bl + ks * 4096 + (kb ^ (nt << 5)));
      SCHED();
    };
    wait_vm<4>(); ld(0); G4_A4(2) mm(0, true);
    wait_vm<4>(); ld(1); G4_A4(3) mm(2);
    wait_vm<4>(); ld(2); G4_A4(4) mm(4);
    wait_vm<4>(); ld(3); G4_A4(5) mm(6);
    if (wm == 0) bar();
    ldb(0, 0); ldb(1, 2);
    wait_vm<4>(); ld(4); G4_A4(6) ldb(2, 4); mm(0);
    wait_vm<4>(); ld(5); mm(2);
    wait_vm<0>(); ld(6); mm(4);
    mfence();
#pragma unroll
    for (int i = 0; i < 8; i++) pin(acc[i >> 1][i & 1]);
    STAMP(21);
    {
      char* d = smem + ring + lane * 16;
#pragma unroll
      for (int k2 = 0; k2 < 4; k2++) {
        *(f32x4*)(d + k2 * 2048) = acc[2 * (k2 >> 1)][k2 & 1];
        *(f32x4*)(d + k2 * 2048 + 1024) = acc[2 * (k2 >> 1) + 1][k2 & 1];
      }
    }
    lds_bar();
    const char* s = smem + (wm ? 0 : 65536) + kq * 2048 + lane * 16;
    f32x4 f0 = (*(const f32x4*)(s) + *(const f32x4*)(s + 8192)) + (*(const f32x4*)(s + 16384) + *(const f32x4*)(s + 24576));
    f32x4 f1 = (*(const f32x4*)(s + 1024) + *(const f32x4*)(s + 9216)) + (*(const f32x4*)(s + 17408) + *(const f32x4*)(s + 25600));
    u32x4 v = pair_swap(pkbf(f0[0], f0[1]), pkbf(f0[2], f0[3]), pkbf(f1[0], f1[1]), pkbf(f1[2], f1[3]));
    u32x4 o;
#pragma unroll
    for (int i = 0; i < 4; i++) o[i] = pkbf2(bfup(hres[i]) + bfup(pkbf2(bfup(xres[i]) + bfup(v[i]))));
    *(u32x4*)(p.out + (size_t)(rt * 16 + r16) * D_ + col0 + 32 * wn + pc4 * 8) = o;
  }
  STAMP(22);
  asm volatile(".Lk_end:");
}

extern "C" void kernel_launch(void* const* d_in, const int* in_sizes, int n_in, void* d_out, int out_size, void* d_ws,
                              size_t ws_size, hipStream_t stream) {
  Params p;
  p.x = (const u16*)d_in[0]; p.are = (const u16*)d_in[1]; p.aim = (const u16*)d_in[2]; p.bre = (const u16*)d_in[3];
  p.bim = (const u16*)d_in[4]; p.cre = (const u16*)d_in[5]; p.cim = (const u16*)d_in[6]; p.logdt = (const u16*)d_in[7];
  p.dskip = (const u16*)d_in[8]; p.w1 = (const u16*)d_in[10]; p.w2 = (const u16*)d_in[11]; p.w3 = (const u16*)d_in[12];
  p.w4 = (const u16*)d_in[13]; p.out = (u16*)d_out; p.ws = (char*)d_ws;
  k_fused<<<256, 512, 0, stream>>>(p);
}
```

```cpp
#include <hip/hip_runtime.h>
#include <stdint.h>

typedef unsigned short u16;
typedef unsigned int u32;
typedef unsigned long long u64;
typedef __bf16 bf16x2 __attribute__((ext_vector_type(2)));
typedef __bf16 bf16x8 __attribute__((ext_vector_type(8)));
typedef _Float16 f16x2 __attribute__((ext_vector_type(2)));
typedef _Float16 f16x8 __attribute__((ext_vector_type(8)));
typedef float f32x2 __attribute__((ext_vector_type(2)));
typedef float f32x4 __attribute__((ext_vector_type(4)));
typedef short s16x4 __attribute__((ext_vector_type(4)));
typedef u32 u32x2 __attribute__((ext_vector_type(2)));
typedef u32 u32x4 __attribute__((ext_vector_type(4)));
#define LDSP __attribute__((address_space(3)))
#define DEV __device__ __forceinline__

constexpr int L_ = 2048, D_ = 512, NS_ = 128, F_ = 1792;
constexpr u32 WS_U8 = 0;
constexpr u32 WS_HT = 7u << 20;
constexpr u32 WS_H1 = 13u << 20;
constexpr u32 WS_X1 = 16u << 20;
constexpr u32 WS_ST = 18u << 20;
constexpr u32 WS_HID = 19u << 20;
constexpr u32 WS_FL = 28u << 20;
constexpr u32 FL_EPOCH = WS_FL, FL_1 = WS_FL + 4096, FL_2 = FL_1 + 32768, FL_3 = FL_2 + 32768, FL_4 = FL_3 + 16384, FL_5 = FL_4 + 16384, FL_6 = FL_5 + 16384;

struct Params {
  const u16 *x, *are, *aim, *bre, *bim, *cre, *cim, *logdt, *dskip, *w1, *w2, *w3, *w4;
  u16* out;
  char* ws;
};

DEV float bf2f(u32 v) { return __uint_as_float(v << 16); }
DEV float bflo(u32 v) { return __uint_as_float(v << 16); }
DEV float bfhi(u32 v) { return __uint_as_float(v & 0xffff0000u); }
DEV f32x2 bfup(u32 v) { return f32x2{bflo(v), bfhi(v)}; }
DEV u32 pkbf2(f32x2 v) { return __builtin_bit_cast(u32, __builtin_convertvector(v, bf16x2)); }
DEV u32 pkbf(float a, float b) { return pkbf2(f32x2{a, b}); }
DEV u32 pkh(float a, float b) { f32x2 v = {a, b}; return __builtin_bit_cast(u32, __builtin_convertvector(v, f16x2)); }
DEV float rbf(float a) { return bflo(pkbf(a, a)); }
DEV f32x2 hup(u32 v) { return __builtin_convertvector(__builtin_bit_cast(f16x2, v), f32x2); }

template <int CTRL> DEV float dppf(float v) {
  return __builtin_bit_cast(float, __builtin_amdgcn_update_dpp(0, __builtin_bit_cast(int, v), CTRL, 0xf, 0xf, true));
}
DEV float gsum(float v) {
  u32 a = __float_as_uint(v);
  auto r = __builtin_amdgcn_permlane16_swap(a, a, false, false);
  v = __uint_as_float(r[0]) + __uint_as_float(r[1]);
  a = __float_as_uint(v);
  auto q = __builtin_amdgcn_permlane32_swap(a, a, false, false);
  return __uint_as_float(q[0]) + __uint_as_float(q[1]);
}
DEV float gmax(float v) {
  u32 a = __float_as_uint(v);
  auto r = __builtin_amdgcn_permlane16_swap(a, a, false, false);
  v = fmaxf(__uint_as_float(r[0]), __uint_as_float(r[1]));
  a = __float_as_uint(v);
  auto q = __builtin_amdgcn_permlane32_swap(a, a, false, false);
  return fmaxf(__uint_as_float(q[0]), __uint_as_float(q[1]));
}
DEV float wave_sum(float v) {
  v += dppf<0xB1>(v); v += dppf<0x4E>(v); v += dppf<0x141>(v); v += dppf<0x140>(v);
  return gsum(v);
}
DEV float wave_max(float v) {
  v = fmaxf(v, dppf<0xB1>(v)); v = fmaxf(v, dppf<0x4E>(v)); v = fmaxf(v, dppf<0x141>(v)); v = fmaxf(v, dppf<0x140>(v));
  return gmax(v);
}
DEV u32x2 tr_read(const char* p) {
  return __builtin_bit_cast(u32x2, __builtin_amdgcn_ds_read_tr16_b64_v4i16((LDSP s16x4*)p));
}
DEV void mma(f32x4& c, u32x4 a, u32x4 b) { asm volatile("v_mfma_f32_16x16x32_bf16 %0, %1, %2, %0" : "+v"(c) : "v"(a), "v"(b)); }
DEV void mma0(f32x4& c, u32x4 a, u32x4 b) { asm volatile("v_mfma_f32_16x16x32_bf16 %0, %1, %2, 0" : "=&v"(c) : "v"(a), "v"(b)); }
DEV void mmah(f32x4& c, u32x4 a, u32x4 b) { asm volatile("v_mfma_f32_16x16x32_f16 %0, %1, %2, %0" : "+v"(c) : "v"(a), "v"(b)); }
DEV void mmah0(f32x4& c, u32x4 a, u32x4 b) { asm volatile("v_mfma_f32_16x16x32_f16 %0, %1, %2, 0" : "=&v"(c) : "v"(a), "v"(b)); }
DEV void mmaz(bool first, f32x4& c, u32x4 a, u32x4 b) { if (first) mma0(c, a, b); else mma(c, a, b); }
DEV void mfence() { asm volatile("s_nop 7" ::: "memory"); __builtin_amdgcn_sched_barrier(0); }
DEV float smul(float a, float b) { float r; asm("v_mul_f32 %0, %1, %2" : "=v"(r) : "v"(a), "v"(b)); return r; }
DEV float sfma(float a, float b, float c) { float r; asm("v_fma_f32 %0, %1, %2, %3" : "=v"(r) : "v"(a), "v"(b), "v"(c)); return r; }
DEV float sfmak(float a, float k1, float k0) { float r; asm("v_fma_f32 %0, %1, %2, %3" : "=v"(r) : "v"(a), "s"(k1), "v"(k0)); return r; }
DEV float sadd1(float a) { float r; asm("v_add_f32 %0, 1.0, %1" : "=v"(r) : "v"(a)); return r; }
DEV void pin(f32x4& c) { asm volatile("" : "+v"(c)); }
DEV float dot2(u32 a, u32 b, float c) {
  return __builtin_amdgcn_fdot2_f32_bf16(__builtin_bit_cast(bf16x2, a), __builtin_bit_cast(bf16x2, b), c, false);
}
template <int LOFF, int MOFF> DEV void glds(u32 voff, const void* sbase, u32 lds) {
  asm volatile("s_add_u32 m0, %1, %3\n\ts_nop 0\n\tglobal_load_lds_dwordx4 %0, %2"
               :: "v"(voff + (u32)MOFF), "s"(lds), "s"(sbase), "n"(LOFF) : "memory", "m0", "scc");
}
template <int LOFF, int MOFF> DEV void gldsd(u32 voff, const void* sbase, u32 lds) {
  asm volatile("s_add_u32 m0, %1, %3\n\ts_nop 0\n\tglobal_load_lds_dwordx4 %0, %2 sc1"
               :: "v"(voff + (u32)MOFF), "s"(lds), "s"(sbase), "n"(LOFF) : "memory", "m0", "scc");
}
DEV void touch(u32& t, const void* sbase, u32 voff) {
  asm volatile("global_load_dword %0, %1, %2" : "+v"(t) : "v"(voff), "s"(sbase) : "memory");
}
DEV void keep(u32 t) { asm volatile("" :: "v"(t)); }
DEV u32 lds_addr(const char* p) { return (u32)(size_t)(LDSP const char*)p; }
#define SCHED() __builtin_amdgcn_sched_barrier(0)
template <int N> DEV void wait_vm() { asm volatile("s_waitcnt vmcnt(%0)" ::"n"(N) : "memory"); }
template <int N> DEV void wait_vm_lgkm() { asm volatile("s_waitcnt vmcnt(%0) lgkmcnt(0)" ::"n"(N) : "memory"); }
DEV void bar() { __builtin_amdgcn_s_barrier(); }
DEV void lds_bar() { asm volatile("s_waitcnt lgkmcnt(0)" ::: "memory"); __builtin_amdgcn_s_barrier(); }

#define STAMP(i)

typedef __amdgpu_buffer_rsrc_t rsrc_t;
DEV void st16_dev(rsrc_t r, u32 off, u32x4 v) { __builtin_amdgcn_raw_buffer_store_b128(v, r, off, 0, 16); }
DEV void st8_dev(rsrc_t r, u32 off, u32x2 v) { __builtin_amdgcn_raw_buffer_store_b64(v, r, off, 0, 16); }
DEV u32x4 ld16_dev(rsrc_t r, u32 off) { return __builtin_amdgcn_raw_buffer_load_b128(r, off, 0, 16); }
DEV u32x2 ld8_dev(rsrc_t r, u32 off) { return __builtin_amdgcn_raw_buffer_load_b64(r, off, 0, 16); }

constexpr int SPIN_MAX = 1 << 18;
DEV void poll256(rsrc_t r, u32 base, u32x2 tag, int lane) {
#pragma unroll 1
  for (int s = 0; s < SPIN_MAX; s++) {
    u32x4 a = ld16_dev(r, base + lane * 32), b = ld16_dev(r, base + lane * 32 + 16);
    bool ok = a[0] == tag[0] && a[1] == tag[1] && a[2] == tag[0] && a[3] == tag[1] &&
              b[0] == tag[0] && b[1] == tag[1] && b[2] == tag[0] && b[3] == tag[1];
    if (__all(ok)) break;
    __builtin_amdgcn_s_sleep(2);
  }
}
DEV void polln(rsrc_t r, u32 base, int lo, int hi, u32x2 tag, int lane) {
  const int i = min(lo + lane, hi - 1);
#pragma unroll 1
  for (int s = 0; s < SPIN_MAX; s++) {
    u32x2 a = ld8_dev(r, base + i * 8);
    if (__all(a[0] == tag[0] && a[1] == tag[1])) break;
    __builtin_amdgcn_s_sleep(2);
  }
}

DEV u32x2 act4(f32x4 a, f32x4 cs, f32x4 rt) {
  const float c0 = -2.0f * 0.7978845608028654f * 1.4426950408889634f;
  const float c1 = c0 * 0.044715f;
  const f32x2 rs = {rt[0], rt[1]}, t = {rt[2], rt[3]};
  f32x2 v0 = f32x2{a[0], a[1]} * rs + f32x2{cs[0], cs[1]} * t, v1 = f32x2{a[2], a[3]} * rs + f32x2{cs[2], cs[3]} * t;
  f32x4 v = {v0[0], v0[1], v1[0], v1[1]};
  f32x4 x = v;
  f32x4 z = x * (x * x * c1 + c0);
  f32x4 e = {__builtin_amdgcn_exp2f(z[0]), __builtin_amdgcn_exp2f(z[1]), __builtin_amdgcn_exp2f(z[2]), __builtin_amdgcn_exp2f(z[3])};
  e += 1.0f;
  f32x4 r = {__builtin_amdgcn_rcpf(e[0]), __builtin_amdgcn_rcpf(e[1]), __builtin_amdgcn_rcpf(e[2]), __builtin_amdgcn_rcpf(e[3])};
  x *= r;
  return u32x2{pkbf(x[0], x[1]), pkbf(x[2], x[3])};
}

constexpr int LDS_ALL = 163840;
constexpr int KG_ROW = 528;
constexpr int KG_CH = 73 * KG_ROW;
constexpr int P0_KT = 77312;
constexpr int P0_LN = 101888;
constexpr int P0_MS = 119296;
constexpr int CV_COPY = 4160, CV_KP = 4 * CV_COPY, CV_CH = CV_KP + 4608;
constexpr int CV_UI = 2 * CV_CH;
constexpr int CV_RED = CV_UI + 16384;
constexpr int CV_STG = 0;
constexpr int GA_A = 0, GA_TAB = 65536, GA_B = 98304;
constexpr int G3_ST = 73728, G3_TAB = 90112;
constexpr int G4_B = 65536;

struct KTbl {
  u16 v[2048];
  constexpr KTbl() : v() {
    for (int pos = 0; pos < 2048; pos++) {
      int l = (pos & ~7) + 7 - (pos & 7);
      int sh = l < 256 ? 0 : l < 512 ? 1 : l < 1024 ? 2 : 3;
      int lv = sh ? (((l + (1 << (sh - 1)) - 1 + ((l >> sh) & 1)) >> sh) << sh) : l;
      int s = lv < 256 ? 0 : lv < 512 ? 1 : lv < 1024 ? 2 : 3;
      int rr = 8 * s + ((lv >> 7) << (3 - s)) + ((lv & 7) >> s);
      v[pos] = (u16)((rr * 16 + ((lv >> 3) & 15)) * 16);
    }
  }
};
__device__ __attribute__((aligned(16))) constexpr KTbl KTBL{};

struct cf { float x, y; };
DEV cf cmul(cf a, cf b) { return {a.x * b.x - a.y * b.y, a.x * b.y + a.y * b.x}; }
DEV cf cscale(cf a, float s) { return {a.x * s, a.y * s}; }
DEV cf cexpk(float tl2, float rh, float rl, float s) {
  float mag = __builtin_amdgcn_exp2f(tl2 * s);
  float p = rh * s;
  float f = __builtin_fmaf(rl, s, p - __builtin_rintf(p));
  return {mag * __builtin_amdgcn_cosf(f), mag * __builtin_amdgcn_sinf(f)};
}
DEV void st_x(char* p, cf a) { *(u32*)p = pkh(a.x, a.y); }
DEV void st_y(char* p, cf a) {
  u32 hi = pkh(a.x, a.y);
  f32x2 r = f32x2{a.x, a.y} - hup(hi);
  *(u32*)p = hi;
  *(u32*)(p + 16 * KG_ROW) = pkh(r[0], r[1]);
}
DEV float pow2_scale(float mx, u32 target, float& inv) {
  u32 eb = (__float_as_uint(mx) >> 23) & 0xffu;
  u32 se = (eb == 0 || eb == 255) ? 127u : min(254u + target - eb, 253u);
  inv = __uint_as_float((254u - se) << 23);
  return __uint_as_float(se << 23);
}

DEV void conv_load1(const char* pa, const char* pb, int b0, int i, u32x4 (&A)[8], u32x4 (&B)[8]) {
  u32x2 lo = *(const volatile LDSP u32x2*)(pa + 64 * b0 + 512 * i), hi = *(const volatile LDSP u32x2*)(pa + 64 * b0 + 512 * i + 8);
  A[i] = u32x4{lo[0], lo[1], hi[0], hi[1]};
  B[i] = *(const u32x4*)(pb - 64 * b0 + 512 * i);
}
DEV void conv_step(const char* pa, const char* pb, int nb, const u32x4 (&A)[8], const u32x4 (&B)[8], u32x4 (&An)[8], u32x4 (&Bn)[8], f32x4 (&acc)[8]) {
  u32x2 lo[8], hi[8];
  int c = 0;
#pragma unroll
  for (int i = 0; i < 8; i++) {
#pragma unroll
    for (int t = i; t < 8; t++) {
      if (nb == 1 && i == 0) mmah0(acc[t], A[i], B[t - i]); else mmah(acc[t], A[i], B[t - i]);
      if (nb < 4 && c < 24) {
        const int f = c / 3, k = c % 3;
        if (k == 0) Bn[f] = *(const u32x4*)(pb - 64 * nb + 512 * f);
        else if (k == 1) lo[f] = *(const volatile LDSP u32x2*)(pa + 64 * nb + 512 * f);
        else hi[f] = *(const volatile LDSP u32x2*)(pa + 64 * nb + 512 * f + 8);
      }
      c++;
      SCHED();
    }
  }
  if (nb < 4) {
#pragma unroll
    for (int f = 0; f < 8; f++) An[f] = u32x4{lo[f][0], lo[f][1], hi[f][0], hi[f][1]};
  }
}

DEV u32 kn_voff(int ld, int w, int lane) {
  int kr = lane >> 3, pos = lane & 7;
  int f2 = ((lane >> 4) & 1) | ((w & 1) << 1);
  return (u32)((8 * w + kr) * ld * 2 + ((pos ^ (f2 << 1)) << 4));
}
DEV u32 mk_voff(int ld, int lane) { int r = lane >> 3, pos = lane & 7; return (u32)(r * ld * 2 + ((pos ^ r) << 4)); }
DEV int kn_base(int lane) {
  int g = lane >> 4, q = (lane & 15) >> 2, pp = lane & 3;
  int f2 = ((q >> 1) & 1) | ((g & 1) << 1);
  return (8 * g + q) * 128 + (f2 << 5) + pp * 8;
}
DEV u32x4 kn_frag(const char* p) { u32x2 a = tr_read(p), b = tr_read(p + 512); return u32x4{a[0], a[1], b[0], b[1]}; }
DEV int mk_base(int lane, int ks) { int r16 = lane & 15, g = lane >> 4; return r16 * 128 + (((4 * ks + g) ^ (r16 & 7)) << 4); }
constexpr u32 ONES = 0x3f803f80u;
DEV u32x4 pair_swap(u32 p01, u32 p23, u32 q01, u32 q23) {
  auto r0 = __builtin_amdgcn_permlane16_swap(p01, q01, false, false);
  auto r1 = __builtin_amdgcn_permlane16_swap(p23, q23, false, false);
  return u32x4{r0[0], r1[0], r0[1], r1[1]};
}
template <int LDW> DEV void w64_issue(const u16* wsrc, u32 dst, int w, int lane) {
  const u32 vo = kn_voff(LDW, w, lane);
  const u32 d = dst + w * 1024;
  glds<0 * 8192, 0 * 128 * LDW>(vo, wsrc, d); glds<1 * 8192, 1 * 128 * LDW>(vo, wsrc, d);
  glds<2 * 8192, 2 * 128 * LDW>(vo, wsrc, d); glds<3 * 8192, 3 * 128 * LDW>(vo, wsrc, d);
  glds<4 * 8192, 4 * 128 * LDW>(vo, wsrc, d); glds<5 * 8192, 5 * 128 * LDW>(vo, wsrc, d);
  glds<6 * 8192, 6 * 128 * LDW>(vo, wsrc, d); glds<7 * 8192, 7 * 128 * LDW>(vo, wsrc, d);
}
DEV void w64_regs(const char* img, int wn, int lane, u32x4 (&B)[16][2]) {
  const char* p0 = img + (kn_base(lane) ^ ((2 * wn) << 5));
  const char* p1 = img + (kn_base(lane) ^ ((2 * wn + 1) << 5));
#pragma unroll
  for (int s = 0; s < 16; s++) { B[s][0] = kn_frag(p0 + s * 4096); B[s][1] = kn_frag(p1 + s * 4096); }
}
template <int LDW> DEV void w64_issue4(const u16* wsrc, u32 dst, int w, int lane) {
  w64_issue<LDW>(wsrc, dst, w - 4, lane); w64_issue<LDW>(wsrc, dst, w, lane);
}
template <int H> DEV void w3_issue(const u16* sb, u32 lds0, int w, int lane) {
  constexpr int CB3 = 128 * F_;
  const u32 vb = kn_voff(F_, w, lane), dw = lds0 + GA_B + w * 1024;
  glds<0, (4 * H) * CB3>(vb, sb, dw); glds<8192, (4 * H) * CB3 + 128>(vb, sb, dw);
  glds<16384, (4 * H + 1) * CB3>(vb, sb, dw); glds<24576, (4 * H + 1) * CB3 + 128>(vb, sb, dw);
  glds<32768, (4 * H + 2) * CB3>(vb, sb, dw); glds<40960, (4 * H + 2) * CB3 + 128>(vb, sb, dw);
  glds<49152, (4 * H + 3) * CB3>(vb, sb, dw); glds<57344, (4 * H + 3) * CB3 + 128>(vb, sb, dw);
}

__global__ __launch_bounds__(512, 2) void k_fused(Params p) {
  __shared__ __attribute__((aligned(16))) char smem[LDS_ALL];
  const int bid = blockIdx.x;
  const int tid = threadIdx.x, w = __builtin_amdgcn_readfirstlane(tid >> 6), lane = tid & 63;
  const int r16 = lane & 15, g = lane >> 4;
  const u32 lds0 = lds_addr(smem);
  const rsrc_t wsr = __builtin_amdgcn_make_buffer_rsrc(p.ws, 0, 0x4000000, 0x00020000);
  STAMP(0);
  const int cp = (bid & 7) * 32 + (bid >> 3);
  const int pm = (bid & 7) * 4 + (bid >> 6), pn = (bid >> 3) & 7;
  const int row0 = pm * 64, col0 = pn * 64;
  u32x2 tag;
  u32 TKW = 0;
  {
    u32x2 ep = ld8_dev(wsr, FL_EPOCH);
    u32x4 xv = *(const u32x4*)(p.x + (bid * 8 + w) * D_ + lane * 8);
    const int chl = w >> 2, role = (w >> 1) & 1, n = tid & 127;
    const int ch = cp * 2 + chl;
    const int pi = ch * NS_ + n;
    u32 r_are = p.are[pi], r_aim = p.aim[pi], r_bre = p.bre[pi], r_bim = p.bim[pi], r_cre = p.cre[pi], r_cim = p.cim[pi];
    u32 r_ldt = p.logdt[ch];
    u32x4 kidx = *(const u32x4*)(KTBL.v + (tid & 255) * 8);
    SCHED();
    u32 sk0, sk1, sk2;
    {
      u64 pc; u32 len;
      asm volatile("s_getpc_b64 %0\n.Lk_here:\n\ts_mov_b32 %1, .Lk_end-.Lk_here" : "=s"(pc), "=s"(len));
      const u32 o = (u32)((bid >> 3) * 24 + w * 3) * 64;
      asm volatile("s_load_dword %0, %3, %4\n\ts_load_dword %1, %3, %5\n\ts_load_dword %2, %3, %6"
                   : "=&s"(sk0), "=&s"(sk1), "=&s"(sk2) : "s"(pc), "s"(min(o, len)), "s"(min(o + 64, len)), "s"(min(o + 128, len)) : "memory");
    }
    {
      u64 e = ((u64)ep[1] << 32 | ep[0]) + 1;
      tag = u32x2{(u32)__builtin_amdgcn_readfirstlane((u32)e), (u32)__builtin_amdgcn_readfirstlane((u32)(e >> 32))};
    }
    char* su = smem + P0_LN;
    float* ms = (float*)(smem + P0_MS);
    {
      f32x2 v[4];
#pragma unroll
      for (int i = 0; i < 4; i++) v[i] = bfup(xv[i]);
      f32x2 s2 = (v[0] + v[1]) + (v[2] + v[3]);
      float mean = wave_sum(s2[0] + s2[1]) * (1.0f / D_);
      f32x2 q2 = {0, 0};
#pragma unroll
      for (int i = 0; i < 4; i++) { f32x2 d = v[i] - mean; q2 += d * d; }
      float var = wave_sum(q2[0] + q2[1]) * (1.0f / D_);
      float m = rbf(mean);
      float inv = __builtin_amdgcn_rcpf(rbf(__builtin_amdgcn_sqrtf(rbf(rbf(var) + 1e-5f))));
      u32x4 uo;
#pragma unroll
      for (int i = 0; i < 4; i++) {
        f32x2 u = bfup(pkbf2(bfup(pkbf2(v[i] - m)) * inv));
        uo[i] = pkh(u[0], u[1]);
      }
      u32x4 lo4, hi4;
#pragma unroll
      for (int i = 0; i < 2; i++) {
        lo4[2 * i] = __builtin_amdgcn_perm(xv[i], uo[i], 0x05040100u); lo4[2 * i + 1] = __builtin_amdgcn_perm(xv[i], uo[i], 0x07060302u);
        hi4[2 * i] = __builtin_amdgcn_perm(xv[2 + i], uo[2 + i], 0x05040100u); hi4[2 * i + 1] = __builtin_amdgcn_perm(xv[2 + i], uo[2 + i], 0x07060302u);
      }
      *(u32x4*)(su + w * 2112 + lane * 32) = lo4;
      *(u32x4*)(su + w * 2112 + lane * 32 + 16) = hi4;
    }
    asm volatile("" : "+v"(kidx));
    STAMP(1);
    lds_bar();
    asm volatile("" :: "s"(sk0), "s"(sk1), "s"(sk2));
    {
      const int q4 = (lane & 15) >> 2, pp = lane & 3;
      const char* a = su + q4 * 2112 + (128 * w + 16 * g + 4 * pp) * 2;
      u32x2 u0 = tr_read(a), u1 = tr_read(a + 4 * 2112), x0 = tr_read(a + 128), x1 = tr_read(a + 4 * 2112 + 128);
      const u32 dst = WS_U8 + bid * 16384 + w * 2048 + lane * 16;
      st16_dev(wsr, dst, u32x4{u0[0], u0[1], u1[0], u1[1]});
      st16_dev(wsr, dst + 1024, u32x4{x0[0], x0[1], x1[0], x1[1]});
    }
    float dt = rbf(expf(bf2f(r_ldt)));
    float ar = dt * bf2f(r_are), ai = dt * bf2f(r_aim);
    cf CB = cmul(cf{bf2f(r_cre), bf2f(r_cim)}, cf{dt * bf2f(r_bre), dt * bf2f(r_bim)});
    float tl2 = ar * 1.4426950408889634f;
    const float I2PH = 0.15915494309189535f;
    const float I2PL = (float)(0.15915494309189533576888 - (double)0.15915494309189535f);
    float rh = ai * I2PH;
    float rl = __builtin_fmaf(ai, I2PH, -rh) + ai * I2PL;
    {
      float ax = fabsf(CB.x), ay = fabsf(CB.y);
      float mx = wave_max(fmaxf(ax, ay)), bs = wave_sum(ax + ay), isx;
      float sx = pow2_scale(mx, 13, isx);
      CB = cscale(CB, sx);
      if (role == 0 && lane == 0) { ms[chl * 2 + (w & 1)] = bs; ms[4 + chl * 2 + (w & 1)] = isx; }
    }
    {
      const u16* wb = w == 0 ? p.w3 : w == 1 ? p.w4 : w == 2 ? p.w1 : p.w2;
      touch(TKW, wb, w < 2 ? (u32)(bid * 56 + min(lane, 55)) * 128 : (u32)(bid * 16 + (lane & 15)) * 128);
    }
    STAMP(2);
    char* img = smem + chl * KG_CH;
    {
      char* wp = img + n * 4;
      if (role == 0) {
        cf e3 = cexpk(tl2, -rh, -rl, 8.f), e4 = cexpk(tl2, -rh, -rl, 16.f), e5 = cexpk(tl2, -rh, -rl, 32.f), e6 = cexpk(tl2, -rh, -rl, 64.f);
        cf y[8];
        y[0] = {16384.f, 0.f}; y[1] = cscale(e3, 16384.f); y[2] = cscale(e4, 16384.f); y[3] = cmul(y[2], e3);
        y[4] = cscale(e5, 16384.f); y[5] = cmul(y[4], e3); y[6] = cmul(y[4], e4); y[7] = cmul(y[6], e3);
#pragma unroll
        for (int j = 0; j < 8; j++) st_y(wp + (41 + j) * KG_ROW, y[j]);
#pragma unroll
        for (int j = 0; j < 8; j++) st_y(wp + (49 + j) * KG_ROW, j ? cmul(y[j], e6) : cscale(e6, 16384.f));
        cf e7 = cexpk(tl2, rh, rl, 128.f), e8 = cexpk(tl2, rh, rl, 256.f), e9 = cexpk(tl2, rh, rl, 512.f);
        cf e10 = cexpk(tl2, rh, rl, 1024.f), e11 = cexpk(tl2, rh, rl, 2048.f);
        cf g8 = cmul(CB, e10), g12 = cmul(g8, e9), g16 = cmul(CB, e11);
        cf g10 = cmul(g8, e8), g14 = cmul(g12, e8);
        st_x(wp + 32 * KG_ROW, g8); st_x(wp + 33 * KG_ROW, cmul(g8, e7));
        st_x(wp + 34 * KG_ROW, g10); st_x(wp + 35 * KG_ROW, cmul(g10, e7));
        st_x(wp + 36 * KG_ROW, g12); st_x(wp + 37 * KG_ROW, cmul(g12, e7));
        st_x(wp + 38 * KG_ROW, g14); st_x(wp + 39 * KG_ROW, cmul(g14, e7));
        st_x(wp + 40 * KG_ROW, g16);
      } else {
        cf e0 = cexpk(tl2, rh, rl, 1.f), e1 = cexpk(tl2, rh, rl, 2.f), e2 = cexpk(tl2, rh, rl, 4.f);
        cf e7 = cexpk(tl2, rh, rl, 128.f), e8 = cexpk(tl2, rh, rl, 256.f), e9 = cexpk(tl2, rh, rl, 512.f);
        cf eb[8];
        eb[1] = e0; eb[2] = e1; eb[3] = cmul(e1, e0); eb[4] = e2; eb[5] = cmul(e2, e0); eb[6] = cmul(e2, e1); eb[7] = cmul(eb[6], e0);
        cf gq[8];
        gq[0] = CB; gq[1] = cmul(CB, e7); gq[2] = cmul(CB, e8); gq[3] = cmul(gq[2], e7);
        gq[4] = cmul(CB, e9); gq[5] = cmul(gq[4], e7); gq[6] = cmul(gq[4], e8); gq[7] = cmul(gq[6], e7);
#pragma unroll
        for (int q = 0; q < 2; q++)
#pragma unroll
          for (int b = 0; b < 8; b++) st_x(wp + (q * 8 + b) * KG_ROW, b ? cmul(gq[q], eb[b]) : gq[q]);
#pragma unroll
        for (int q = 2; q < 4; q++)
#pragma unroll
          for (int b = 0; b < 4; b++) st_x(wp + (16 + (q - 2) * 4 + b) * KG_ROW, b ? cmul(gq[q], eb[2 * b]) : gq[q]);
#pragma unroll
        for (int q = 4; q < 8; q++)
#pragma unroll
          for (int b = 0; b < 2; b++) st_x(wp + (24 + (q - 4) * 2 + b) * KG_ROW, b ? cmul(gq[q], e2) : gq[q]);
      }
    }
    STAMP(3);
    wait_vm_lgkm<1>(); bar();
    if (tid < 16) st8_dev(wsr, FL_1 + tid * 2048 + bid * 8, tag);
    char* kt = smem + P0_KT + chl * 12288;
    float kinv;
    const float kscale = pow2_scale(ms[chl * 2] + ms[chl * 2 + 1], 14, kinv);
    const float fsc = ms[4 + chl * 2 + ((w & 3) >> 1)] * (kscale * (1.0f / 16384.f));
    {
      const int wv = w & 3;
      const char* pa = img + r16 * KG_ROW + wv * 128 + g * 16;
      u32x4 a[3][2], yh[2], yl[2];
#pragma unroll
      for (int s = 0; s < 2; s++) {
#pragma unroll
        for (int t = 0; t < 3; t++) a[t][s] = *(const u32x4*)(pa + t * 16 * KG_ROW + s * 64);
        yh[s] = *(const u32x4*)(pa + 41 * KG_ROW + s * 64);
        yl[s] = *(const u32x4*)(pa + 57 * KG_ROW + s * 64);
      }
      const float f = fsc;
      char* kw = kt + (4 * g * 16 + r16) * 16 + ((wv + (r16 >> 3) + 2 * (g & 1)) & 3) * 4;
      f32x4 c[3];
#pragma unroll
      for (int t = 0; t < 3; t++) { mmah0(c[t], a[t][0], yl[0]); mmah(c[t], a[t][1], yl[1]); }
#pragma unroll
      for (int t = 0; t < 3; t++) { mmah(c[t], a[t][0], yh[0]); mmah(c[t], a[t][1], yh[1]); }
      mfence(); pin(c[0]); pin(c[1]); pin(c[2]);
#pragma unroll
      for (int t = 0; t < 3; t++)
#pragma unroll
        for (int r = 0; r < 4; r++) *(float*)(kw + (t * 16 + r) * 256) = c[t][r] * f;
    }
    if (w == 0) {
      poll256(wsr, FL_1 + (bid & 15) * 2048, tag, lane);
      if (bid == 0 && lane == 0) st8_dev(wsr, FL_EPOCH, tag);
    }
    lds_bar();
    STAMP(4);
    {
      const char* sa = p.ws + WS_U8 + (size_t)w * 16 * 16384 + cp * 64;
      const u32 vo = (u32)((lane >> 2) * 16384 + (lane & 3) * 16);
      gldsd<CV_UI, 0>(vo, sa, lds0 + w * 1024); gldsd<CV_UI + 8192, 128 * 16384>(vo, sa, lds0 + w * 1024);
    }
    {
      u32x4 o;
#pragma unroll
      for (int i = 0; i < 4; i++) {
        f32x4 a = *(const f32x4*)(kt + (kidx[i] & 0xffffu)), b = *(const f32x4*)(kt + (kidx[i] >> 16));
        o[i] = pkh((a[0] + a[1]) + (a[2] + a[3]), (b[0] + b[1]) + (b[2] + b[3]));
      }
      const int e = tid & 255;
      char* base = smem + chl * CV_CH;
      *(u32x4*)(base + CV_KP + (30 + e) * 16) = o;
      if (e < 30) *(u32x4*)(base + CV_KP + e * 16) = u32x4{0, 0, 0, 0};
      else if (e < 34) *(u32x4*)(base + (e - 30) * CV_COPY) = u32x4{0, 0, 0, 0};
      else if (e == 34) *(float*)(base + CV_KP + 4576) = kinv;
    }
  }
  STAMP(5);
  STAMP(6);
  {
    {
      const int chl = w >> 2, q = tid & 255;
      char* base = smem + chl * CV_CH;
      wait_vm<0>(); keep(TKW); bar();
      const char* ui = smem + CV_UI + q * 64 + chl * 32;
      u32x4 cur = *(const u32x4*)ui, prv = *(const u32x4*)(ui - 64);
      if (q == 0) prv = u32x4{0, 0, 0, 0};
      u32 wd[8] = {prv[0], prv[1], prv[2], prv[3], cur[0], cur[1], cur[2], cur[3]};
      u32 o[5];
#pragma unroll
      for (int k = 0; k < 5; k++) o[k] = __builtin_amdgcn_alignbit(wd[k + 1], wd[k], 16);
      char* d = base + (q + 1) * 16;
      *(u32x4*)(d) = u32x4{o[0], o[1], o[2], o[3]};
      *(u32x4*)(d + CV_COPY) = u32x4{wd[1], wd[2], wd[3], wd[4]};
      *(u32x4*)(d + 2 * CV_COPY) = u32x4{o[1], o[2], o[3], o[4]};
      *(u32x4*)(d + 3 * CV_COPY) = u32x4{wd[2], wd[3], wd[4], wd[5]};
      if (q == 255) {
        u32 o4 = __builtin_amdgcn_alignbit(cur[1], cur[0], 16), o5 = __builtin_amdgcn_alignbit(cur[2], cur[1], 16);
        u32 o6 = __builtin_amdgcn_alignbit(cur[3], cur[2], 16), o7 = cur[3] >> 16;
        char* d2 = base + 257 * 16;
        *(u32x4*)(d2) = u32x4{o4, o5, o6, o7};
        *(u32x4*)(d2 + CV_COPY) = u32x4{cur[1], cur[2], cur[3], 0};
        *(u32x4*)(d2 + 2 * CV_COPY) = u32x4{o5, o6, o7, 0};
        *(u32x4*)(d2 + 3 * CV_COPY) = u32x4{cur[2], cur[3], 0, 0};
      }
    }
    const int chl = (w >> 1) & 1, hh = w & 1;
    const float dsk = bf2f(p.dskip[cp * 2 + chl]);
    const char* base = smem + chl * CV_CH;
    const int j2 = 2 * (hh + 2 * (w >> 2));
    char* red = smem + CV_RED + chl * 16384 + lane * 16;
    lds_bar();
    STAMP(7);
    f32x4 f0 = {0, 0, 0, 0}, f1 = {0, 0, 0, 0};
    if (w < 4) {
      f32x4 acc[8];
      const char* pa = base + (r16 & 3) * CV_COPY + 48 - 16 * g + 2 * (r16 & 12) + 256 * hh;
      const char* pb = base + CV_KP + 448 + 32 * r16 + 16 * g - 256 * hh;
      u32x4 A0[8], B0[8], A1[8], B1[8];
#pragma unroll
      for (int i = 0; i < 8; i++) conv_load1(pa, pb, 0, i, A0, B0);
      conv_step(pa, pb, 1, A0, B0, A1, B1, acc);
      conv_step(pa, pb, 2, A1, B1, A0, B0, acc);
      conv_step(pa, pb, 3, A0, B0, A1, B1, acc);
      conv_step(pa, pb, 4, A1, B1, A0, B0, acc);
      mfence();
#pragma unroll
      for (int i = 0; i < 8; i++) pin(acc[i]);
      STAMP(27);
      char* d = red + hh * 8192;
      if (hh) { *(f32x4*)(d) = acc[0]; *(f32x4*)(d + 1024) = acc[1]; f0 = acc[2]; f1 = acc[3]; }
      else { *(f32x4*)(d + 2048) = acc[2]; *(f32x4*)(d + 3072) = acc[3]; f0 = acc[0]; f1 = acc[1]; }
#pragma unroll
      for (int t = 4; t < 8; t++) *(f32x4*)(d + t * 1024) = acc[t];
    } else {
      w64_issue<D_>(p.w1 + col0, lds0 + GA_B, w - 4, lane);
      w64_issue<D_>(p.w1 + col0, lds0 + GA_B, w, lane);
      wait_vm<0>();
    }
    const float kinv = *(const float*)(base + CV_KP + 4576);
    const int l0 = 256 * j2 + 16 * r16 + 4 * g;
    u32x2 xx[2];
    u32 dd[2][2];
#pragma unroll
    for (int k = 0; k < 2; k++) {
      const char* ui = smem + CV_UI + ((l0 + 256 * k) >> 3) * 64 + chl * 32 + (l0 & 4) * 2;
      u32x2 uu = *(const u32x2*)ui;
      xx[k] = *(const u32x2*)(ui + 16);
      dd[k][0] = pkbf2(hup(uu[0]) * dsk); dd[k][1] = pkbf2(hup(uu[1]) * dsk);
    }
    lds_bar();
    STAMP(8);
    if (w < 4) {
      f0 += *(const f32x4*)(red + (hh ^ 1) * 8192 + j2 * 1024);
      f1 += *(const f32x4*)(red + (hh ^ 1) * 8192 + j2 * 1024 + 1024);
    } else {
      f0 = *(const f32x4*)(red + j2 * 1024) + *(const f32x4*)(red + 8192 + j2 * 1024);
      f1 = *(const f32x4*)(red + j2 * 1024 + 1024) + *(const f32x4*)(red + 8192 + j2 * 1024 + 1024);
    }
    char* stg = smem + CV_STG;
#pragma unroll
    for (int k = 0; k < 2; k++) {
      f32x4 f = k ? f1 : f0;
      u32 y01 = pkbf2(f32x2{f[0], f[1]} * kinv), y23 = pkbf2(f32x2{f[2], f[3]} * kinv);
      u32 s01 = pkbf2(bfup(y01) + bfup(dd[k][0])), s23 = pkbf2(bfup(y23) + bfup(dd[k][1]));
      u32 h01 = pkbf2(bfup(xx[k][0]) + bfup(s01)), h23 = pkbf2(bfup(xx[k][1]) + bfup(s23));
      *(u32x2*)(stg + (16 * (j2 + k) + r16) * 80 + chl * 32 + g * 8) = u32x2{h01, h23};
    }
    lds_bar();
    {
      const int lb = tid >> 2, pc = tid & 3;
      st16_dev(wsr, WS_HT + cp * 8192 + tid * 16, *(const u32x4*)(stg + lb * 80 + pc * 16));
    }
  }
  const int mt = w & 3, wn = w >> 2;
  const int rt = (pm >> 1) * 8 + (pm & 1) * 2 + (mt >> 1) * 4 + (mt & 1);
  const int pc4 = (g & 1) * 2 + (g >> 1);
  const u32 boff = (u32)((rt * 16 + pn * 2 + wn) * 1024 + r16 * 64 + pc4 * 16);
  u32x4 ONES4 = {ONES, ONES, ONES, ONES};
  asm volatile("s_nop 1" : "+v"(ONES4));
  const u32 dw = lds0 + w * 1024;
  const u32 vbD = kn_voff(D_, w, lane);
  u32x4 hres, xres;
  u32x4 B[16][2];
  constexpr int CBD = 128 * D_, CB3 = 128 * F_;
  const int idx3 = bid >> 3;
  const bool has3 = idx3 < 28;
  const int pm3 = (bid & 7) * 2 + (idx3 >= 14 ? 1 : 0), pn3 = idx3 >= 28 ? idx3 - 28 : idx3 >= 14 ? idx3 - 14 : idx3;
  const u16* sb3 = p.w3 + pn3 * 128;
  const u32 vbF = kn_voff(F_, w, lane);
  const u32 vbF4 = kn_voff(F_, w - 4, lane);
  const int wm3 = w & 1, wn3 = w >> 1;
  const int ob0 = (kn_base(lane) + (wn3 >> 1) * 8192) ^ ((2 * (wn3 & 1)) << 5), ob1 = ob0 ^ 32;
  f32x4 cw0, cw1;
  u32x4 Bt[2][8];
  const char* pb0 = smem + GA_B + (kn_base(lane) ^ ((2 * wn) << 5));
  const char* pb1 = smem + GA_B + (kn_base(lane) ^ ((2 * wn + 1) << 5));
  auto ldB = [&](int j) {
#pragma unroll
    for (int i = 0; i < 4; i++) { Bt[j & 1][2 * i] = kn_frag(pb0 + (4 * j + i) * 4096); Bt[j & 1][2 * i + 1] = kn_frag(pb1 + (4 * j + i) * 4096); }
    SCHED();
  };
  auto ldB3 = [&](int s0, int n, int o) {
#pragma unroll
    for (int s = 0; s < 8; s++) if (s < n) {
      B[s0 + s][0] = kn_frag(smem + o + (s >> 1) * 16384 + (s & 1) * 4096 + ob0);
      B[s0 + s][1] = kn_frag(smem + o + (s >> 1) * 16384 + (s & 1) * 4096 + ob1);
    }
#pragma unroll
    for (int s = 0; s < 8; s++) if (s < n) { mmaz(s0 + s == 0, cw0, B[s0 + s][0], ONES4); mmaz(s0 + s == 0, cw1, B[s0 + s][1], ONES4); }
  };
#define W2C(c) glds<GA_B + (c) * 8192, (c) * CBD>(vbD, p.w2 + col0, dw);
#define W3C(c, dst) glds<(dst), (c) * CB3>(vbF, sb3, dw); glds<(dst) + 8192, (c) * CB3 + 128>(vbF, sb3, dw);
#define W3C4(c, dst) W3C(c, dst) glds<(dst), (c) * CB3>(vbF4, sb3, dw - 4096); glds<(dst) + 8192, (c) * CB3 + 128>(vbF4, sb3, dw - 4096);
  {
    f32x4 cv0, cv1;
    {
#pragma unroll
      for (int i = 0; i < 4; i++) { Bt[0][2 * i] = kn_frag(pb0 + (4 * mt + i) * 4096); Bt[0][2 * i + 1] = kn_frag(pb1 + (4 * mt + i) * 4096); }
#pragma unroll
      for (int i = 0; i < 4; i++) { mmaz(i == 0, cv0, Bt[0][2 * i], ONES4); mmaz(i == 0, cv1, Bt[0][2 * i + 1], ONES4); }
      mfence(); pin(cv0); pin(cv1);
      char* ct = smem + 65536 + wn * 128 + g * 16;
      if (r16 == 0) { *(f32x4*)(ct + mt * 256) = cv0; *(f32x4*)(ct + mt * 256 + 64) = cv1; }
      lds_bar();
      cv0 = (*(const f32x4*)(ct) + *(const f32x4*)(ct + 256)) + (*(const f32x4*)(ct + 512) + *(const f32x4*)(ct + 768));
      cv1 = (*(const f32x4*)(ct + 64) + *(const f32x4*)(ct + 320)) + (*(const f32x4*)(ct + 576) + *(const f32x4*)(ct + 832));
    }
    wait_vm_lgkm<0>(); bar();
    if (tid < 16) st8_dev(wsr, FL_2 + tid * 2048 + bid * 8, tag);
    STAMP(9);
    if (w >= 4) { W3C4(0, 65536) W3C4(1, 81920) }
    ldB(0); ldB(1);
    if (w == 0) poll256(wsr, FL_2 + (bid & 15) * 2048, tag, lane);
    bar();
    STAMP(10);
    {
      const char* sa = p.ws + WS_HT + rt * 64 + (size_t)wn * 262144;
      const int j2 = lane ^ (((lane >> 4) & 1) << 3);
      const u32 vo = (u32)((j2 >> 2) * 8192 + (j2 & 3) * 16);
      const u32 d = lds0 + GA_A + mt * 16384 + wn * 2048;
      gldsd<0, 0>(vo, sa, d); gldsd<1024, 131072>(vo, sa, d); gldsd<4096, 4 * 131072>(vo, sa, d); gldsd<5120, 5 * 131072>(vo, sa, d);
      gldsd<8192, 8 * 131072>(vo, sa, d); gldsd<9216, 9 * 131072>(vo, sa, d); gldsd<12288, 12 * 131072>(vo, sa, d); gldsd<13312, 13 * 131072>(vo, sa, d);
    }
    const char* aw = smem + GA_A + mt * 16384;
    const int ab = g * 256 + ((g & 1) << 7) + ((lane & 15) >> 2) * 32 + (lane & 3) * 8;
    const char* pa0 = aw + ab;
    const char* pa1 = aw + (ab ^ 128);
    f32x4 acc0, acc1, sacc, gacc;
    u32x4 a[2][4];
    auto ld = [&](int j) {
#pragma unroll
      for (int i = 0; i < 4; i++) {
        u32x2 x0 = tr_read(pa0 + (4 * j + i) * 1024), x1 = tr_read(pa1 + (4 * j + i) * 1024);
        a[j & 1][i] = u32x4{x0[0], x0[1], x1[0], x1[1]};
      }
      SCHED();
    };
    auto mm = [&](int j) {
#pragma unroll
      for (int i = 0; i < 4; i++) {
        mmaz(j + i == 0, sacc, ONES4, a[j & 1][i]);
        mmaz(j + i == 0, gacc, a[j & 1][i], a[j & 1][i]);
        mmaz(j + i == 0, acc0, Bt[j & 1][2 * i], a[j & 1][i]);
        mmaz(j + i == 0, acc1, Bt[j & 1][2 * i + 1], a[j & 1][i]);
      }
      SCHED();
    };
    SCHED();
    wait_vm<6>(); bar(); ld(0);
    STAMP(23);
    mm(0); ldB(2);
    wait_vm<4>(); bar(); ld(1); mm(1); ldB(3);
    wait_vm<2>(); bar(); ld(2); mm(2);
    wait_vm_lgkm<0>(); bar(); ld(3);
    W2C(0) W2C(1)
    STAMP(24);
    const int ab2 = pc4 * 256 + ((pc4 & 1) << 7) + ((lane & 15) >> 2) * 32 + (lane & 3) * 8;
    const char* hs = aw + (2 * pn + wn) * 1024;
    u32x2 hx0 = tr_read(hs + ab2), hx1 = tr_read(hs + (ab2 ^ 128));
    SCHED(); mm(3); W2C(2) W2C(3)
    mfence(); pin(acc0); pin(acc1); pin(sacc); pin(gacc);
    STAMP(11);
    float dg = (r16 & 2) ? ((r16 & 1) ? gacc[3] : gacc[2]) : ((r16 & 1) ? gacc[1] : gacc[0]);
    f32x2 sa2 = {sacc[0], gsum(g == (r16 >> 2) ? dg : 0.f)};
    float mean1 = sa2[0] * (1.0f / D_);
    float var1 = fmaxf(sa2[1] * (1.0f / D_) - mean1 * mean1, 0.f);
    float m1 = rbf(mean1);
    float sd1 = rbf(__builtin_amdgcn_sqrtf(rbf(rbf(var1) + 1e-5f)));
    float i1 = __builtin_amdgcn_rcpf(sd1);
    float m2 = rbf((mean1 - m1) * i1);
    float sd2 = rbf(__builtin_amdgcn_sqrtf(rbf(rbf(var1 * i1 * i1) + 1e-5f)));
    float rs = i1 * __builtin_amdgcn_rcpf(sd2), tt = -(m1 + m2 * sd1) * rs;
    {
      f32x4 rt4 = {rs, rs, tt, tt};
      pin(rt4);
      u32x2 o0 = act4(acc0, cv0, rt4), o1 = act4(acc1, cv1, rt4);
      st16_dev(wsr, WS_H1 + 16384 + boff, pair_swap(o0[0], o0[1], o1[0], o1[1]));
    }
    if (tid < 16) {
      const u32 o = (u32)((pn * 2 + (tid >> 3)) * 1024 + (tid & 7) * 16);
      if (pm == 0) st16_dev(wsr, WS_H1 + 14 * 64 + o, u32x4{0, 0, 0, 0});
      if (pm == 31) st16_dev(wsr, WS_H1 + 129 * 16384 + o, u32x4{0, 0, 0, 0});
    }
    hres = u32x4{hx0[0], hx0[1], hx1[0], hx1[1]};
#pragma unroll
    for (int i = 0; i < 4; i++) xres[i] = pkbf2(bfup(pkbf2(bfup(hres[i]) - m1)) * i1);
    W2C(4) W2C(5) W2C(6) W2C(7)
    wait_vm_lgkm<4>(); bar();
    if (tid < 8) st8_dev(wsr, FL_3 + tid * 2048 + (pm * 8 + pn) * 8, tag);
    ldB3(0, 4, 65536);
    ldB(0); ldB(1);
  }
  STAMP(12);
  const int ar = lane >> 3, asp = (lane & 7) ^ ar;
  const u32 apo = (u32)((asp >> 2) * 1024 + (asp & 3) * 16);
  {
    u32 vo[8];
#pragma unroll
    for (int k = 0; k < 8; k++) {
      const int sh = (k >> 1) == 0 ? -1 : (k >> 1) == 1 ? 1 : (k >> 1) == 2 ? -2 : 2;
      const int row = 16 + ar + (k & 1) * 8 + sh;
      vo[k] = (u32)((row >> 4) * 16384 + (row & 15) * 64) + apo;
    }
    if (w == 0) polln(wsr, FL_3 + pn * 2048, max((pm & ~1) - 1, 0) * 8, min((pm & ~1) + 3, 32) * 8, tag, lane);
    lds_bar();
    STAMP(13);
    {
      const char* sa = p.ws + WS_H1 + (size_t)rt * 16384 + wn * 2048;
      const u32 d = lds0 + GA_A + mt * 16384 + wn * 2048;
      gldsd<0, 0>(vo[0], sa, d); gldsd<1024, 0>(vo[1], sa, d); gldsd<4096, 4096>(vo[2], sa, d); gldsd<5120, 4096>(vo[3], sa, d);
      gldsd<8192, 8192>(vo[4], sa, d); gldsd<9216, 8192>(vo[5], sa, d); gldsd<12288, 12288>(vo[6], sa, d); gldsd<13312, 12288>(vo[7], sa, d);
    }

    const char* pa0 = smem + GA_A + mt * 16384 + mk_base(lane, 0);
    const char* pa1 = smem + GA_A + mt * 16384 + mk_base(lane, 1);
    f32x4 acc0, acc1;
    u32x4 a[2][4];
    auto ld = [&](int j) {
      a[j & 1][0] = *(const u32x4*)(pa0 + j * 4096); a[j & 1][1] = *(const u32x4*)(pa1 + j * 4096);
      a[j & 1][2] = *(const u32x4*)(pa0 + j * 4096 + 2048); a[j & 1][3] = *(const u32x4*)(pa1 + j * 4096 + 2048);
      SCHED();
    };
    auto mm = [&](int j) {
#pragma unroll
      for (int i = 0; i < 4; i++) {
        mmaz(j + i == 0, acc0, Bt[j & 1][2 * i], a[j & 1][i]);
        mmaz(j + i == 0, acc1, Bt[j & 1][2 * i + 1], a[j & 1][i]);
      }
      SCHED();
    };
    SCHED();
    wait_vm<6>(); bar(); ld(0);
    STAMP(25);
    wait_vm<4>(); bar(); ld(1); mm(0); ldB(2);
    wait_vm<2>(); bar(); ld(2); mm(1); ldB(3);
    wait_vm_lgkm<0>(); bar(); ld(3);
    W3C(2, 65536)
    STAMP(26);
    mm(2); W3C(3, 81920) mm(3);
    mfence(); pin(acc0); pin(acc1);
    STAMP(14);
    u32x4 v = pair_swap(pkbf(acc0[0], acc0[1]), pkbf(acc0[2], acc0[3]), pkbf(acc1[0], acc1[1]), pkbf(acc1[2], acc1[3]));
#pragma unroll
    for (int i = 0; i < 4; i++) xres[i] = pkbf2(bfup(xres[i]) + bfup(v[i]));
    st16_dev(wsr, WS_X1 + boff, xres);
    float s1 = 0, s2 = 0;
#pragma unroll
    for (int i = 0; i < 4; i++) { s1 = dot2(xres[i], ONES, s1); s2 = dot2(xres[i], xres[i], s2); }
    s1 = gsum(s1); s2 = gsum(s2);
    if (g == 0) st8_dev(wsr, WS_ST + (rt * 16 + pn * 2 + wn) * 128 + r16 * 8, u32x2{__float_as_uint(s1), __float_as_uint(s2)});
    W3C(4, GA_B) W3C(5, GA_B + 16384)
    wait_vm_lgkm<4>(); bar();
    if (tid < 8) st8_dev(wsr, FL_4 + tid * 2048 + (pm * 8 + pn) * 8, tag);
    W3C(6, GA_B + 32768) W3C(7, GA_B + 49152)
  }
  STAMP(15);
  const u16* sb4 = p.w4 + col0;
#define G4_R1A(i) glds<GA_B + (i) * 8192, (((i) >> 1) * 7 + ((i) & 1)) * CBD>(vbD, sb4, dw);
#define G4_R1A4(i) G4_R1A(i) glds<GA_B + (i) * 8192, (((i) >> 1) * 7 + ((i) & 1)) * CBD>(vbD4, sb4, dw - 4096);
#define G4_R1A_ALL4 G4_R1A4(0) G4_R1A4(1) G4_R1A4(2) G4_R1A4(3) G4_R1A4(4) G4_R1A4(5) G4_R1A4(6) G4_R1A4(7)
  const u32 vbD4 = kn_voff(D_, w - 4, lane);
#define G4_R1B(i) glds<((i) < 2 ? G3_ST : 0) + (i) * 8192, (((i) >> 1) * 7 + 2 + ((i) & 1)) * CBD>(vbD, sb4, dw);
#define G4_R2POS(i) ((i) < 4 ? 32768 + (i) * 8192 : GA_B + ((i) - 4) * 8192)
#define G4_R2(i) glds<G4_R2POS(i), (((i) / 3) * 7 + 4 + ((i) % 3)) * CBD>(vbD, sb4, dw);
  const int wm4 = mt >> 1, kq = (mt & 1) + 2 * wn;
  const int kb = kn_base(lane);
  u32x4(&B4)[8][4] = *reinterpret_cast<u32x4(*)[8][4]>(&B);
  const char* sa4 = p.ws + WS_HID + (size_t)((rt & ~1) * 56 + 14 * kq) * 1024;
  const u32 va4 = (u32)(ar * 64) + apo;
  const int ring = (wm4 ? 0 : 65536) + kq * 8192;
  const u32 da = lds0 + ring;
#define G4_A4(b) gldsd<((b) & 1) * 4096, (b) * 2048>(va4, sa4, da); gldsd<((b) & 1) * 4096 + 1024, (b) * 2048 + 512>(va4, sa4, da); \
                 gldsd<((b) & 1) * 4096 + 2048, (b) * 2048 + 57344>(va4, sa4, da); gldsd<((b) & 1) * 4096 + 3072, (b) * 2048 + 57344 + 512>(va4, sa4, da);
  auto ldB4 = [&](int s) {
    const char* p0 = smem + GA_B + kq * 16384 + s * 4096;
    const char* p1 = smem + (kq ? kq * 16384 : G3_ST) + s * 4096;
#pragma unroll
    for (int nt = 0; nt < 4; nt++) { B4[s][nt] = kn_frag(p0 + (kb ^ (nt << 5))); B4[4 + s][nt] = kn_frag(p1 + (kb ^ (nt << 5))); }
    SCHED();
  };
  {
    const int wm = wm3;
    ldB3(4, 4, 65536);
    wait_vm<0>(); bar();
    ldB3(8, 8, GA_B);
    if (w == 0) polln(wsr, FL_4 + pn * 2048, pm3 * 16, pm3 * 16 + 16, tag, lane);
    lds_bar();
    STAMP(16);
    const char* sa = p.ws + WS_X1 + (size_t)(pm3 * 8 + (w >> 1)) * 16384 + (w & 1) * 512;
    const u32 va = (u32)(ar * 64) + apo;
#define G3_A(i) gldsd<(((i) + 4) % 9) * 8192, ((i) >> 3) * 65536 + ((i) & 7) * 2048>(va, sa, dw);
    {
      const char* ss = p.ws + WS_ST + (size_t)pm3 * 16384 + w * 1024;
      gldsd<G3_ST, 0>(lane * 16, ss, dw); gldsd<G3_ST + 8192, 8192>(lane * 16, ss, dw);
    }
    G3_A(0) G3_A(1) G3_A(2) G3_A(3) G3_A(4) G3_A(5) G3_A(6) G3_A(7) G3_A(8)
    wait_vm<9>(); bar();
    {
      const char* sst = smem + G3_ST + (tid >> 6) * 2048 + (tid & 3) * 512 + ((tid >> 2) & 15) * 8;
      f32x2 st = (*(const f32x2*)(sst) + *(const f32x2*)(sst + 128)) + (*(const f32x2*)(sst + 256) + *(const f32x2*)(sst + 384));
      float t1 = st[0] + dppf<0xB1>(st[0]), t2 = st[1] + dppf<0xB1>(st[1]);
      t1 += dppf<0x4E>(t1); t2 += dppf<0x4E>(t2);
      float mean = t1 * (1.0f / D_);
      float var = fmaxf(t2 * (1.0f / D_) - mean * mean, 0.f);
      float rs = __builtin_amdgcn_rcpf(rbf(__builtin_amdgcn_sqrtf(rbf(rbf(var) + 1e-5f))));
      if ((tid & 3) == 0) { float tt = -rbf(mean) * rs; *(f32x4*)(smem + G3_TAB + (tid >> 2) * 16) = f32x4{rs, rs, tt, tt}; }
    }
    const f32x4& cv0 = cw0;
    const f32x4& cv1 = cw1;
    float* rowt = (float*)(smem + G3_TAB);
    const int oa0 = mk_base(lane, 0) + wm * 4096, oa1 = mk_base(lane, 1) + wm * 4096;
    f32x4 acc[2][2][2];
    u32x4 a[2][4];
    auto ld = [&](int slot, u32x4 (&d)[4]) {
      const char* s = smem + slot * 8192;
      d[0] = *(const u32x4*)(s + oa0); d[1] = *(const u32x4*)(s + oa0 + 2048);
      d[2] = *(const u32x4*)(s + oa1); d[3] = *(const u32x4*)(s + oa1 + 2048);
      SCHED();
    };
    auto m1 = [&](int ps, int b, int i, const u32x4 (&d)[4]) {
      mmaz(b == 0 && i < 4, acc[ps][(i >> 1) & 1][i & 1], B[2 * b + (i >> 2)][(i >> 1) & 1], d[2 * (i >> 2) + (i & 1)]);
      SCHED();
    };
    auto mm = [&](int ps, int b, const u32x4 (&d)[4]) {
#pragma unroll
      for (int i = 0; i < 8; i++) m1(ps, b, i, d);
    };
    const u32 hoff = (has3 ? WS_HID : 0x40000000u) + (u32)(((pm3 * 8 + 2 * wm) * 56 + pn3 * 4 + wn3) * 1024 + r16 * 64 + pc4 * 16);
    u32x2 o0;
    f32x4 rtv[2];
    auto ldrt = [&](int ps) {
      rtv[0] = *(const f32x4*)(rowt + (64 * ps + 32 * wm + r16) * 4);
      rtv[1] = *(const f32x4*)(rowt + (64 * ps + 32 * wm + 16 + r16) * 4);
    };
    auto ep = [&](int ps, int i) {
      const int mi = i >> 1;
      if ((i & 1) == 0) o0 = act4(acc[ps][0][mi], cv0, rtv[mi]);
      else {
        u32x2 o1 = act4(acc[ps][1][mi], cv1, rtv[mi]);
        st16_dev(wsr, hoff + (4 * ps + mi) * 57344, pair_swap(o0[0], o0[1], o1[0], o1[1]));
      }
    };
    float c0v = -2.0f * 0.7978845608028654f * 1.4426950408889634f;
    asm volatile("" : "+v"(c0v));
    auto mme = [&](int b, const u32x4 (&d)[4], int e) {
      const float c1 = -2.0f * 0.7978845608028654f * 1.4426950408889634f * 0.044715f;
      const int mi = e >> 1;
      const f32x4 aa = acc[0][e & 1][mi], cc = (e & 1) ? cv1 : cv0;
      float v[4], x[4], z[4];
      m1(1, b, 0, d);
#pragma unroll
      for (int q = 0; q < 4; q++) v[q] = smul(aa[q], rtv[mi][0]);
      SCHED(); m1(1, b, 1, d);
#pragma unroll
      for (int q = 0; q < 4; q++) v[q] = sfma(cc[q], rtv[mi][2], v[q]);
      u32 p0 = pkbf(v[0], v[1]), p1 = pkbf(v[2], v[3]);
      SCHED(); m1(1, b, 2, d);
      x[0] = bflo(p0); x[1] = bfhi(p0); x[2] = bflo(p1); x[3] = bfhi(p1);
      z[0] = smul(x[0], x[0]); z[1] = smul(x[1], x[1]);
      SCHED(); m1(1, b, 3, d);
      z[2] = smul(x[2], x[2]); z[3] = smul(x[3], x[3]);
#pragma unroll
      for (int q = 0; q < 4; q++) z[q] = sfmak(z[q], c1, c0v);
      SCHED(); m1(1, b, 4, d);
#pragma unroll
      for (int q = 0; q < 4; q++) z[q] = smul(z[q], x[q]);
      z[0] = __builtin_amdgcn_exp2f(z[0]); z[1] = __builtin_amdgcn_exp2f(z[1]);
      SCHED(); m1(1, b, 5, d);
      z[2] = __builtin_amdgcn_exp2f(z[2]); z[3] = __builtin_amdgcn_exp2f(z[3]);
#pragma unroll
      for (int q = 0; q < 4; q++) z[q] = sadd1(z[q]);
      SCHED(); m1(1, b, 6, d);
#pragma unroll
      for (int q = 0; q < 4; q++) z[q] = __builtin_amdgcn_rcpf(z[q]);
      SCHED(); m1(1, b, 7, d);
#pragma unroll
      for (int q = 0; q < 4; q++) x[q] = smul(x[q], z[q]);
      u32x2 o = {pkbf(x[0], x[1]), pkbf(x[2], x[3])};
      if (e & 1) st16_dev(wsr, hoff + mi * 57344, pair_swap(o0[0], o0[1], o[0], o[1])); else o0 = o;
      SCHED();
    };
    SCHED();
#define SL(i) (((i) + 4) % 9)
    wait_vm_lgkm<8>(); bar(); ld(SL(0), a[0]); G4_R1A(0)
    wait_vm_lgkm<8>(); bar(); ld(SL(1), a[1]); G4_R1A(1) mm(0, 0, a[0]); SCHED();
    wait_vm_lgkm<8>(); bar(); G3_A(9) ld(SL(2), a[0]); G4_R1A(2) mm(0, 1, a[1]); SCHED();
    wait_vm_lgkm<9>(); bar(); G3_A(10) ld(SL(3), a[1]); G4_R1A(3) mm(0, 2, a[0]); SCHED();
    wait_vm_lgkm<10>(); bar(); G3_A(11) ld(SL(4), a[0]); G4_R1A(4) mm(0, 3, a[1]); SCHED();
    wait_vm_lgkm<11>(); bar(); G3_A(12) ld(SL(5), a[1]); G4_R1A(5) mm(0, 4, a[0]); SCHED();
    wait_vm_lgkm<12>(); bar(); G3_A(13) ld(SL(6), a[0]); G4_R1A(6) mm(0, 5, a[1]); SCHED();
    wait_vm_lgkm<13>(); bar(); G3_A(14) ld(SL(7), a[1]); G4_R1A(7) mm(0, 6, a[0]); SCHED();
    wait_vm_lgkm<14>(); bar(); G3_A(15) ld(SL(8), a[0]); ldrt(0); mm(0, 7, a[1]); SCHED();
    STAMP(17);
    mfence();
#pragma unroll
    for (int i = 0; i < 4; i++) pin(acc[0][i >> 1][i & 1]);
#pragma unroll
    for (int i = 0; i < 4; i++) ep(0, i);
    SCHED();
    wait_vm_lgkm<14>(); bar(); ld(SL(9), a[1]); G4_R1B(2) G4_R1B(0) mm(1, 0, a[0]); SCHED();
    wait_vm_lgkm<14>(); bar(); ld(SL(10), a[0]); G4_R1B(3) G4_R1B(1) mm(1, 1, a[1]); SCHED();
    wait_vm_lgkm<14>(); bar(); ld(SL(11), a[1]); G4_R1B(4) mm(1, 2, a[0]); SCHED();
    wait_vm_lgkm<5>(); bar();
    if (has3 && tid < 8) st8_dev(wsr, FL_5 + tid * 2048 + (pm3 * 14 + pn3) * 8, tag);
    ld(SL(12), a[0]); G4_R1B(5) mm(1, 3, a[1]); SCHED();
    lds_bar(); ld(SL(13), a[1]); G4_R1B(6) mm(1, 4, a[0]); SCHED();
    lds_bar(); ld(SL(14), a[0]); G4_R1B(7) mm(1, 5, a[1]); SCHED();
    lds_bar(); ld(SL(15), a[1]); ldrt(1); mm(1, 6, a[0]); SCHED();
    wait_vm_lgkm<0>(); bar();
    mm(1, 7, a[1]);
    mfence();
#pragma unroll
    for (int i = 0; i < 4; i++) pin(acc[1][i >> 1][i & 1]);
    STAMP(18);
#pragma unroll
    for (int i = 0; i < 4; i++) { ldB4(i); ep(1, i); SCHED(); }
    if (w == 0) polln(wsr, FL_5 + pn * 2048, (pm >> 1) * 14, (pm >> 1) * 14 + 14, tag, lane);
    lds_bar();
    if (wm4 == 0) { G4_A4(0) G4_A4(1) wait_vm<8>(); } else wait_vm<0>();
    bar();
    if (has3 && tid < 8) st8_dev(wsr, FL_6 + tid * 2048 + (pm3 * 14 + pn3) * 8, tag);
  }
  STAMP(19);
  {
    const int wm = wm4;
    if (wm) {
      const u32 vb2 = kn_voff(D_, w - 2, lane), dw2 = dw - 2048;
#define G4_R2X(i) G4_R2(i) glds<G4_R2POS(i), (((i) / 3) * 7 + 4 + ((i) % 3)) * CBD>(vb2, sb4, dw2);
      G4_R2X(0) G4_R2X(3) G4_R2X(6) G4_R2X(9) G4_R2X(1) G4_R2X(4) G4_R2X(7) G4_R2X(10) G4_R2X(2) G4_R2X(5) G4_R2X(8) G4_R2X(11)
      polln(wsr, FL_6 + (pn ^ kq) * 2048, (pm >> 1) * 14, (pm >> 1) * 14 + 14, tag, lane);
      G4_A4(0) G4_A4(1)
      wait_vm<8>(); bar();
    }
    STAMP(20);
    const char* pa0 = smem + ring + mk_base(lane, 0);
    const char* pa1 = smem + ring + mk_base(lane, 1);
    f32x4 acc[4][2];
    u32x4 a[4];
    auto ld = [&](int b) {
      a[0] = *(const u32x4*)(pa0 + (b & 1) * 4096); a[1] = *(const u32x4*)(pa0 + (b & 1) * 4096 + 2048);
      a[2] = *(const u32x4*)(pa1 + (b & 1) * 4096); a[3] = *(const u32x4*)(pa1 + (b & 1) * 4096 + 2048);
      asm volatile("s_waitcnt lgkmcnt(0)" ::: "memory");
      SCHED();
    };
    auto mm = [&](int s0, bool first = false) {
#pragma unroll
      for (int ks = 0; ks < 2; ks++)
#pragma unroll
        for (int nt = 0; nt < 4; nt++)
#pragma unroll
          for (int mi = 0; mi < 2; mi++) mmaz(first && ks == 0, acc[nt][mi], B4[s0 + ks][nt], a[2 * ks + mi]);
      SCHED();
    };
    auto ldb = [&](int j, int s0) {
      const int i = kq * 3 + j;
      const char* bl = smem + (i < 4 ? 32768 + i * 8192 : GA_B + (i - 4) * 8192);
#pragma unroll
      for (int ks = 0; ks < 2; ks++)
#pragma unroll
        for (int nt = 0; nt < 4; nt++) B4[s0 + ks][nt] = kn_frag(bl + ks * 4096 + (kb ^ (nt << 5)));
      SCHED();
    };
    wait_vm<4>(); ld(0); G4_A4(2) mm(0, true);
    wait_vm<4>(); ld(1); G4_A4(3) mm(2);
    wait_vm<4>(); ld(2); G4_A4(4) mm(4);
    wait_vm<4>(); ld(3); G4_A4(5) mm(6);
    if (wm == 0) bar();
    ldb(0, 0); ldb(1, 2);
    wait_vm<4>(); ld(4); G4_A4(6) ldb(2, 4); mm(0);
    wait_vm<4>(); ld(5); mm(2);
    wait_vm<0>(); ld(6); mm(4);
    mfence();
#pragma unroll
    for (int i = 0; i < 8; i++) pin(acc[i >> 1][i & 1]);
    STAMP(21);
    {
      char* d = smem + ring + lane * 16;
#pragma unroll
      for (int k2 = 0; k2 < 4; k2++) {
        *(f32x4*)(d + k2 * 2048) = acc[2 * (k2 >> 1)][k2 & 1];
        *(f32x4*)(d + k2 * 2048 + 1024) = acc[2 * (k2 >> 1) + 1][k2 & 1];
      }
    }
    lds_bar();
    const char* s = smem + (wm ? 0 : 65536) + kq * 2048 + lane * 16;
    f32x4 f0 = (*(const f32x4*)(s) + *(const f32x4*)(s + 8192)) + (*(const f32x4*)(s + 16384) + *(const f32x4*)(s + 24576));
    f32x4 f1 = (*(const f32x4*)(s + 1024) + *(const f32x4*)(s + 9216)) + (*(const f32x4*)(s + 17408) + *(const f32x4*)(s + 25600));
    u32x4 v = pair_swap(pkbf(f0[0], f0[1]), pkbf(f0[2], f0[3]), pkbf(f1[0], f1[1]), pkbf(f1[2], f1[3]));
    u32x4 o;
#pragma unroll
    for (int i = 0; i < 4; i++) o[i] = pkbf2(bfup(hres[i]) + bfup(pkbf2(bfup(xres[i]) + bfup(v[i]))));
    *(u32x4*)(p.out + (size_t)(rt * 16 + r16) * D_ + col0 + 32 * wn + pc4 * 8) = o;
  }
  STAMP(22);
  asm volatile(".Lk_end:");
}

extern "C" void kernel_launch(void* const* d_in, const int* in_sizes, int n_in, void* d_out, int out_size, void* d_ws,
                              size_t ws_size, hipStream_t stream) {
  Params p;
  p.x = (const u16*)d_in[0]; p.are = (const u16*)d_in[1]; p.aim = (const u16*)d_in[2]; p.bre = (const u16*)d_in[3];
  p.bim = (const u16*)d_in[4]; p.cre = (const u16*)d_in[5]; p.cim = (const u16*)d_in[6]; p.logdt = (const u16*)d_in[7];
  p.dskip = (const u16*)d_in[8]; p.w1 = (const u16*)d_in[10]; p.w2 = (const u16*)d_in[11]; p.w3 = (const u16*)d_in[12];
  p.w4 = (const u16*)d_in[13]; p.out = (u16*)d_out; p.ws = (char*)d_ws;
  k_fused<<<256, 512, 0, stream>>>(p);
}
```

```cpp
#include <hip/hip_runtime.h>
#include <stdint.h>

typedef unsigned short u16;
typedef unsigned int u32;
typedef unsigned long long u64;
typedef __bf16 bf16x2 __attribute__((ext_vector_type(2)));
typedef __bf16 bf16x8 __attribute__((ext_vector_type(8)));
typedef _Float16 f16x2 __attribute__((ext_vector_type(2)));
typedef _Float16 f16x8 __attribute__((ext_vector_type(8)));
typedef float f32x2 __attribute__((ext_vector_type(2)));
typedef float f32x4 __attribute__((ext_vector_type(4)));
typedef short s16x4 __attribute__((ext_vector_type(4)));
typedef u32 u32x2 __attribute__((ext_vector_type(2)));
typedef u32 u32x4 __attribute__((ext_vector_type(4)));
#define LDSP __attribute__((address_space(3)))
#define DEV __device__ __forceinline__

constexpr int L_ = 2048, D_ = 512, NS_ = 128, F_ = 1792;
constexpr u32 WS_U8 = 0;
constexpr u32 WS_HT = 7u << 20;
constexpr u32 WS_H1 = 13u << 20;
constexpr u32 WS_X1 = 16u << 20;
constexpr u32 WS_ST = 18u << 20;
constexpr u32 WS_HID = 19u << 20;
constexpr u32 WS_FL = 28u << 20;
constexpr u32 FL_EPOCH = WS_FL, FL_1 = WS_FL + 4096, FL_2 = FL_1 + 32768, FL_3 = FL_2 + 32768, FL_4 = FL_3 + 16384, FL_5 = FL_4 + 16384, FL_6 = FL_5 + 16384;

struct Params {
  const u16 *x, *are, *aim, *bre, *bim, *cre, *cim, *logdt, *dskip, *w1, *w2, *w3, *w4;
  u16* out;
  char* ws;
};

DEV float bf2f(u32 v) { return __uint_as_float(v << 16); }
DEV float bflo(u32 v) { return __uint_as_float(v << 16); }
DEV float bfhi(u32 v) { return __uint_as_float(v & 0xffff0000u); }
DEV f32x2 bfup(u32 v) { return f32x2{bflo(v), bfhi(v)}; }
DEV u32 pkbf2(f32x2 v) { return __builtin_bit_cast(u32, __builtin_convertvector(v, bf16x2)); }
DEV u32 pkbf(float a, float b) { return pkbf2(f32x2{a, b}); }
DEV u32 pkh(float a, float b) { f32x2 v = {a, b}; return __builtin_bit_cast(u32, __builtin_convertvector(v, f16x2)); }
DEV float rbf(float a) { return bflo(pkbf(a, a)); }
DEV f32x2 hup(u32 v) { return __builtin_convertvector(__builtin_bit_cast(f16x2, v), f32x2); }

template <int CTRL> DEV float dppf(float v) {
  return __builtin_bit_cast(float, __builtin_amdgcn_update_dpp(0, __builtin_bit_cast(int, v), CTRL, 0xf, 0xf, true));
}
DEV float gsum(float v) {
  u32 a = __float_as_uint(v);
  auto r = __builtin_amdgcn_permlane16_swap(a, a, false, false);
  v = __uint_as_float(r[0]) + __uint_as_float(r[1]);
  a = __float_as_uint(v);
  auto q = __builtin_amdgcn_permlane32_swap(a, a, false, false);
  return __uint_as_float(q[0]) + __uint_as_float(q[1]);
}
DEV float gmax(float v) {
  u32 a = __float_as_uint(v);
  auto r = __builtin_amdgcn_permlane16_swap(a, a, false, false);
  v = fmaxf(__uint_as_float(r[0]), __uint_as_float(r[1]));
  a = __float_as_uint(v);
  auto q = __builtin_amdgcn_permlane32_swap(a, a, false, false);
  return fmaxf(__uint_as_float(q[0]), __uint_as_float(q[1]));
}
DEV float wave_sum(float v) {
  v += dppf<0xB1>(v); v += dppf<0x4E>(v); v += dppf<0x141>(v); v += dppf<0x140>(v);
  return gsum(v);
}
DEV float wave_max(float v) {
  v = fmaxf(v, dppf<0xB1>(v)); v = fmaxf(v, dppf<0x4E>(v)); v = fmaxf(v, dppf<0x141>(v)); v = fmaxf(v, dppf<0x140>(v));
  return gmax(v);
}
DEV u32x2 tr_read(const char* p) {
  return __builtin_bit_cast(u32x2, __builtin_amdgcn_ds_read_tr16_b64_v4i16((LDSP s16x4*)p));
}
DEV void mma(f32x4& c, u32x4 a, u32x4 b) { asm volatile("v_mfma_f32_16x16x32_bf16 %0, %1, %2, %0" : "+v"(c) : "v"(a), "v"(b)); }
DEV void mma0(f32x4& c, u32x4 a, u32x4 b) { asm volatile("v_mfma_f32_16x16x32_bf16 %0, %1, %2, 0" : "=&v"(c) : "v"(a), "v"(b)); }
DEV void mmah(f32x4& c, u32x4 a, u32x4 b) { asm volatile("v_mfma_f32_16x16x32_f16 %0, %1, %2, %0" : "+v"(c) : "v"(a), "v"(b)); }
DEV void mmah0(f32x4& c, u32x4 a, u32x4 b) { asm volatile("v_mfma_f32_16x16x32_f16 %0, %1, %2, 0" : "=&v"(c) : "v"(a), "v"(b)); }
DEV void mmaz(bool first, f32x4& c, u32x4 a, u32x4 b) { if (first) mma0(c, a, b); else mma(c, a, b); }
DEV void mfence() { asm volatile("s_nop 7" ::: "memory"); __builtin_amdgcn_sched_barrier(0); }
DEV float smul(float a, float b) { float r; asm("v_mul_f32 %0, %1, %2" : "=v"(r) : "v"(a), "v"(b)); return r; }
DEV float sfma(float a, float b, float c) { float r; asm("v_fma_f32 %0, %1, %2, %3" : "=v"(r) : "v"(a), "v"(b), "v"(c)); return r; }
DEV float sfmak(float a, float k1, float k0) { float r; asm("v_fma_f32 %0, %1, %2, %3" : "=v"(r) : "v"(a), "s"(k1), "v"(k0)); return r; }
DEV float sadd1(float a) { float r; asm("v_add_f32 %0, 1.0, %1" : "=v"(r) : "v"(a)); return r; }
DEV void pin(f32x4& c) { asm volatile("" : "+v"(c)); }
DEV float dot2(u32 a, u32 b, float c) {
  return __builtin_amdgcn_fdot2_f32_bf16(__builtin_bit_cast(bf16x2, a), __builtin_bit_cast(bf16x2, b), c, false);
}
template <int LOFF, int MOFF> DEV void glds(u32 voff, const void* sbase, u32 lds) {
  asm volatile("s_add_u32 m0, %1, %3\n\ts_nop 0\n\tglobal_load_lds_dwordx4 %0, %2"
               :: "v"(voff + (u32)MOFF), "s"(lds), "s"(sbase), "n"(LOFF) : "memory", "m0", "scc");
}
template <int LOFF, int MOFF> DEV void gldsd(u32 voff, const void* sbase, u32 lds) {
  asm volatile("s_add_u32 m0, %1, %3\n\ts_nop 0\n\tglobal_load_lds_dwordx4 %0, %2 sc1"
               :: "v"(voff + (u32)MOFF), "s"(lds), "s"(sbase), "n"(LOFF) : "memory", "m0", "scc");
}
DEV void touch(u32& t, const void* sbase, u32 voff) {
  asm volatile("global_load_dword %0, %1, %2" : "+v"(t) : "v"(voff), "s"(sbase) : "memory");
}
DEV void keep(u32 t) { asm volatile("" :: "v"(t)); }
DEV u32 lds_addr(const char* p) { return (u32)(size_t)(LDSP const char*)p; }
#define SCHED() __builtin_amdgcn_sched_barrier(0)
template <int N> DEV void wait_vm() { asm volatile("s_waitcnt vmcnt(%0)" ::"n"(N) : "memory"); }
template <int N> DEV void wait_vm_lgkm() { asm volatile("s_waitcnt vmcnt(%0) lgkmcnt(0)" ::"n"(N) : "memory"); }
DEV void bar() { __builtin_amdgcn_s_barrier(); }
DEV void lds_bar() { asm volatile("s_waitcnt lgkmcnt(0)" ::: "memory"); __builtin_amdgcn_s_barrier(); }

#define STAMP(i)

typedef __amdgpu_buffer_rsrc_t rsrc_t;
DEV void st16_dev(rsrc_t r, u32 off, u32x4 v) { __builtin_amdgcn_raw_buffer_store_b128(v, r, off, 0, 16); }
DEV void st8_dev(rsrc_t r, u32 off, u32x2 v) { __builtin_amdgcn_raw_buffer_store_b64(v, r, off, 0, 16); }
DEV u32x4 ld16_dev(rsrc_t r, u32 off) { return __builtin_amdgcn_raw_buffer_load_b128(r, off, 0, 16); }
DEV u32x2 ld8_dev(rsrc_t r, u32 off) { return __builtin_amdgcn_raw_buffer_load_b64(r, off, 0, 16); }

constexpr int SPIN_MAX = 1 << 18;
DEV void poll256(rsrc_t r, u32 base, u32x2 tag, int lane) {
#pragma unroll 1
  for (int s = 0; s < SPIN_MAX; s++) {
    u32x4 a = ld16_dev(r, base + lane * 32), b = ld16_dev(r, base + lane * 32 + 16);
    bool ok = a[0] == tag[0] && a[1] == tag[1] && a[2] == tag[0] && a[3] == tag[1] &&
              b[0] == tag[0] && b[1] == tag[1] && b[2] == tag[0] && b[3] == tag[1];
    if (__all(ok)) break;
    __builtin_amdgcn_s_sleep(2);
    asm volatile("" ::: "memory");
  }
}
DEV void polln(rsrc_t r, u32 base, int lo, int hi, u32x2 tag, int lane) {
  const int i = min(lo + lane, hi - 1);
#pragma unroll 1
  for (int s = 0; s < SPIN_MAX; s++) {
    u32x2 a = ld8_dev(r, base + i * 8);
    if (__all(a[0] == tag[0] && a[1] == tag[1])) break;
    __builtin_amdgcn_s_sleep(2);
    asm volatile("" ::: "memory");
  }
}

DEV u32x2 act4(f32x4 a, f32x4 cs, f32x4 rt) {
  const float c0 = -2.0f * 0.7978845608028654f * 1.4426950408889634f;
  const float c1 = c0 * 0.044715f;
  const f32x2 rs = {rt[0], rt[1]}, t = {rt[2], rt[3]};
  f32x2 v0 = f32x2{a[0], a[1]} * rs + f32x2{cs[0], cs[1]} * t, v1 = f32x2{a[2], a[3]} * rs + f32x2{cs[2], cs[3]} * t;
  f32x4 v = {v0[0], v0[1], v1[0], v1[1]};
  f32x4 x = v;
  f32x4 z = x * (x * x * c1 + c0);
  f32x4 e = {__builtin_amdgcn_exp2f(z[0]), __builtin_amdgcn_exp2f(z[1]), __builtin_amdgcn_exp2f(z[2]), __builtin_amdgcn_exp2f(z[3])};
  e += 1.0f;
  f32x4 r = {__builtin_amdgcn_rcpf(e[0]), __builtin_amdgcn_rcpf(e[1]), __builtin_amdgcn_rcpf(e[2]), __builtin_amdgcn_rcpf(e[3])};
  x *= r;
  return u32x2{pkbf(x[0], x[1]), pkbf(x[2], x[3])};
}

constexpr int LDS_ALL = 163840;
constexpr int KG_ROW = 528;
constexpr int KG_CH = 73 * KG_ROW;
constexpr int P0_KT = 77312;
constexpr int P0_LN = 101888;
constexpr int P0_MS = 119296;
constexpr int CV_COPY = 4160, CV_KP = 4 * CV_COPY, CV_CH = CV_KP + 4608;
constexpr int CV_UI = 2 * CV_CH;
constexpr int CV_RED = CV_UI + 16384;
constexpr int CV_STG = 0;
constexpr int GA_A = 0, GA_TAB = 65536, GA_B = 98304;
constexpr int G3_ST = 73728, G3_TAB = 90112;
constexpr int G4_B = 65536;

struct KTbl {
  u16 v[2048];
  constexpr KTbl() : v() {
    for (int pos = 0; pos < 2048; pos++) {
      int l = (pos & ~7) + 7 - (pos & 7);
      int sh = l < 256 ? 0 : l < 512 ? 1 : l < 1024 ? 2 : 3;
      int lv = sh ? (((l + (1 << (sh - 1)) - 1 + ((l >> sh) & 1)) >> sh) << sh) : l;
      int s = lv < 256 ? 0 : lv < 512 ? 1 : lv < 1024 ? 2 : 3;
      int rr = 8 * s + ((lv >> 7) << (3 - s)) + ((lv & 7) >> s);
      v[pos] = (u16)((rr * 16 + ((lv >> 3) & 15)) * 16);
    }
  }
};
__device__ __attribute__((aligned(16))) constexpr KTbl KTBL{};

typedef f32x2 cf;
DEV cf cmul(cf a, cf b) {
  cf r;
  asm("v_pk_mul_f32 %0, %1, %2 op_sel:[1,1] op_sel_hi:[1,0]\n\t"
      "v_pk_fma_f32 %0, %1, %2, %0 op_sel:[0,0,0] op_sel_hi:[0,1,1] neg_lo:[0,0,1]"
      : "=&v"(r) : "v"(a), "v"(b));
  return r;
}
DEV cf cscale(cf a, float s) { return a * s; }
DEV cf cexpk(float tl2, float rh, float rl, float s) {
  float mag = __builtin_amdgcn_exp2f(tl2 * s);
  float p = rh * s;
  float f = __builtin_fmaf(rl, s, p - __builtin_rintf(p));
  return cf{__builtin_amdgcn_cosf(f), __builtin_amdgcn_sinf(f)} * mag;
}
DEV void st_x(char* p, cf a) { *(u32*)p = pkh(a[0], a[1]); }
DEV void st_y(char* p, cf a) {
  u32 hi = pkh(a[0], a[1]);
  f32x2 r = a - hup(hi);
  *(u32*)p = hi;
  *(u32*)(p + 16 * KG_ROW) = pkh(r[0], r[1]);
}
DEV float pow2_scale(float mx, u32 target, float& inv) {
  u32 eb = __float_as_uint(mx) >> 23;
  u32 se = min(max(254u + target - eb, 1u), 253u);
  inv = __uint_as_float((254u - se) << 23);
  return __uint_as_float(se << 23);
}

DEV void conv_load1(const char* pa, const char* pb, int b0, int i, u32x4 (&A)[8], u32x4 (&B)[8]) {
  u32x2 lo = *(const volatile LDSP u32x2*)(pa + 64 * b0 + 512 * i), hi = *(const volatile LDSP u32x2*)(pa + 64 * b0 + 512 * i + 8);
  A[i] = u32x4{lo[0], lo[1], hi[0], hi[1]};
  B[i] = *(const u32x4*)(pb - 64 * b0 + 512 * i);
}
DEV void conv_step(const char* pa, const char* pb, int nb, const u32x4 (&A)[8], const u32x4 (&B)[8], u32x4 (&An)[8], u32x4 (&Bn)[8], f32x4 (&acc)[8]) {
  u32x2 lo[8], hi[8];
  int c = 0;
#pragma unroll
  for (int i = 0; i < 8; i++) {
#pragma unroll
    for (int t = i; t < 8; t++) {
      if (nb == 1 && i == 0) mmah0(acc[t], A[i], B[t - i]); else mmah(acc[t], A[i], B[t - i]);
      if (nb < 4 && c < 24) {
        const int f = c / 3, k = c % 3;
        if (k == 0) Bn[f] = *(const u32x4*)(pb - 64 * nb + 512 * f);
        else if (k == 1) lo[f] = *(const volatile LDSP u32x2*)(pa + 64 * nb + 512 * f);
        else hi[f] = *(const volatile LDSP u32x2*)(pa + 64 * nb + 512 * f + 8);
      }
      c++;
      SCHED();
    }
  }
  if (nb < 4) {
#pragma unroll
    for (int f = 0; f < 8; f++) An[f] = u32x4{lo[f][0], lo[f][1], hi[f][0], hi[f][1]};
  }
}

DEV u32 kn_voff(int ld, int w, int lane) {
  int kr = lane >> 3, pos = lane & 7;
  int f2 = ((lane >> 4) & 1) | ((w & 1) << 1);
  return (u32)((8 * w + kr) * ld * 2 + ((pos ^ (f2 << 1)) << 4));
}
DEV u32 mk_voff(int ld, int lane) { int r = lane >> 3, pos = lane & 7; return (u32)(r * ld * 2 + ((pos ^ r) << 4)); }
DEV int kn_base(int lane) {
  int g = lane >> 4, q = (lane & 15) >> 2, pp = lane & 3;
  int f2 = ((q >> 1) & 1) | ((g & 1) << 1);
  return (8 * g + q) * 128 + (f2 << 5) + pp * 8;
}
DEV u32x4 kn_frag(const char* p) { u32x2 a = tr_read(p), b = tr_read(p + 512); return u32x4{a[0], a[1], b[0], b[1]}; }
DEV int mk_base(int lane, int ks) { int r16 = lane & 15, g = lane >> 4; return r16 * 128 + (((4 * ks + g) ^ (r16 & 7)) << 4); }
constexpr u32 ONES = 0x3f803f80u;
DEV u32x4 pair_swap(u32 p01, u32 p23, u32 q01, u32 q23) {
  auto r0 = __builtin_amdgcn_permlane16_swap(p01, q01, false, false);
  auto r1 = __builtin_amdgcn_permlane16_swap(p23, q23, false, false);
  return u32x4{r0[0], r1[0], r0[1], r1[1]};
}
template <int LDW> DEV void w64_issue(const u16* wsrc, u32 dst, int w, int lane) {
  const u32 vo = kn_voff(LDW, w, lane);
  const u32 d = dst + w * 1024;
  glds<0 * 8192, 0 * 128 * LDW>(vo, wsrc, d); glds<1 * 8192, 1 * 128 * LDW>(vo, wsrc, d);
  glds<2 * 8192, 2 * 128 * LDW>(vo, wsrc, d); glds<3 * 8192, 3 * 128 * LDW>(vo, wsrc, d);
  glds<4 * 8192, 4 * 128 * LDW>(vo, wsrc, d); glds<5 * 8192, 5 * 128 * LDW>(vo, wsrc, d);
  glds<6 * 8192, 6 * 128 * LDW>(vo, wsrc, d); glds<7 * 8192, 7 * 128 * LDW>(vo, wsrc, d);
}
DEV void w64_regs(const char* img, int wn, int lane, u32x4 (&B)[16][2]) {
  const char* p0 = img + (kn_base(lane) ^ ((2 * wn) << 5));
  const char* p1 = img + (kn_base(lane) ^ ((2 * wn + 1) << 5));
#pragma unroll
  for (int s = 0; s < 16; s++) { B[s][0] = kn_frag(p0 + s * 4096); B[s][1] = kn_frag(p1 + s * 4096); }
}
template <int LDW> DEV void w64_issue4(const u16* wsrc, u32 dst, int w, int lane) {
  w64_issue<LDW>(wsrc, dst, w - 4, lane); w64_issue<LDW>(wsrc, dst, w, lane);
}
template <int H> DEV void w3_issue(const u16* sb, u32 lds0, int w, int lane) {
  constexpr int CB3 = 128 * F_;
  const u32 vb = kn_voff(F_, w, lane), dw = lds0 + GA_B + w * 1024;
  glds<0, (4 * H) * CB3>(vb, sb, dw); glds<8192, (4 * H) * CB3 + 128>(vb, sb, dw);
  glds<16384, (4 * H + 1) * CB3>(vb, sb, dw); glds<24576, (4 * H + 1) * CB3 + 128>(vb, sb, dw);
  glds<32768, (4 * H + 2) * CB3>(vb, sb, dw); glds<40960, (4 * H + 2) * CB3 + 128>(vb, sb, dw);
  glds<49152, (4 * H + 3) * CB3>(vb, sb, dw); glds<57344, (4 * H + 3) * CB3 + 128>(vb, sb, dw);
}

__global__ __launch_bounds__(512, 2) void k_fused(Params p) {
  __shared__ __attribute__((aligned(16))) char smem[LDS_ALL];
  const int bid = blockIdx.x;
  const int tid = threadIdx.x, w = __builtin_amdgcn_readfirstlane(tid >> 6), lane = tid & 63;
  const int r16 = lane & 15, g = lane >> 4;
  const u32 lds0 = lds_addr(smem);
  const rsrc_t wsr = __builtin_amdgcn_make_buffer_rsrc(p.ws, 0, 0x4000000, 0x00020000);
  STAMP(0);
  const int cp = (bid & 7) * 32 + (bid >> 3);
  const int pm = (bid & 7) * 4 + (bid >> 6), pn = (bid >> 3) & 7;
  const int row0 = pm * 64, col0 = pn * 64;
  u32x2 tag;
  u32 TKW = 0;
  {
    u32x2 ep = ld8_dev(wsr, FL_EPOCH);
    u32x4 xv = *(const u32x4*)(p.x + (bid * 8 + w) * D_ + lane * 8);
    const int chl = w >> 2, role = (w >> 1) & 1, n = tid & 127;
    const int ch = cp * 2 + chl;
    const int pi = ch * NS_ + n;
    u32 r_are = p.are[pi], r_aim = p.aim[pi], r_bre = p.bre[pi], r_bim = p.bim[pi], r_cre = p.cre[pi], r_cim = p.cim[pi];
    u32 r_ldt = p.logdt[ch];
    u32x4 kidx = *(const u32x4*)(KTBL.v + (tid & 255) * 8);
    SCHED();
    u32 sk0, sk1, sk2;
    {
      u64 pc; u32 len;
      asm volatile("s_getpc_b64 %0\n.Lk_here:\n\ts_mov_b32 %1, .Lk_end-.Lk_here" : "=s"(pc), "=s"(len));
      const u32 o = (u32)((bid >> 3) * 24 + w * 3) * 64;
      asm volatile("s_load_dword %0, %3, %4\n\ts_load_dword %1, %3, %5\n\ts_load_dword %2, %3, %6"
                   : "=&s"(sk0), "=&s"(sk1), "=&s"(sk2) : "s"(pc), "s"(min(o, len)), "s"(min(o + 64, len)), "s"(min(o + 128, len)) : "memory");
    }
    {
      u64 e = ((u64)ep[1] << 32 | ep[0]) + 1;
      tag = u32x2{(u32)__builtin_amdgcn_readfirstlane((u32)e), (u32)__builtin_amdgcn_readfirstlane((u32)(e >> 32))};
    }
    char* su = smem + P0_LN;
    float* ms = (float*)(smem + P0_MS);
    {
      f32x2 v[4];
#pragma unroll
      for (int i = 0; i < 4; i++) v[i] = bfup(xv[i]);
      f32x2 s2 = (v[0] + v[1]) + (v[2] + v[3]);
      float mean = wave_sum(s2[0] + s2[1]) * (1.0f / D_);
      f32x2 q2 = {0, 0};
#pragma unroll
      for (int i = 0; i < 4; i++) { f32x2 d = v[i] - mean; q2 += d * d; }
      float var = wave_sum(q2[0] + q2[1]) * (1.0f / D_);
      float m = rbf(mean);
      float inv = __builtin_amdgcn_rcpf(rbf(__builtin_amdgcn_sqrtf(rbf(rbf(var) + 1e-5f))));
      u32x4 uo;
#pragma unroll
      for (int i = 0; i < 4; i++) {
        f32x2 u = bfup(pkbf2(bfup(pkbf2(v[i] - m)) * inv));
        uo[i] = pkh(u[0], u[1]);
      }
      u32x4 lo4, hi4;
#pragma unroll
      for (int i = 0; i < 2; i++) {
        lo4[2 * i] = __builtin_amdgcn_perm(xv[i], uo[i], 0x05040100u); lo4[2 * i + 1] = __builtin_amdgcn_perm(xv[i], uo[i], 0x07060302u);
        hi4[2 * i] = __builtin_amdgcn_perm(xv[2 + i], uo[2 + i], 0x05040100u); hi4[2 * i + 1] = __builtin_amdgcn_perm(xv[2 + i], uo[2 + i], 0x07060302u);
      }
      *(u32x4*)(su + w * 2112 + lane * 32) = lo4;
      *(u32x4*)(su + w * 2112 + lane * 32 + 16) = hi4;
    }
    asm volatile("" : "+v"(kidx));
    STAMP(1);
    lds_bar();
    asm volatile("" :: "s"(sk0), "s"(sk1), "s"(sk2));
    {
      const int q4 = (lane & 15) >> 2, pp = lane & 3;
      const char* a = su + q4 * 2112 + (128 * w + 16 * g + 4 * pp) * 2;
      u32x2 u0 = tr_read(a), u1 = tr_read(a + 4 * 2112), x0 = tr_read(a + 128), x1 = tr_read(a + 4 * 2112 + 128);
      const u32 dst = WS_U8 + bid * 16384 + w * 2048 + lane * 16;
      st16_dev(wsr, dst, u32x4{u0[0], u0[1], u1[0], u1[1]});
      st16_dev(wsr, dst + 1024, u32x4{x0[0], x0[1], x1[0], x1[1]});
    }
    float dt = rbf(expf(bf2f(r_ldt)));
    float ar = dt * bf2f(r_are), ai = dt * bf2f(r_aim);
    cf CB = cmul(cf{bf2f(r_cre), bf2f(r_cim)}, cf{dt * bf2f(r_bre), dt * bf2f(r_bim)});
    float tl2 = ar * 1.4426950408889634f;
    const float I2PH = 0.15915494309189535f;
    const float I2PL = (float)(0.15915494309189533576888 - (double)0.15915494309189535f);
    float rh = ai * I2PH;
    float rl = __builtin_fmaf(ai, I2PH, -rh) + ai * I2PL;
    {
      float bs = wave_sum(fabsf(CB[0]) + fabsf(CB[1])), isx;
      float sx = pow2_scale(bs, 14, isx);
      CB = cscale(CB, sx);
      if (role == 0 && lane == 0) { ms[chl * 2 + (w & 1)] = bs; ms[4 + chl * 2 + (w & 1)] = isx; }
    }
    {
      const u16* wb = w == 0 ? p.w3 : w == 1 ? p.w4 : w == 2 ? p.w1 : p.w2;
      touch(TKW, wb, w < 2 ? (u32)(bid * 56 + min(lane, 55)) * 128 : (u32)(bid * 16 + (lane & 15)) * 128);
    }
    STAMP(2);
    char* img = smem + chl * KG_CH;
    {
      char* wp = img + n * 4;
      if (role == 0) {
        cf e3 = cexpk(tl2, -rh, -rl, 8.f), e4 = cexpk(tl2, -rh, -rl, 16.f), e5 = cexpk(tl2, -rh, -rl, 32.f), e6 = cexpk(tl2, -rh, -rl, 64.f);
        cf y[8];
        y[0] = cf{16384.f, 0.f}; y[1] = cscale(e3, 16384.f); y[2] = cscale(e4, 16384.f); y[3] = cmul(y[2], e3);
        y[4] = cscale(e5, 16384.f); y[5] = cmul(y[4], e3); y[6] = cmul(y[4], e4); y[7] = cmul(y[6], e3);
#pragma unroll
        for (int j = 0; j < 8; j++) st_y(wp + (41 + j) * KG_ROW, y[j]);
#pragma unroll
        for (int j = 0; j < 8; j++) st_y(wp + (49 + j) * KG_ROW, j ? cmul(y[j], e6) : cscale(e6, 16384.f));
        cf e7 = cexpk(tl2, rh, rl, 128.f), e8 = cexpk(tl2, rh, rl, 256.f), e9 = cexpk(tl2, rh, rl, 512.f);
        cf e10 = cexpk(tl2, rh, rl, 1024.f), e11 = cexpk(tl2, rh, rl, 2048.f);
        cf g8 = cmul(CB, e10), g12 = cmul(g8, e9), g16 = cmul(CB, e11);
        cf g10 = cmul(g8, e8), g14 = cmul(g12, e8);
        st_x(wp + 32 * KG_ROW, g8); st_x(wp + 33 * KG_ROW, cmul(g8, e7));
        st_x(wp + 34 * KG_ROW, g10); st_x(wp + 35 * KG_ROW, cmul(g10, e7));
        st_x(wp + 36 * KG_ROW, g12); st_x(wp + 37 * KG_ROW, cmul(g12, e7));
        st_x(wp + 38 * KG_ROW, g14); st_x(wp + 39 * KG_ROW, cmul(g14, e7));
        st_x(wp + 40 * KG_ROW, g16);
      } else {
        cf e0 = cexpk(tl2, rh, rl, 1.f), e1 = cexpk(tl2, rh, rl, 2.f), e2 = cexpk(tl2, rh, rl, 4.f);
        cf e7 = cexpk(tl2, rh, rl, 128.f), e8 = cexpk(tl2, rh, rl, 256.f), e9 = cexpk(tl2, rh, rl, 512.f);
        cf eb[8];
        eb[1] = e0; eb[2] = e1; eb[3] = cmul(e1, e0); eb[4] = e2; eb[5] = cmul(e2, e0); eb[6] = cmul(e2, e1); eb[7] = cmul(eb[6], e0);
        cf gq[8];
        gq[0] = CB; gq[1] = cmul(CB, e7); gq[2] = cmul(CB, e8); gq[3] = cmul(gq[2], e7);
        gq[4] = cmul(CB, e9); gq[5] = cmul(gq[4], e7); gq[6] = cmul(gq[4], e8); gq[7] = cmul(gq[6], e7);
#pragma unroll
        for (int q = 0; q < 2; q++)
#pragma unroll
          for (int b = 0; b < 8; b++) st_x(wp + (q * 8 + b) * KG_ROW, b ? cmul(gq[q], eb[b]) : gq[q]);
#pragma unroll
        for (int q = 2; q < 4; q++)
#pragma unroll
          for (int b = 0; b < 4; b++) st_x(wp + (16 + (q - 2) * 4 + b) * KG_ROW, b ? cmul(gq[q], eb[2 * b]) : gq[q]);
#pragma unroll
        for (int q = 4; q < 8; q++)
#pragma unroll
          for (int b = 0; b < 2; b++) st_x(wp + (24 + (q - 4) * 2 + b) * KG_ROW, b ? cmul(gq[q], e2) : gq[q]);
      }
    }
    STAMP(3);
    wait_vm_lgkm<1>(); bar();
    if (tid < 16) st8_dev(wsr, FL_1 + tid * 2048 + bid * 8, tag);
    char* kt = smem + P0_KT + chl * 12288;
    float kinv;
    const float kscale = pow2_scale(ms[chl * 2] + ms[chl * 2 + 1], 14, kinv);
    const float fsc = ms[4 + chl * 2 + ((w & 3) >> 1)] * (kscale * (1.0f / 16384.f));
    {
      const int wv = w & 3;
      const char* pa = img + r16 * KG_ROW + wv * 128 + g * 16;
      u32x4 a[3][2], yh[2], yl[2];
#pragma unroll
      for (int s = 0; s < 2; s++) {
#pragma unroll
        for (int t = 0; t < 3; t++) a[t][s] = *(const u32x4*)(pa + t * 16 * KG_ROW + s * 64);
        yh[s] = *(const u32x4*)(pa + 41 * KG_ROW + s * 64);
        yl[s] = *(const u32x4*)(pa + 57 * KG_ROW + s * 64);
      }
      const float f = fsc;
      char* kw = kt + (4 * g * 16 + r16) * 16 + ((wv + (r16 >> 3) + 2 * (g & 1)) & 3) * 4;
      f32x4 c[3];
#pragma unroll
      for (int t = 0; t < 3; t++) { mmah0(c[t], a[t][0], yl[0]); mmah(c[t], a[t][1], yl[1]); }
#pragma unroll
      for (int t = 0; t < 3; t++) { mmah(c[t], a[t][0], yh[0]); mmah(c[t], a[t][1], yh[1]); }
      mfence(); pin(c[0]); pin(c[1]); pin(c[2]);
#pragma unroll
      for (int t = 0; t < 3; t++)
#pragma unroll
        for (int r = 0; r < 4; r++) *(float*)(kw + (t * 16 + r) * 256) = c[t][r] * f;
    }
    if (w == 0) {
      poll256(wsr, FL_1 + (bid & 15) * 2048, tag, lane);
      if (bid == 0 && lane == 0) st8_dev(wsr, FL_EPOCH, tag);
    }
    lds_bar();
    STAMP(4);
    {
      const char* sa = p.ws + WS_U8 + (size_t)w * 16 * 16384 + cp * 64;
      const u32 vo = (u32)((lane >> 2) * 16384 + (lane & 3) * 16);
      gldsd<CV_UI, 0>(vo, sa, lds0 + w * 1024); gldsd<CV_UI + 8192, 128 * 16384>(vo, sa, lds0 + w * 1024);
    }
    {
      u32x4 o;
#pragma unroll
      for (int i = 0; i < 4; i++) {
        f32x4 a = *(const f32x4*)(kt + (kidx[i] & 0xffffu)), b = *(const f32x4*)(kt + (kidx[i] >> 16));
        o[i] = pkh((a[0] + a[1]) + (a[2] + a[3]), (b[0] + b[1]) + (b[2] + b[3]));
      }
      const int e = tid & 255;
      char* base = smem + chl * CV_CH;
      *(u32x4*)(base + CV_KP + (30 + e) * 16) = o;
      if (e < 30) *(u32x4*)(base + CV_KP + e * 16) = u32x4{0, 0, 0, 0};
      else if (e < 34) *(u32x4*)(base + (e - 30) * CV_COPY) = u32x4{0, 0, 0, 0};
      else if (e == 34) *(float*)(base + CV_KP + 4576) = kinv;
    }
  }
  STAMP(5);
  STAMP(6);
  {
    {
      const int chl = w >> 2, q = tid & 255;
      char* base = smem + chl * CV_CH;
      wait_vm<0>(); keep(TKW); bar();
      const char* ui = smem + CV_UI + q * 64 + chl * 32;
      u32x4 cur = *(const u32x4*)ui, prv = *(const u32x4*)(ui - 64);
      if (q == 0) prv = u32x4{0, 0, 0, 0};
      u32 wd[8] = {prv[0], prv[1], prv[2], prv[3], cur[0], cur[1], cur[2], cur[3]};
      u32 o[5];
#pragma unroll
      for (int k = 0; k < 5; k++) o[k] = __builtin_amdgcn_alignbit(wd[k + 1], wd[k], 16);
      char* d = base + (q + 1) * 16;
      *(u32x4*)(d) = u32x4{o[0], o[1], o[2], o[3]};
      *(u32x4*)(d + CV_COPY) = u32x4{wd[1], wd[2], wd[3], wd[4]};
      *(u32x4*)(d + 2 * CV_COPY) = u32x4{o[1], o[2], o[3], o[4]};
      *(u32x4*)(d + 3 * CV_COPY) = u32x4{wd[2], wd[3], wd[4], wd[5]};
      if (q == 255) {
        u32 o4 = __builtin_amdgcn_alignbit(cur[1], cur[0], 16), o5 = __builtin_amdgcn_alignbit(cur[2], cur[1], 16);
        u32 o6 = __builtin_amdgcn_alignbit(cur[3], cur[2], 16), o7 = cur[3] >> 16;
        char* d2 = base + 257 * 16;
        *(u32x4*)(d2) = u32x4{o4, o5, o6, o7};
        *(u32x4*)(d2 + CV_COPY) = u32x4{cur[1], cur[2], cur[3], 0};
        *(u32x4*)(d2 + 2 * CV_COPY) = u32x4{o5, o6, o7, 0};
        *(u32x4*)(d2 + 3 * CV_COPY) = u32x4{cur[2], cur[3], 0, 0};
      }
    }
    const int chl = (w >> 1) & 1, hh = w & 1;
    const float dsk = bf2f(p.dskip[cp * 2 + chl]);
    const char* base = smem + chl * CV_CH;
    const int j2 = 2 * (hh + 2 * (w >> 2));
    char* red = smem + CV_RED + chl * 16384 + lane * 16;
    lds_bar();
    STAMP(7);
    f32x4 f0 = {0, 0, 0, 0}, f1 = {0, 0, 0, 0};
    if (w < 4) {
      f32x4 acc[8];
      const char* pa = base + (r16 & 3) * CV_COPY + 48 - 16 * g + 2 * (r16 & 12) + 256 * hh;
      const char* pb = base + CV_KP + 448 + 32 * r16 + 16 * g - 256 * hh;
      u32x4 A0[8], B0[8], A1[8], B1[8];
#pragma unroll
      for (int i = 0; i < 8; i++) conv_load1(pa, pb, 0, i, A0, B0);
      conv_step(pa, pb, 1, A0, B0, A1, B1, acc);
      conv_step(pa, pb, 2, A1, B1, A0, B0, acc);
      conv_step(pa, pb, 3, A0, B0, A1, B1, acc);
      conv_step(pa, pb, 4, A1, B1, A0, B0, acc);
      mfence();
#pragma unroll
      for (int i = 0; i < 8; i++) pin(acc[i]);
      STAMP(27);
      char* d = red + hh * 8192;
      if (hh) { *(f32x4*)(d) = acc[0]; *(f32x4*)(d + 1024) = acc[1]; f0 = acc[2]; f1 = acc[3]; }
      else { *(f32x4*)(d + 2048) = acc[2]; *(f32x4*)(d + 3072) = acc[3]; f0 = acc[0]; f1 = acc[1]; }
#pragma unroll
      for (int t = 4; t < 8; t++) *(f32x4*)(d + t * 1024) = acc[t];
    } else {
      w64_issue<D_>(p.w1 + col0, lds0 + GA_B, w - 4, lane);
      w64_issue<D_>(p.w1 + col0, lds0 + GA_B, w, lane);
      wait_vm<0>();
    }
    const float kinv = *(const float*)(base + CV_KP + 4576);
    const int l0 = 256 * j2 + 16 * r16 + 4 * g;
    u32x2 xx[2];
    u32 dd[2][2];
#pragma unroll
    for (int k = 0; k < 2; k++) {
      const char* ui = smem + CV_UI + ((l0 + 256 * k) >> 3) * 64 + chl * 32 + (l0 & 4) * 2;
      u32x2 uu = *(const u32x2*)ui;
      xx[k] = *(const u32x2*)(ui + 16);
      dd[k][0] = pkbf2(hup(uu[0]) * dsk); dd[k][1] = pkbf2(hup(uu[1]) * dsk);
    }
    lds_bar();
    STAMP(8);
    if (w < 4) {
      f0 += *(const f32x4*)(red + (hh ^ 1) * 8192 + j2 * 1024);
      f1 += *(const f32x4*)(red + (hh ^ 1) * 8192 + j2 * 1024 + 1024);
    } else {
      f0 = *(const f32x4*)(red + j2 * 1024) + *(const f32x4*)(red + 8192 + j2 * 1024);
      f1 = *(const f32x4*)(red + j2 * 1024 + 1024) + *(const f32x4*)(red + 8192 + j2 * 1024 + 1024);
    }
    char* stg = smem + CV_STG;
#pragma unroll
    for (int k = 0; k < 2; k++) {
      f32x4 f = k ? f1 : f0;
      u32 y01 = pkbf2(f32x2{f[0], f[1]} * kinv), y23 = pkbf2(f32x2{f[2], f[3]} * kinv);
      u32 s01 = pkbf2(bfup(y01) + bfup(dd[k][0])), s23 = pkbf2(bfup(y23) + bfup(dd[k][1]));
      u32 h01 = pkbf2(bfup(xx[k][0]) + bfup(s01)), h23 = pkbf2(bfup(xx[k][1]) + bfup(s23));
      *(u32x2*)(stg + (16 * (j2 + k) + r16) * 80 + chl * 32 + g * 8) = u32x2{h01, h23};
    }
    lds_bar();
    {
      const int lb = tid >> 2, pc = tid & 3;
      st16_dev(wsr, WS_HT + cp * 8192 + tid * 16, *(const u32x4*)(stg + lb * 80 + pc * 16));
    }
  }
  const int mt = w & 3, wn = w >> 2;
  const int rt = (pm >> 1) * 8 + (pm & 1) * 2 + (mt >> 1) * 4 + (mt & 1);
  const int pc4 = (g & 1) * 2 + (g >> 1);
  const u32 boff = (u32)((rt * 16 + pn * 2 + wn) * 1024 + r16 * 64 + pc4 * 16);
  u32x4 ONES4 = {ONES, ONES, ONES, ONES};
  asm volatile("s_nop 1" : "+v"(ONES4));
  const u32 dw = lds0 + w * 1024;
  const u32 vbD = kn_voff(D_, w, lane);
  u32x4 hres, xres;
  u32x4 B[16][2];
  constexpr int CBD = 128 * D_, CB3 = 128 * F_;
  const int idx3 = bid >> 3;
  const bool has3 = idx3 < 28;
  const int pm3 = (bid & 7) * 2 + (idx3 >= 14 ? 1 : 0), pn3 = idx3 >= 28 ? idx3 - 28 : idx3 >= 14 ? idx3 - 14 : idx3;
  const u16* sb3 = p.w3 + pn3 * 128;
  const u32 vbF = kn_voff(F_, w, lane);
  const u32 vbF4 = kn_voff(F_, w - 4, lane);
  const int wm3 = w & 1, wn3 = w >> 1;
  const int ob0 = (kn_base(lane) + (wn3 >> 1) * 8192) ^ ((2 * (wn3 & 1)) << 5), ob1 = ob0 ^ 32;
  f32x4 cw0, cw1;
  u32x4 Bt[2][8];
  const char* pb0 = smem + GA_B + (kn_base(lane) ^ ((2 * wn) << 5));
  const char* pb1 = smem + GA_B + (kn_base(lane) ^ ((2 * wn + 1) << 5));
  auto ldB = [&](int j) {
#pragma unroll
    for (int i = 0; i < 4; i++) { Bt[j & 1][2 * i] = kn_frag(pb0 + (4 * j + i) * 4096); Bt[j & 1][2 * i + 1] = kn_frag(pb1 + (4 * j + i) * 4096); }
    SCHED();
  };
  auto ldB3 = [&](int s0, int n, int o) {
#pragma unroll
    for (int s = 0; s < 8; s++) if (s < n) {
      B[s0 + s][0] = kn_frag(smem + o + (s >> 1) * 16384 + (s & 1) * 4096 + ob0);
      B[s0 + s][1] = kn_frag(smem + o + (s >> 1) * 16384 + (s & 1) * 4096 + ob1);
    }
#pragma unroll
    for (int s = 0; s < 8; s++) if (s < n) { mmaz(s0 + s == 0, cw0, B[s0 + s][0], ONES4); mmaz(s0 + s == 0, cw1, B[s0 + s][1], ONES4); }
  };
#define W2C(c) glds<GA_B + (c) * 8192, (c) * CBD>(vbD, p.w2 + col0, dw);
#define W3C(c, dst) glds<(dst), (c) * CB3>(vbF, sb3, dw); glds<(dst) + 8192, (c) * CB3 + 128>(vbF, sb3, dw);
#define W3C4(c, dst) W3C(c, dst) glds<(dst), (c) * CB3>(vbF4, sb3, dw - 4096); glds<(dst) + 8192, (c) * CB3 + 128>(vbF4, sb3, dw - 4096);
  {
    f32x4 cv0, cv1;
    {
#pragma unroll
      for (int i = 0; i < 4; i++) { Bt[0][2 * i] = kn_frag(pb0 + (4 * mt + i) * 4096); Bt[0][2 * i + 1] = kn_frag(pb1 + (4 * mt + i) * 4096); }
#pragma unroll
      for (int i = 0; i < 4; i++) { mmaz(i == 0, cv0, Bt[0][2 * i], ONES4); mmaz(i == 0, cv1, Bt[0][2 * i + 1], ONES4); }
      mfence(); pin(cv0); pin(cv1);
      char* ct = smem + 65536 + wn * 128 + g * 16;
      if (r16 == 0) { *(f32x4*)(ct + mt * 256) = cv0; *(f32x4*)(ct + mt * 256 + 64) = cv1; }
      lds_bar();
      cv0 = (*(const f32x4*)(ct) + *(const f32x4*)(ct + 256)) + (*(const f32x4*)(ct + 512) + *(const f32x4*)(ct + 768));
      cv1 = (*(const f32x4*)(ct + 64) + *(const f32x4*)(ct + 320)) + (*(const f32x4*)(ct + 576) + *(const f32x4*)(ct + 832));
    }
    wait_vm_lgkm<0>(); bar();
    if (tid < 16) st8_dev(wsr, FL_2 + tid * 2048 + bid * 8, tag);
    STAMP(9);
    if (w >= 4) { W3C4(0, 65536) W3C4(1, 81920) }
    ldB(0); ldB(1);
    if (w == 0) poll256(wsr, FL_2 + (bid & 15) * 2048, tag, lane);
    bar();
    STAMP(10);
    {
      const int v4 = (mt & 1) + 2 * wn;
      const char* sa = p.ws + WS_HT + (rt & ~1) * 64 + (size_t)v4 * 131072;
      const u32 vo = (u32)((lane >> 3) * 8192 + ((lane & 7) ^ (((lane >> 5) & 1) << 2)) * 16);
      const u32 d = lds0 + GA_A + (mt >> 1) * 32768 + v4 * 2048;
      gldsd<0, 0>(vo, sa, d); gldsd<1024, 65536>(vo, sa, d); gldsd<8192, 4 * 131072>(vo, sa, d); gldsd<9216, 4 * 131072 + 65536>(vo, sa, d);
      gldsd<16384, 8 * 131072>(vo, sa, d); gldsd<17408, 8 * 131072 + 65536>(vo, sa, d); gldsd<24576, 12 * 131072>(vo, sa, d); gldsd<25600, 12 * 131072 + 65536>(vo, sa, d);
    }
    const char* aw = smem + GA_A + (mt >> 1) * 32768 + ((lane & 15) >> 3) * 128 + ((lane >> 2) & 1) * 32 + (lane & 3) * 8;
    const char* pa0 = aw + g * 512 + (((mt ^ g) & 1) << 6);
    f32x4 acc0, acc1, sacc, gacc;
    u32x4 a[2][4];
    auto ld = [&](int j) {
#pragma unroll
      for (int i = 0; i < 4; i++) {
        u32x2 x0 = tr_read(pa0 + (4 * j + i) * 2048), x1 = tr_read(pa0 + (4 * j + i) * 2048 + 256);
        a[j & 1][i] = u32x4{x0[0], x0[1], x1[0], x1[1]};
      }
      SCHED();
    };
    auto mm = [&](int j) {
#pragma unroll
      for (int i = 0; i < 4; i++) {
        mmaz(j + i == 0, sacc, ONES4, a[j & 1][i]);
        mmaz(j + i == 0, gacc, a[j & 1][i], a[j & 1][i]);
        mmaz(j + i == 0, acc0, Bt[j & 1][2 * i], a[j & 1][i]);
        mmaz(j + i == 0, acc1, Bt[j & 1][2 * i + 1], a[j & 1][i]);
      }
      SCHED();
    };
    SCHED();
    wait_vm<6>(); bar(); ld(0);
    STAMP(23);
    mm(0); ldB(2);
    wait_vm<4>(); bar(); ld(1); mm(1); ldB(3);
    wait_vm<2>(); bar(); ld(2); mm(2);
    wait_vm_lgkm<0>(); bar(); ld(3);
    W2C(0) W2C(1)
    STAMP(24);
    const char* hs = aw + (2 * pn + wn) * 2048 + pc4 * 512 + (((mt ^ pc4) & 1) << 6);
    u32x2 hx0 = tr_read(hs), hx1 = tr_read(hs + 256);
    SCHED(); mm(3); W2C(2) W2C(3)
    mfence(); pin(acc0); pin(acc1); pin(sacc); pin(gacc);
    STAMP(11);
    float dg = (r16 & 2) ? ((r16 & 1) ? gacc[3] : gacc[2]) : ((r16 & 1) ? gacc[1] : gacc[0]);
    f32x2 sa2 = {sacc[0], gsum(g == (r16 >> 2) ? dg : 0.f)};
    float mean1 = sa2[0] * (1.0f / D_);
    float var1 = fmaxf(sa2[1] * (1.0f / D_) - mean1 * mean1, 0.f);
    float m1 = rbf(mean1);
    float sd1 = rbf(__builtin_amdgcn_sqrtf(rbf(rbf(var1) + 1e-5f)));
    float i1 = __builtin_amdgcn_rcpf(sd1);
    float m2 = rbf((mean1 - m1) * i1);
    float sd2 = rbf(__builtin_amdgcn_sqrtf(rbf(rbf(var1 * i1 * i1) + 1e-5f)));
    float rs = i1 * __builtin_amdgcn_rcpf(sd2), tt = -(m1 + m2 * sd1) * rs;
    {
      f32x4 rt4 = {rs, rs, tt, tt};
      pin(rt4);
      u32x2 o0 = act4(acc0, cv0, rt4), o1 = act4(acc1, cv1, rt4);
      st16_dev(wsr, WS_H1 + 16384 + boff, pair_swap(o0[0], o0[1], o1[0], o1[1]));
    }
    if (tid < 16) {
      const u32 o = (u32)((pn * 2 + (tid >> 3)) * 1024 + (tid & 7) * 16);
      if (pm == 0) st16_dev(wsr, WS_H1 + 14 * 64 + o, u32x4{0, 0, 0, 0});
      if (pm == 31) st16_dev(wsr, WS_H1 + 129 * 16384 + o, u32x4{0, 0, 0, 0});
    }
    hres = u32x4{hx0[0], hx0[1], hx1[0], hx1[1]};
#pragma unroll
    for (int i = 0; i < 4; i++) xres[i] = pkbf2(bfup(pkbf2(bfup(hres[i]) - m1)) * i1);
    W2C(4) W2C(5) W2C(6) W2C(7)
    wait_vm_lgkm<4>(); bar();
    if (tid < 8) st8_dev(wsr, FL_3 + tid * 2048 + (pm * 8 + pn) * 8, tag);
    ldB3(0, 4, 65536);
    ldB(0); ldB(1);
  }
  STAMP(12);
  const int ar = lane >> 3, asp = (lane & 7) ^ ar;
  const u32 apo = (u32)((asp >> 2) * 1024 + (asp & 3) * 16);
  {
    u32 vo[8];
#pragma unroll
    for (int k = 0; k < 8; k++) {
      const int sh = (k >> 1) == 0 ? -1 : (k >> 1) == 1 ? 1 : (k >> 1) == 2 ? -2 : 2;
      const int row = 16 + ar + (k & 1) * 8 + sh;
      vo[k] = (u32)((row >> 4) * 16384 + (row & 15) * 64) + apo;
    }
    if (w == 0) polln(wsr, FL_3 + pn * 2048, max((pm & ~1) - 1, 0) * 8, min((pm & ~1) + 3, 32) * 8, tag, lane);
    lds_bar();
    STAMP(13);
    {
      const char* sa = p.ws + WS_H1 + (size_t)rt * 16384 + wn * 2048;
      const u32 d = lds0 + GA_A + mt * 16384 + wn * 2048;
      gldsd<0, 0>(vo[0], sa, d); gldsd<1024, 0>(vo[1], sa, d); gldsd<4096, 4096>(vo[2], sa, d); gldsd<5120, 4096>(vo[3], sa, d);
      gldsd<8192, 8192>(vo[4], sa, d); gldsd<9216, 8192>(vo[5], sa, d); gldsd<12288, 12288>(vo[6], sa, d); gldsd<13312, 12288>(vo[7], sa, d);
    }

    const char* pa0 = smem + GA_A + mt * 16384 + mk_base(lane, 0);
    const char* pa1 = smem + GA_A + mt * 16384 + mk_base(lane, 1);
    f32x4 acc0, acc1;
    u32x4 a[2][4];
    auto ld = [&](int j) {
      a[j & 1][0] = *(const u32x4*)(pa0 + j * 4096); a[j & 1][1] = *(const u32x4*)(pa1 + j * 4096);
      a[j & 1][2] = *(const u32x4*)(pa0 + j * 4096 + 2048); a[j & 1][3] = *(const u32x4*)(pa1 + j * 4096 + 2048);
      SCHED();
    };
    auto mm = [&](int j) {
#pragma unroll
      for (int i = 0; i < 4; i++) {
        mmaz(j + i == 0, acc0, Bt[j & 1][2 * i], a[j & 1][i]);
        mmaz(j + i == 0, acc1, Bt[j & 1][2 * i + 1], a[j & 1][i]);
      }
      SCHED();
    };
    SCHED();
    wait_vm<6>(); bar(); ld(0);
    STAMP(25);
    wait_vm<4>(); bar(); ld(1); mm(0); ldB(2);
    wait_vm<2>(); bar(); ld(2); mm(1); ldB(3);
    wait_vm_lgkm<0>(); bar(); ld(3);
    W3C(2, 65536)
    STAMP(26);
    mm(2); W3C(3, 81920) mm(3);
    mfence(); pin(acc0); pin(acc1);
    STAMP(14);
    u32x4 v = pair_swap(pkbf(acc0[0], acc0[1]), pkbf(acc0[2], acc0[3]), pkbf(acc1[0], acc1[1]), pkbf(acc1[2], acc1[3]));
#pragma unroll
    for (int i = 0; i < 4; i++) xres[i] = pkbf2(bfup(xres[i]) + bfup(v[i]));
    st16_dev(wsr, WS_X1 + boff, xres);
    float s1 = 0, s2 = 0;
#pragma unroll
    for (int i = 0; i < 4; i++) { s1 = dot2(xres[i], ONES, s1); s2 = dot2(xres[i], xres[i], s2); }
    s1 = gsum(s1); s2 = gsum(s2);
    if (g == 0) st8_dev(wsr, WS_ST + (rt * 16 + pn * 2 + wn) * 128 + r16 * 8, u32x2{__float_as_uint(s1), __float_as_uint(s2)});
    W3C(4, GA_B) W3C(5, GA_B + 16384)
    wait_vm_lgkm<4>(); bar();
    if (tid < 8) st8_dev(wsr, FL_4 + tid * 2048 + (pm * 8 + pn) * 8, tag);
    W3C(6, GA_B + 32768) W3C(7, GA_B + 49152)
  }
  STAMP(15);
  const u16* sb4 = p.w4 + col0;
#define G4_R1A(i) glds<GA_B + (i) * 8192, (((i) >> 1) * 7 + ((i) & 1)) * CBD>(vbD, sb4, dw);
#define G4_R1A4(i) G4_R1A(i) glds<GA_B + (i) * 8192, (((i) >> 1) * 7 + ((i) & 1)) * CBD>(vbD4, sb4, dw - 4096);
#define G4_R1A_ALL4 G4_R1A4(0) G4_R1A4(1) G4_R1A4(2) G4_R1A4(3) G4_R1A4(4) G4_R1A4(5) G4_R1A4(6) G4_R1A4(7)
  const u32 vbD4 = kn_voff(D_, w - 4, lane);
#define G4_R1B(i) glds<((i) < 2 ? G3_ST : 0) + (i) * 8192, (((i) >> 1) * 7 + 2 + ((i) & 1)) * CBD>(vbD, sb4, dw);
#define G4_R2POS(i) ((i) < 4 ? 32768 + (i) * 8192 : GA_B + ((i) - 4) * 8192)
#define G4_R2(i) glds<G4_R2POS(i), (((i) / 3) * 7 + 4 + ((i) % 3)) * CBD>(vbD, sb4, dw);
  const int wm4 = mt >> 1, kq = (mt & 1) + 2 * wn;
  const int kb = kn_base(lane);
  u32x4(&B4)[8][4] = *reinterpret_cast<u32x4(*)[8][4]>(&B);
  const char* sa4 = p.ws + WS_HID + (size_t)((rt & ~1) * 56 + 14 * kq) * 1024;
  const u32 va4 = (u32)(ar * 64) + apo;
  const int ring = (wm4 ? 0 : 65536) + kq * 8192;
  const u32 da = lds0 + ring;
#define G4_A4(b) gldsd<((b) & 1) * 4096, (b) * 2048>(va4, sa4, da); gldsd<((b) & 1) * 4096 + 1024, (b) * 2048 + 512>(va4, sa4, da); \
                 gldsd<((b) & 1) * 4096 + 2048, (b) * 2048 + 57344>(va4, sa4, da); gldsd<((b) & 1) * 4096 + 3072, (b) * 2048 + 57344 + 512>(va4, sa4, da);
  auto ldB4 = [&](int s) {
    const char* p0 = smem + GA_B + kq * 16384 + s * 4096;
    const char* p1 = smem + (kq ? kq * 16384 : G3_ST) + s * 4096;
#pragma unroll
    for (int nt = 0; nt < 4; nt++) { B4[s][nt] = kn_frag(p0 + (kb ^ (nt << 5))); B4[4 + s][nt] = kn_frag(p1 + (kb ^ (nt << 5))); }
    SCHED();
  };
  {
    const int wm = wm3;
    ldB3(4, 4, 65536);
    wait_vm<0>(); bar();
    ldB3(8, 8, GA_B);
    if (w == 0) polln(wsr, FL_4 + pn * 2048, pm3 * 16, pm3 * 16 + 16, tag, lane);
    lds_bar();
    STAMP(16);
    const char* sa = p.ws + WS_X1 + (size_t)(pm3 * 8 + (w >> 1)) * 16384 + (w & 1) * 512;
    const u32 va = (u32)(ar * 64) + apo;
#define G3_A(i) gldsd<(((i) + 4) % 9) * 8192, ((i) >> 3) * 65536 + ((i) & 7) * 2048>(va, sa, dw);
    {
      const char* ss = p.ws + WS_ST + (size_t)pm3 * 16384 + w * 1024;
      gldsd<G3_ST, 0>(lane * 16, ss, dw); gldsd<G3_ST + 8192, 8192>(lane * 16, ss, dw);
    }
    G3_A(0) G3_A(1) G3_A(2) G3_A(3) G3_A(4) G3_A(5) G3_A(6) G3_A(7) G3_A(8)
    wait_vm<9>(); bar();
    {
      const char* sst = smem + G3_ST + (tid >> 6) * 2048 + (tid & 3) * 512 + ((tid >> 2) & 15) * 8;
      f32x2 st = (*(const f32x2*)(sst) + *(const f32x2*)(sst + 128)) + (*(const f32x2*)(sst + 256) + *(const f32x2*)(sst + 384));
      float t1 = st[0] + dppf<0xB1>(st[0]), t2 = st[1] + dppf<0xB1>(st[1]);
      t1 += dppf<0x4E>(t1); t2 += dppf<0x4E>(t2);
      float mean = t1 * (1.0f / D_);
      float var = fmaxf(t2 * (1.0f / D_) - mean * mean, 0.f);
      float rs = __builtin_amdgcn_rcpf(rbf(__builtin_amdgcn_sqrtf(rbf(rbf(var) + 1e-5f))));
      if ((tid & 3) == 0) { float tt = -rbf(mean) * rs; *(f32x4*)(smem + G3_TAB + (tid >> 2) * 16) = f32x4{rs, rs, tt, tt}; }
    }
    const f32x4& cv0 = cw0;
    const f32x4& cv1 = cw1;
    float* rowt = (float*)(smem + G3_TAB);
    const int oa0 = mk_base(lane, 0) + wm * 4096, oa1 = mk_base(lane, 1) + wm * 4096;
    f32x4 acc[2][2][2];
    u32x4 a[2][4];
    auto ld = [&](int slot, u32x4 (&d)[4]) {
      const char* s = smem + slot * 8192;
      d[0] = *(const u32x4*)(s + oa0); d[1] = *(const u32x4*)(s + oa0 + 2048);
      d[2] = *(const u32x4*)(s + oa1); d[3] = *(const u32x4*)(s + oa1 + 2048);
      SCHED();
    };
    auto m1 = [&](int ps, int b, int i, const u32x4 (&d)[4]) {
      mmaz(b == 0 && i < 4, acc[ps][(i >> 1) & 1][i & 1], B[2 * b + (i >> 2)][(i >> 1) & 1], d[2 * (i >> 2) + (i & 1)]);
      SCHED();
    };
    auto mm = [&](int ps, int b, const u32x4 (&d)[4]) {
#pragma unroll
      for (int i = 0; i < 8; i++) m1(ps, b, i, d);
    };
    const u32 hoff = (has3 ? WS_HID : 0x40000000u) + (u32)(((pm3 * 8 + 2 * wm) * 56 + pn3 * 4 + wn3) * 1024 + r16 * 64 + pc4 * 16);
    u32x2 o0;
    f32x4 rtv[2];
    auto ldrt = [&](int ps) {
      rtv[0] = *(const f32x4*)(rowt + (64 * ps + 32 * wm + r16) * 4);
      rtv[1] = *(const f32x4*)(rowt + (64 * ps + 32 * wm + 16 + r16) * 4);
    };
    auto ep = [&](int ps, int i) {
      const int mi = i >> 1;
      if ((i & 1) == 0) o0 = act4(acc[ps][0][mi], cv0, rtv[mi]);
      else {
        u32x2 o1 = act4(acc[ps][1][mi], cv1, rtv[mi]);
        st16_dev(wsr, hoff + (4 * ps + mi) * 57344, pair_swap(o0[0], o0[1], o1[0], o1[1]));
      }
    };
    float c0v = -2.0f * 0.7978845608028654f * 1.4426950408889634f;
    asm volatile("" : "+v"(c0v));
    auto mme = [&](int b, const u32x4 (&d)[4], int e) {
      const float c1 = -2.0f * 0.7978845608028654f * 1.4426950408889634f * 0.044715f;
      const int mi = e >> 1;
      const f32x4 aa = acc[0][e & 1][mi], cc = (e & 1) ? cv1 : cv0;
      float v[4], x[4], z[4];
      m1(1, b, 0, d);
#pragma unroll
      for (int q = 0; q < 4; q++) v[q] = smul(aa[q], rtv[mi][0]);
      SCHED(); m1(1, b, 1, d);
#pragma unroll
      for (int q = 0; q < 4; q++) v[q] = sfma(cc[q], rtv[mi][2], v[q]);
      u32 p0 = pkbf(v[0], v[1]), p1 = pkbf(v[2], v[3]);
      SCHED(); m1(1, b, 2, d);
      x[0] = bflo(p0); x[1] = bfhi(p0); x[2] = bflo(p1); x[3] = bfhi(p1);
      z[0] = smul(x[0], x[0]); z[1] = smul(x[1], x[1]);
      SCHED(); m1(1, b, 3, d);
      z[2] = smul(x[2], x[2]); z[3] = smul(x[3], x[3]);
#pragma unroll
      for (int q = 0; q < 4; q++) z[q] = sfmak(z[q], c1, c0v);
      SCHED(); m1(1, b, 4, d);
#pragma unroll
      for (int q = 0; q < 4; q++) z[q] = smul(z[q], x[q]);
      z[0] = __builtin_amdgcn_exp2f(z[0]); z[1] = __builtin_amdgcn_exp2f(z[1]);
      SCHED(); m1(1, b, 5, d);
      z[2] = __builtin_amdgcn_exp2f(z[2]); z[3] = __builtin_amdgcn_exp2f(z[3]);
#pragma unroll
      for (int q = 0; q < 4; q++) z[q] = sadd1(z[q]);
      SCHED(); m1(1, b, 6, d);
#pragma unroll
      for (int q = 0; q < 4; q++) z[q] = __builtin_amdgcn_rcpf(z[q]);
      SCHED(); m1(1, b, 7, d);
#pragma unroll
      for (int q = 0; q < 4; q++) x[q] = smul(x[q], z[q]);
      u32x2 o = {pkbf(x[0], x[1]), pkbf(x[2], x[3])};
      if (e & 1) st16_dev(wsr, hoff + mi * 57344, pair_swap(o0[0], o0[1], o[0], o[1])); else o0 = o;
      SCHED();
    };
    SCHED();
#define SL(i) (((i) + 4) % 9)
    wait_vm_lgkm<8>(); bar(); ld(SL(0), a[0]); G4_R1A(0)
    wait_vm_lgkm<8>(); bar(); ld(SL(1), a[1]); G4_R1A(1) mm(0, 0, a[0]); SCHED();
    wait_vm_lgkm<8>(); bar(); G3_A(9) ld(SL(2), a[0]); G4_R1A(2) mm(0, 1, a[1]); SCHED();
    wait_vm_lgkm<9>(); bar(); G3_A(10) ld(SL(3), a[1]); G4_R1A(3) mm(0, 2, a[0]); SCHED();
    wait_vm_lgkm<10>(); bar(); G3_A(11) ld(SL(4), a[0]); G4_R1A(4) mm(0, 3, a[1]); SCHED();
    wait_vm_lgkm<11>(); bar(); G3_A(12) ld(SL(5), a[1]); G4_R1A(5) mm(0, 4, a[0]); SCHED();
    wait_vm_lgkm<12>(); bar(); G3_A(13) ld(SL(6), a[0]); G4_R1A(6) mm(0, 5, a[1]); SCHED();
    wait_vm_lgkm<13>(); bar(); G3_A(14) ld(SL(7), a[1]); G4_R1A(7) mm(0, 6, a[0]); SCHED();
    wait_vm_lgkm<14>(); bar(); G3_A(15) ld(SL(8), a[0]); ldrt(0); mm(0, 7, a[1]); SCHED();
    STAMP(17);
    mfence();
#pragma unroll
    for (int i = 0; i < 4; i++) pin(acc[0][i >> 1][i & 1]);
#pragma unroll
    for (int i = 0; i < 4; i++) ep(0, i);
    SCHED();
    wait_vm_lgkm<14>(); bar(); ld(SL(9), a[1]); G4_R1B(2) G4_R1B(0) mm(1, 0, a[0]); SCHED();
    wait_vm_lgkm<14>(); bar(); ld(SL(10), a[0]); G4_R1B(3) G4_R1B(1) mm(1, 1, a[1]); SCHED();
    wait_vm_lgkm<14>(); bar(); ld(SL(11), a[1]); G4_R1B(4) mm(1, 2, a[0]); SCHED();
    wait_vm_lgkm<5>(); bar();
    if (has3 && tid < 8) st8_dev(wsr, FL_5 + tid * 2048 + (pm3 * 14 + pn3) * 8, tag);
    ld(SL(12), a[0]); G4_R1B(5) mm(1, 3, a[1]); SCHED();
    lds_bar(); ld(SL(13), a[1]); G4_R1B(6) mm(1, 4, a[0]); SCHED();
    lds_bar(); ld(SL(14), a[0]); G4_R1B(7) mm(1, 5, a[1]); SCHED();
    lds_bar(); ld(SL(15), a[1]); ldrt(1); mm(1, 6, a[0]); SCHED();
    wait_vm_lgkm<0>(); bar();
    mm(1, 7, a[1]);
    mfence();
#pragma unroll
    for (int i = 0; i < 4; i++) pin(acc[1][i >> 1][i & 1]);
    STAMP(18);
#pragma unroll
    for (int i = 0; i < 4; i++) { ldB4(i); ep(1, i); SCHED(); }
    if (w == 0) polln(wsr, FL_5 + pn * 2048, (pm >> 1) * 14, (pm >> 1) * 14 + 14, tag, lane);
    lds_bar();
    if (wm4 == 0) { G4_A4(0) G4_A4(1) wait_vm<8>(); } else wait_vm<0>();
    bar();
    if (has3 && tid < 8) st8_dev(wsr, FL_6 + tid * 2048 + (pm3 * 14 + pn3) * 8, tag);
  }
  STAMP(19);
  {
    const int wm = wm4;
    if (wm) {
      const u32 vb2 = kn_voff(D_, w - 2, lane), dw2 = dw - 2048;
#define G4_R2X(i) G4_R2(i) glds<G4_R2POS(i), (((i) / 3) * 7 + 4 + ((i) % 3)) * CBD>(vb2, sb4, dw2);
      G4_R2X(0) G4_R2X(3) G4_R2X(6) G4_R2X(9) G4_R2X(1) G4_R2X(4) G4_R2X(7) G4_R2X(10) G4_R2X(2) G4_R2X(5) G4_R2X(8) G4_R2X(11)
      polln(wsr, FL_6 + (pn ^ kq) * 2048, (pm >> 1) * 14, (pm >> 1) * 14 + 14, tag, lane);
      G4_A4(0) G4_A4(1)
      wait_vm<8>(); bar();
    }
    STAMP(20);
    const char* pa0 = smem + ring + mk_base(lane, 0);
    const char* pa1 = smem + ring + mk_base(lane, 1);
    f32x4 acc[4][2];
    u32x4 a[4];
    auto ld = [&](int b) {
      a[0] = *(const u32x4*)(pa0 + (b & 1) * 4096); a[1] = *(const u32x4*)(pa0 + (b & 1) * 4096 + 2048);
      a[2] = *(const u32x4*)(pa1 + (b & 1) * 4096); a[3] = *(const u32x4*)(pa1 + (b & 1) * 4096 + 2048);
      asm volatile("s_waitcnt lgkmcnt(0)" ::: "memory");
      SCHED();
    };
    auto mm = [&](int s0, bool first = false) {
#pragma unroll
      for (int ks = 0; ks < 2; ks++)
#pragma unroll
        for (int nt = 0; nt < 4; nt++)
#pragma unroll
          for (int mi = 0; mi < 2; mi++) mmaz(first && ks == 0, acc[nt][mi], B4[s0 + ks][nt], a[2 * ks + mi]);
      SCHED();
    };
    auto ldb = [&](int j, int s0) {
      const int i = kq * 3 + j;
      const char* bl = smem + (i < 4 ? 32768 + i * 8192 : GA_B + (i - 4) * 8192);
#pragma unroll
      for (int ks = 0; ks < 2; ks++)
#pragma unroll
        for (int nt = 0; nt < 4; nt++) B4[s0 + ks][nt] = kn_frag(bl + ks * 4096 + (kb ^ (nt << 5)));
      SCHED();
    };
    wait_vm<4>(); ld(0); G4_A4(2) mm(0, true);
    wait_vm<4>(); ld(1); G4_A4(3) mm(2);
    wait_vm<4>(); ld(2); G4_A4(4) mm(4);
    wait_vm<4>(); ld(3); G4_A4(5) mm(6);
    if (wm == 0) bar();
    ldb(0, 0); ldb(1, 2);
    wait_vm<4>(); ld(4); G4_A4(6) ldb(2, 4); mm(0);
    wait_vm<4>(); ld(5); mm(2);
    wait_vm<0>(); ld(6); mm(4);
    mfence();
#pragma unroll
    for (int i = 0; i < 8; i++) pin(acc[i >> 1][i & 1]);
    STAMP(21);
    {
      char* d = smem + ring + lane * 16;
#pragma unroll
      for (int k2 = 0; k2 < 4; k2++) {
        *(f32x4*)(d + k2 * 2048) = acc[2 * (k2 >> 1)][k2 & 1];
        *(f32x4*)(d + k2 * 2048 + 1024) = acc[2 * (k2 >> 1) + 1][k2 & 1];
      }
    }
    lds_bar();
    const char* s = smem + (wm ? 0 : 65536) + kq * 2048 + lane * 16;
    f32x4 f0 = (*(const f32x4*)(s) + *(const f32x4*)(s + 8192)) + (*(const f32x4*)(s + 16384) + *(const f32x4*)(s + 24576));
    f32x4 f1 = (*(const f32x4*)(s + 1024) + *(const f32x4*)(s + 9216)) + (*(const f32x4*)(s + 17408) + *(const f32x4*)(s + 25600));
    u32x4 v = pair_swap(pkbf(f0[0], f0[1]), pkbf(f0[2], f0[3]), pkbf(f1[0], f1[1]), pkbf(f1[2], f1[3]));
    u32x4 o;
#pragma unroll
    for (int i = 0; i < 4; i++) o[i] = pkbf2(bfup(hres[i]) + bfup(pkbf2(bfup(xres[i]) + bfup(v[i]))));
    __builtin_amdgcn_raw_buffer_store_b128(o, __builtin_amdgcn_make_buffer_rsrc(p.out, 0, L_ * D_ * 2, 0x00020000),
                                           (u32)(((rt * 16 + r16) * D_ + col0 + 32 * wn + pc4 * 8) * 2), 0, 16);
  }
  STAMP(22);
  asm volatile(".Lk_end:");
}

extern "C" void kernel_launch(void* const* d_in, const int* in_sizes, int n_in, void* d_out, int out_size, void* d_ws,
                              size_t ws_size, hipStream_t stream) {
  Params p;
  p.x = (const u16*)d_in[0]; p.are = (const u16*)d_in[1]; p.aim = (const u16*)d_in[2]; p.bre = (const u16*)d_in[3];
  p.bim = (const u16*)d_in[4]; p.cre = (const u16*)d_in[5]; p.cim = (const u16*)d_in[6]; p.logdt = (const u16*)d_in[7];
  p.dskip = (const u16*)d_in[8]; p.w1 = (const u16*)d_in[10]; p.w2 = (const u16*)d_in[11]; p.w3 = (const u16*)d_in[12];
  p.w4 = (const u16*)d_in[13]; p.out = (u16*)d_out; p.ws = (char*)d_ws;
  k_fused<<<256, 512, 0, stream>>>(p);
}
```

```cpp
#include <hip/hip_runtime.h>
#include <stdint.h>

typedef unsigned short u16;
typedef unsigned int u32;
typedef unsigned long long u64;
typedef __bf16 bf16x2 __attribute__((ext_vector_type(2)));
typedef __bf16 bf16x8 __attribute__((ext_vector_type(8)));
typedef _Float16 f16x2 __attribute__((ext_vector_type(2)));
typedef _Float16 f16x8 __attribute__((ext_vector_type(8)));
typedef float f32x2 __attribute__((ext_vector_type(2)));
typedef float f32x4 __attribute__((ext_vector_type(4)));
typedef short s16x4 __attribute__((ext_vector_type(4)));
typedef u32 u32x2 __attribute__((ext_vector_type(2)));
typedef u32 u32x4 __attribute__((ext_vector_type(4)));
#define LDSP __attribute__((address_space(3)))
#define DEV __device__ __forceinline__

constexpr int L_ = 2048, D_ = 512, NS_ = 128, F_ = 1792;
constexpr u32 WS_U8 = 0;
constexpr u32 WS_HT = 7u << 20;
constexpr u32 WS_H1 = 13u << 20;
constexpr u32 WS_X1 = 16u << 20;
constexpr u32 WS_ST = 18u << 20;
constexpr u32 WS_HID = 19u << 20;
constexpr u32 WS_FL = 28u << 20;
constexpr u32 FL_EPOCH = WS_FL, FL_1 = WS_FL + 4096, FL_2 = FL_1 + 32768, FL_3 = FL_2 + 32768, FL_4 = FL_3 + 16384, FL_5 = FL_4 + 16384, FL_6 = FL_5 + 16384;

struct Params {
  const u16 *x, *are, *aim, *bre, *bim, *cre, *cim, *logdt, *dskip, *w1, *w2, *w3, *w4;
  u16* out;
  char* ws;
};

DEV float bf2f(u32 v) { return __uint_as_float(v << 16); }
DEV float bflo(u32 v) { return __uint_as_float(v << 16); }
DEV float bfhi(u32 v) { return __uint_as_float(v & 0xffff0000u); }
DEV f32x2 bfup(u32 v) { return f32x2{bflo(v), bfhi(v)}; }
DEV u32 pkbf2(f32x2 v) { return __builtin_bit_cast(u32, __builtin_convertvector(v, bf16x2)); }
DEV u32 pkbf(float a, float b) { return pkbf2(f32x2{a, b}); }
DEV u32 pkh(float a, float b) { f32x2 v = {a, b}; return __builtin_bit_cast(u32, __builtin_convertvector(v, f16x2)); }
DEV float rbf(float a) { return bflo(pkbf(a, a)); }
DEV f32x2 hup(u32 v) { return __builtin_convertvector(__builtin_bit_cast(f16x2, v), f32x2); }

template <int CTRL> DEV float dppf(float v) {
  return __builtin_bit_cast(float, __builtin_amdgcn_update_dpp(0, __builtin_bit_cast(int, v), CTRL, 0xf, 0xf, true));
}
DEV float gsum(float v) {
  u32 a = __float_as_uint(v);
  auto r = __builtin_amdgcn_permlane16_swap(a, a, false, false);
  v = __uint_as_float(r[0]) + __uint_as_float(r[1]);
  a = __float_as_uint(v);
  auto q = __builtin_amdgcn_permlane32_swap(a, a, false, false);
  return __uint_as_float(q[0]) + __uint_as_float(q[1]);
}
DEV float wave_sum(float v) {
  v += dppf<0xB1>(v); v += dppf<0x4E>(v); v += dppf<0x141>(v); v += dppf<0x140>(v);
  return gsum(v);
}
DEV u32x2 tr_read(const char* p) {
  return __builtin_bit_cast(u32x2, __builtin_amdgcn_ds_read_tr16_b64_v4i16((LDSP s16x4*)p));
}
DEV void mma(f32x4& c, u32x4 a, u32x4 b) { asm volatile("v_mfma_f32_16x16x32_bf16 %0, %1, %2, %0" : "+v"(c) : "v"(a), "v"(b)); }
DEV void mma0(f32x4& c, u32x4 a, u32x4 b) { asm volatile("v_mfma_f32_16x16x32_bf16 %0, %1, %2, 0" : "=&v"(c) : "v"(a), "v"(b)); }
DEV void mmah(f32x4& c, u32x4 a, u32x4 b) { asm volatile("v_mfma_f32_16x16x32_f16 %0, %1, %2, %0" : "+v"(c) : "v"(a), "v"(b)); }
DEV void mmah0(f32x4& c, u32x4 a, u32x4 b) { asm volatile("v_mfma_f32_16x16x32_f16 %0, %1, %2, 0" : "=&v"(c) : "v"(a), "v"(b)); }
DEV void mmaz(bool first, f32x4& c, u32x4 a, u32x4 b) { if (first) mma0(c, a, b); else mma(c, a, b); }
DEV void mfence() { asm volatile("s_nop 7" ::: "memory"); __builtin_amdgcn_sched_barrier(0); }
DEV void pin(f32x4& c) { asm volatile("" : "+v"(c)); }
DEV float dot2(u32 a, u32 b, float c) {
  return __builtin_amdgcn_fdot2_f32_bf16(__builtin_bit_cast(bf16x2, a), __builtin_bit_cast(bf16x2, b), c, false);
}
template <int LOFF, int MOFF> DEV void glds(u32 voff, const void* sbase, u32 lds) {
  asm volatile("s_add_u32 m0, %1, %3\n\ts_nop 0\n\tglobal_load_lds_dwordx4 %0, %2"
               :: "v"(voff + (u32)MOFF), "s"(lds), "s"(sbase), "n"(LOFF) : "memory", "m0", "scc");
}
template <int LOFF, int MOFF> DEV void gldsd(u32 voff, const void* sbase, u32 lds) {
  asm volatile("s_add_u32 m0, %1, %3\n\ts_nop 0\n\tglobal_load_lds_dwordx4 %0, %2 sc1"
               :: "v"(voff + (u32)MOFF), "s"(lds), "s"(sbase), "n"(LOFF) : "memory", "m0", "scc");
}
DEV void touch(u32& t, const void* sbase, u32 voff) {
  asm volatile("global_load_dword %0, %1, %2" : "+v"(t) : "v"(voff), "s"(sbase) : "memory");
}
DEV void keep(u32 t) { asm volatile("" :: "v"(t)); }
DEV u32 lds_addr(const char* p) { return (u32)(size_t)(LDSP const char*)p; }
#define SCHED() __builtin_amdgcn_sched_barrier(0)
template <int N> DEV void wait_vm() { asm volatile("s_waitcnt vmcnt(%0)" ::"n"(N) : "memory"); }
template <int N> DEV void wait_vm_lgkm() { asm volatile("s_waitcnt vmcnt(%0) lgkmcnt(0)" ::"n"(N) : "memory"); }
DEV void bar() { __builtin_amdgcn_s_barrier(); }
DEV void lds_bar() { asm volatile("s_waitcnt lgkmcnt(0)" ::: "memory"); __builtin_amdgcn_s_barrier(); }


typedef __amdgpu_buffer_rsrc_t rsrc_t;
DEV void st16_dev(rsrc_t r, u32 off, u32x4 v) { __builtin_amdgcn_raw_buffer_store_b128(v, r, off, 0, 16); }
DEV void st8_dev(rsrc_t r, u32 off, u32x2 v) { __builtin_amdgcn_raw_buffer_store_b64(v, r, off, 0, 16); }
DEV u32x4 ld16_dev(rsrc_t r, u32 off) { return __builtin_amdgcn_raw_buffer_load_b128(r, off, 0, 16); }
DEV u32x2 ld8_dev(rsrc_t r, u32 off) { return __builtin_amdgcn_raw_buffer_load_b64(r, off, 0, 16); }

constexpr int SPIN_MAX = 1 << 18;
DEV void poll256(rsrc_t r, u32 base, u32x2 tag, int lane) {
#pragma unroll 1
  for (int s = 0; s < SPIN_MAX; s++) {
    u32x4 a = ld16_dev(r, base + lane * 32), b = ld16_dev(r, base + lane * 32 + 16);
    bool ok = a[0] == tag[0] && a[1] == tag[1] && a[2] == tag[0] && a[3] == tag[1] &&
              b[0] == tag[0] && b[1] == tag[1] && b[2] == tag[0] && b[3] == tag[1];
    if (__all(ok)) break;
    __builtin_amdgcn_s_sleep(2);
    asm volatile("" ::: "memory");
  }
}
DEV void polln(rsrc_t r, u32 base, int lo, int hi, u32x2 tag, int lane) {
  const int i = min(lo + lane, hi - 1);
#pragma unroll 1
  for (int s = 0; s < SPIN_MAX; s++) {
    u32x2 a = ld8_dev(r, base + i * 8);
    if (__all(a[0] == tag[0] && a[1] == tag[1])) break;
    __builtin_amdgcn_s_sleep(2);
    asm volatile("" ::: "memory");
  }
}

DEV u32x2 act4(f32x4 a, f32x4 cs, f32x4 rt) {
  const float c0 = -2.0f * 0.7978845608028654f * 1.4426950408889634f;
  const float c1 = c0 * 0.044715f;
  const f32x2 rs = {rt[0], rt[1]}, t = {rt[2], rt[3]};
  f32x2 v0 = f32x2{a[0], a[1]} * rs + f32x2{cs[0], cs[1]} * t, v1 = f32x2{a[2], a[3]} * rs + f32x2{cs[2], cs[3]} * t;
  f32x4 v = {v0[0], v0[1], v1[0], v1[1]};
  f32x4 x = v;
  f32x4 z = x * (x * x * c1 + c0);
  f32x4 e = {__builtin_amdgcn_exp2f(z[0]), __builtin_amdgcn_exp2f(z[1]), __builtin_amdgcn_exp2f(z[2]), __builtin_amdgcn_exp2f(z[3])};
  e += 1.0f;
  f32x4 r = {__builtin_amdgcn_rcpf(e[0]), __builtin_amdgcn_rcpf(e[1]), __builtin_amdgcn_rcpf(e[2]), __builtin_amdgcn_rcpf(e[3])};
  x *= r;
  return u32x2{pkbf(x[0], x[1]), pkbf(x[2], x[3])};
}

constexpr int LDS_ALL = 163840;
constexpr int KG_ROW = 528;
constexpr int KG_CH = 73 * KG_ROW;
constexpr int P0_KT = 77312;
constexpr int P0_LN = 101888;
constexpr int P0_MS = 119296;
constexpr int CV_COPY = 4160, CV_KP = 4 * CV_COPY, CV_CH = CV_KP + 4608;
constexpr int CV_UI = 2 * CV_CH;
constexpr int CV_RED = CV_UI + 16384;
constexpr int CV_STG = 0;
constexpr int GA_A = 0, GA_B = 98304;
constexpr int G3_ST = 73728, G3_TAB = 90112;

struct KTbl {
  u16 v[2048];
  constexpr KTbl() : v() {
    for (int pos = 0; pos < 2048; pos++) {
      int l = (pos & ~7) + 7 - (pos & 7);
      int sh = l < 256 ? 0 : l < 512 ? 1 : l < 1024 ? 2 : 3;
      int lv = sh ? (((l + (1 << (sh - 1)) - 1 + ((l >> sh) & 1)) >> sh) << sh) : l;
      int s = lv < 256 ? 0 : lv < 512 ? 1 : lv < 1024 ? 2 : 3;
      int rr = 8 * s + ((lv >> 7) << (3 - s)) + ((lv & 7) >> s);
      v[pos] = (u16)((rr * 16 + ((lv >> 3) & 15)) * 16);
    }
  }
};
__device__ __attribute__((aligned(16))) constexpr KTbl KTBL{};

typedef f32x2 cf;
DEV cf cmul(cf a, cf b) {
  cf r;
  asm("v_pk_mul_f32 %0, %1, %2 op_sel:[1,1] op_sel_hi:[1,0]\n\t"
      "v_pk_fma_f32 %0, %1, %2, %0 op_sel:[0,0,0] op_sel_hi:[0,1,1] neg_lo:[0,0,1]"
      : "=&v"(r) : "v"(a), "v"(b));
  return r;
}
DEV cf cscale(cf a, float s) { return a * s; }
DEV cf cexpk(float tl2, float rh, float rl, float s) {
  float mag = __builtin_amdgcn_exp2f(tl2 * s);
  float p = rh * s;
  float f = __builtin_fmaf(rl, s, p - __builtin_rintf(p));
  return cf{__builtin_amdgcn_cosf(f), __builtin_amdgcn_sinf(f)} * mag;
}
DEV void st_x(char* p, cf a) { *(u32*)p = pkh(a[0], a[1]); }
DEV void st_y(char* p, cf a) {
  u32 hi = pkh(a[0], a[1]);
  f32x2 r = a - hup(hi);
  *(u32*)p = hi;
  *(u32*)(p + 16 * KG_ROW) = pkh(r[0], r[1]);
}
DEV float pow2_scale(float mx, u32 target, float& inv) {
  u32 eb = __float_as_uint(mx) >> 23;
  u32 se = min(max(254u + target - eb, 1u), 253u);
  inv = __uint_as_float((254u - se) << 23);
  return __uint_as_float(se << 23);
}

DEV void conv_load1(const char* pa, const char* pb, int b0, int i, u32x4 (&A)[8], u32x4 (&B)[8]) {
  u32x2 lo = *(const volatile LDSP u32x2*)(pa + 64 * b0 + 512 * i), hi = *(const volatile LDSP u32x2*)(pa + 64 * b0 + 512 * i + 8);
  A[i] = u32x4{lo[0], lo[1], hi[0], hi[1]};
  B[i] = *(const u32x4*)(pb - 64 * b0 + 512 * i);
}
DEV void conv_step(const char* pa, const char* pb, int nb, const u32x4 (&A)[8], const u32x4 (&B)[8], u32x4 (&An)[8], u32x4 (&Bn)[8], f32x4 (&acc)[8]) {
  u32x2 lo[8], hi[8];
  int c = 0;
#pragma unroll
  for (int i = 0; i < 8; i++) {
#pragma unroll
    for (int t = i; t < 8; t++) {
      if (nb == 1 && i == 0) mmah0(acc[t], A[i], B[t - i]); else mmah(acc[t], A[i], B[t - i]);
      if (nb < 4 && c < 24) {
        const int f = c / 3, k = c % 3;
        if (k == 0) Bn[f] = *(const u32x4*)(pb - 64 * nb + 512 * f);
        else if (k == 1) lo[f] = *(const volatile LDSP u32x2*)(pa + 64 * nb + 512 * f);
        else hi[f] = *(const volatile LDSP u32x2*)(pa + 64 * nb + 512 * f + 8);
      }
      c++;
      SCHED();
    }
  }
  if (nb < 4) {
#pragma unroll
    for (int f = 0; f < 8; f++) An[f] = u32x4{lo[f][0], lo[f][1], hi[f][0], hi[f][1]};
  }
}

DEV u32 kn_voff(int ld, int w, int lane) {
  int kr = lane >> 3, pos = lane & 7;
  int f2 = ((lane >> 4) & 1) | ((w & 1) << 1);
  return (u32)((8 * w + kr) * ld * 2 + ((pos ^ (f2 << 1)) << 4));
}
DEV int kn_base(int lane) {
  int g = lane >> 4, q = (lane & 15) >> 2, pp = lane & 3;
  int f2 = ((q >> 1) & 1) | ((g & 1) << 1);
  return (8 * g + q) * 128 + (f2 << 5) + pp * 8;
}
DEV u32x4 kn_frag(const char* p) { u32x2 a = tr_read(p), b = tr_read(p + 512); return u32x4{a[0], a[1], b[0], b[1]}; }
DEV int mk_base(int lane, int ks) { int r16 = lane & 15, g = lane >> 4; return r16 * 128 + (((4 * ks + g) ^ (r16 & 7)) << 4); }
constexpr u32 ONES = 0x3f803f80u;
DEV u32x4 pair_swap(u32 p01, u32 p23, u32 q01, u32 q23) {
  auto r0 = __builtin_amdgcn_permlane16_swap(p01, q01, false, false);
  auto r1 = __builtin_amdgcn_permlane16_swap(p23, q23, false, false);
  return u32x4{r0[0], r1[0], r0[1], r1[1]};
}
template <int LDW> DEV void w64_issue(const u16* wsrc, u32 dst, int w, int lane) {
  const u32 vo = kn_voff(LDW, w, lane);
  const u32 d = dst + w * 1024;
  glds<0 * 8192, 0 * 128 * LDW>(vo, wsrc, d); glds<1 * 8192, 1 * 128 * LDW>(vo, wsrc, d);
  glds<2 * 8192, 2 * 128 * LDW>(vo, wsrc, d); glds<3 * 8192, 3 * 128 * LDW>(vo, wsrc, d);
  glds<4 * 8192, 4 * 128 * LDW>(vo, wsrc, d); glds<5 * 8192, 5 * 128 * LDW>(vo, wsrc, d);
  glds<6 * 8192, 6 * 128 * LDW>(vo, wsrc, d); glds<7 * 8192, 7 * 128 * LDW>(vo, wsrc, d);
}
__global__ __launch_bounds__(512, 2) void k_fused(Params p) {
  __shared__ __attribute__((aligned(16))) char smem[LDS_ALL];
  const int bid = blockIdx.x;
  const int tid = threadIdx.x, w = __builtin_amdgcn_readfirstlane(tid >> 6), lane = tid & 63;
  const int r16 = lane & 15, g = lane >> 4;
  const u32 lds0 = lds_addr(smem);
  const rsrc_t wsr = __builtin_amdgcn_make_buffer_rsrc(p.ws, 0, 0x4000000, 0x00020000);
  const int cp = (bid & 7) * 32 + (bid >> 3);
  const int pm = (bid & 7) * 4 + (bid >> 6), pn = (bid >> 3) & 7;
  const int row0 = pm * 64, col0 = pn * 64;
  u32x2 tag;
  u32 TKW = 0;
  {
    u32x2 ep = ld8_dev(wsr, FL_EPOCH);
    u32x4 xv = *(const u32x4*)(p.x + (bid * 8 + w) * D_ + lane * 8);
    const int chl = w >> 2, role = (w >> 1) & 1, n = tid & 127;
    const int ch = cp * 2 + chl;
    const int pi = ch * NS_ + n;
    u32 r_are = p.are[pi], r_aim = p.aim[pi], r_bre = p.bre[pi], r_bim = p.bim[pi], r_cre = p.cre[pi], r_cim = p.cim[pi];
    u32 r_ldt = p.logdt[ch];
    u32x4 kidx = *(const u32x4*)(KTBL.v + (tid & 255) * 8);
    SCHED();
    u32 sk0, sk1, sk2;
    {
      u64 pc; u32 len;
      asm volatile("s_getpc_b64 %0\n.Lk_here:\n\ts_mov_b32 %1, .Lk_end-.Lk_here" : "=s"(pc), "=s"(len));
      const u32 o = (u32)((bid >> 3) * 24 + w * 3) * 64;
      asm volatile("s_load_dword %0, %3, %4\n\ts_load_dword %1, %3, %5\n\ts_load_dword %2, %3, %6"
                   : "=&s"(sk0), "=&s"(sk1), "=&s"(sk2) : "s"(pc), "s"(min(o, len)), "s"(min(o + 64, len)), "s"(min(o + 128, len)) : "memory");
    }
    {
      u64 e = ((u64)ep[1] << 32 | ep[0]) + 1;
      tag = u32x2{(u32)__builtin_amdgcn_readfirstlane((u32)e), (u32)__builtin_amdgcn_readfirstlane((u32)(e >> 32))};
    }
    char* su = smem + P0_LN;
    float* ms = (float*)(smem + P0_MS);
    {
      f32x2 v[4];
#pragma unroll
      for (int i = 0; i < 4; i++) v[i] = bfup(xv[i]);
      f32x2 s2 = (v[0] + v[1]) + (v[2] + v[3]);
      float mean = wave_sum(s2[0] + s2[1]) * (1.0f / D_);
      f32x2 q2 = {0, 0};
#pragma unroll
      for (int i = 0; i < 4; i++) { f32x2 d = v[i] - mean; q2 += d * d; }
      float var = wave_sum(q2[0] + q2[1]) * (1.0f / D_);
      float m = rbf(mean);
      float inv = __builtin_amdgcn_rcpf(rbf(__builtin_amdgcn_sqrtf(rbf(rbf(var) + 1e-5f))));
      u32x4 uo;
#pragma unroll
      for (int i = 0; i < 4; i++) {
        f32x2 u = bfup(pkbf2(bfup(pkbf2(v[i] - m)) * inv));
        uo[i] = pkh(u[0], u[1]);
      }
      u32x4 lo4, hi4;
#pragma unroll
      for (int i = 0; i < 2; i++) {
        lo4[2 * i] = __builtin_amdgcn_perm(xv[i], uo[i], 0x05040100u); lo4[2 * i + 1] = __builtin_amdgcn_perm(xv[i], uo[i], 0x07060302u);
        hi4[2 * i] = __builtin_amdgcn_perm(xv[2 + i], uo[2 + i], 0x05040100u); hi4[2 * i + 1] = __builtin_amdgcn_perm(xv[2 + i], uo[2 + i], 0x07060302u);
      }
      *(u32x4*)(su + w * 2112 + lane * 32) = lo4;
      *(u32x4*)(su + w * 2112 + lane * 32 + 16) = hi4;
    }
    asm volatile("" : "+v"(kidx));
    lds_bar();
    asm volatile("" :: "s"(sk0), "s"(sk1), "s"(sk2));
    {
      const int q4 = (lane & 15) >> 2, pp = lane & 3;
      const char* a = su + q4 * 2112 + (128 * w + 16 * g + 4 * pp) * 2;
      u32x2 u0 = tr_read(a), u1 = tr_read(a + 4 * 2112), x0 = tr_read(a + 128), x1 = tr_read(a + 4 * 2112 + 128);
      const u32 dst = WS_U8 + bid * 16384 + w * 2048 + lane * 16;
      st16_dev(wsr, dst, u32x4{u0[0], u0[1], u1[0], u1[1]});
      st16_dev(wsr, dst + 1024, u32x4{x0[0], x0[1], x1[0], x1[1]});
    }
    float dt = rbf(expf(bf2f(r_ldt)));
    float ar = dt * bf2f(r_are), ai = dt * bf2f(r_aim);
    cf CB = cmul(cf{bf2f(r_cre), bf2f(r_cim)}, cf{dt * bf2f(r_bre), dt * bf2f(r_bim)});
    float tl2 = ar * 1.4426950408889634f;
    const float I2PH = 0.15915494309189535f;
    const float I2PL = (float)(0.15915494309189533576888 - (double)0.15915494309189535f);
    float rh = ai * I2PH;
    float rl = __builtin_fmaf(ai, I2PH, -rh) + ai * I2PL;
    {
      float bs = wave_sum(fabsf(CB[0]) + fabsf(CB[1])), isx;
      float sx = pow2_scale(bs, 14, isx);
      CB = cscale(CB, sx);
      if (role == 0 && lane == 0) { ms[chl * 2 + (w & 1)] = bs; ms[4 + chl * 2 + (w & 1)] = isx; }
    }
    {
      const u16* wb = w == 0 ? p.w3 : w == 1 ? p.w4 : w == 2 ? p.w1 : p.w2;
      touch(TKW, wb, w < 2 ? (u32)(bid * 56 + min(lane, 55)) * 128 : (u32)(bid * 16 + (lane & 15)) * 128);
    }
    char* img = smem + chl * KG_CH;
    {
      char* wp = img + n * 4;
      if (role == 0) {
        cf e3 = cexpk(tl2, -rh, -rl, 8.f), e4 = cexpk(tl2, -rh, -rl, 16.f), e5 = cexpk(tl2, -rh, -rl, 32.f), e6 = cexpk(tl2, -rh, -rl, 64.f);
        cf y[8];
        y[0] = cf{16384.f, 0.f}; y[1] = cscale(e3, 16384.f); y[2] = cscale(e4, 16384.f); y[3] = cmul(y[2], e3);
        y[4] = cscale(e5, 16384.f); y[5] = cmul(y[4], e3); y[6] = cmul(y[4], e4); y[7] = cmul(y[6], e3);
#pragma unroll
        for (int j = 0; j < 8; j++) st_y(wp + (41 + j) * KG_ROW, y[j]);
#pragma unroll
        for (int j = 0; j < 8; j++) st_y(wp + (49 + j) * KG_ROW, j ? cmul(y[j], e6) : cscale(e6, 16384.f));
        cf e7 = cexpk(tl2, rh, rl, 128.f), e8 = cexpk(tl2, rh, rl, 256.f), e9 = cexpk(tl2, rh, rl, 512.f);
        cf e10 = cexpk(tl2, rh, rl, 1024.f), e11 = cexpk(tl2, rh, rl, 2048.f);
        cf g8 = cmul(CB, e10), g12 = cmul(g8, e9), g16 = cmul(CB, e11);
        cf g10 = cmul(g8, e8), g14 = cmul(g12, e8);
        st_x(wp + 32 * KG_ROW, g8); st_x(wp + 33 * KG_ROW, cmul(g8, e7));
        st_x(wp + 34 * KG_ROW, g10); st_x(wp + 35 * KG_ROW, cmul(g10, e7));
        st_x(wp + 36 * KG_ROW, g12); st_x(wp + 37 * KG_ROW, cmul(g12, e7));
        st_x(wp + 38 * KG_ROW, g14); st_x(wp + 39 * KG_ROW, cmul(g14, e7));
        st_x(wp + 40 * KG_ROW, g16);
      } else {
        cf e0 = cexpk(tl2, rh, rl, 1.f), e1 = cexpk(tl2, rh, rl, 2.f), e2 = cexpk(tl2, rh, rl, 4.f);
        cf e7 = cexpk(tl2, rh, rl, 128.f), e8 = cexpk(tl2, rh, rl, 256.f), e9 = cexpk(tl2, rh, rl, 512.f);
        cf eb[8];
        eb[1] = e0; eb[2] = e1; eb[3] = cmul(e1, e0); eb[4] = e2; eb[5] = cmul(e2, e0); eb[6] = cmul(e2, e1); eb[7] = cmul(eb[6], e0);
        cf gq[8];
        gq[0] = CB; gq[1] = cmul(CB, e7); gq[2] = cmul(CB, e8); gq[3] = cmul(gq[2], e7);
        gq[4] = cmul(CB, e9); gq[5] = cmul(gq[4], e7); gq[6] = cmul(gq[4], e8); gq[7] = cmul(gq[6], e7);
#pragma unroll
        for (int q = 0; q < 2; q++)
#pragma unroll
          for (int b = 0; b < 8; b++) st_x(wp + (q * 8 + b) * KG_ROW, b ? cmul(gq[q], eb[b]) : gq[q]);
#pragma unroll
        for (int q = 2; q < 4; q++)
#pragma unroll
          for (int b = 0; b < 4; b++) st_x(wp + (16 + (q - 2) * 4 + b) * KG_ROW, b ? cmul(gq[q], eb[2 * b]) : gq[q]);
#pragma unroll
        for (int q = 4; q < 8; q++)
#pragma unroll
          for (int b = 0; b < 2; b++) st_x(wp + (24 + (q - 4) * 2 + b) * KG_ROW, b ? cmul(gq[q], e2) : gq[q]);
      }
    }
    wait_vm_lgkm<1>(); bar();
    if (tid < 16) st8_dev(wsr, FL_1 + tid * 2048 + bid * 8, tag);
    char* kt = smem + P0_KT + chl * 12288;
    float kinv;
    const float kscale = pow2_scale(ms[chl * 2] + ms[chl * 2 + 1], 14, kinv);
    const float fsc = ms[4 + chl * 2 + ((w & 3) >> 1)] * (kscale * (1.0f / 16384.f));
    {
      const int wv = w & 3;
      const char* pa = img + r16 * KG_ROW + wv * 128 + g * 16;
      u32x4 a[3][2], yh[2], yl[2];
#pragma unroll
      for (int s = 0; s < 2; s++) {
#pragma unroll
        for (int t = 0; t < 3; t++) a[t][s] = *(const u32x4*)(pa + t * 16 * KG_ROW + s * 64);
        yh[s] = *(const u32x4*)(pa + 41 * KG_ROW + s * 64);
        yl[s] = *(const u32x4*)(pa + 57 * KG_ROW + s * 64);
      }
      const float f = fsc;
      char* kw = kt + (4 * g * 16 + r16) * 16 + ((wv + (r16 >> 3) + 2 * (g & 1)) & 3) * 4;
      f32x4 c[3];
#pragma unroll
      for (int t = 0; t < 3; t++) { mmah0(c[t], a[t][0], yl[0]); mmah(c[t], a[t][1], yl[1]); }
#pragma unroll
      for (int t = 0; t < 3; t++) { mmah(c[t], a[t][0], yh[0]); mmah(c[t], a[t][1], yh[1]); }
      mfence(); pin(c[0]); pin(c[1]); pin(c[2]);
#pragma unroll
      for (int t = 0; t < 3; t++)
#pragma unroll
        for (int r = 0; r < 4; r++) *(float*)(kw + (t * 16 + r) * 256) = c[t][r] * f;
    }
    if (w == 0) {
      poll256(wsr, FL_1 + (bid & 15) * 2048, tag, lane);
      if (bid == 0 && lane == 0) st8_dev(wsr, FL_EPOCH, tag);
    }
    lds_bar();
    {
      const char* sa = p.ws + WS_U8 + (size_t)w * 16 * 16384 + cp * 64;
      const u32 vo = (u32)((lane >> 2) * 16384 + (lane & 3) * 16);
      gldsd<CV_UI, 0>(vo, sa, lds0 + w * 1024); gldsd<CV_UI + 8192, 128 * 16384>(vo, sa, lds0 + w * 1024);
    }
    {
      u32x4 o;
#pragma unroll
      for (int i = 0; i < 4; i++) {
        f32x4 a = *(const f32x4*)(kt + (kidx[i] & 0xffffu)), b = *(const f32x4*)(kt + (kidx[i] >> 16));
        o[i] = pkh((a[0] + a[1]) + (a[2] + a[3]), (b[0] + b[1]) + (b[2] + b[3]));
      }
      const int e = tid & 255;
      char* base = smem + chl * CV_CH;
      *(u32x4*)(base + CV_KP + (30 + e) * 16) = o;
      if (e < 30) *(u32x4*)(base + CV_KP + e * 16) = u32x4{0, 0, 0, 0};
      else if (e < 34) *(u32x4*)(base + (e - 30) * CV_COPY) = u32x4{0, 0, 0, 0};
      else if (e == 34) *(float*)(base + CV_KP + 4576) = kinv;
    }
  }
  {
    {
      const int chl = w >> 2, q = tid & 255;
      char* base = smem + chl * CV_CH;
      wait_vm<0>(); keep(TKW); bar();
      const char* ui = smem + CV_UI + q * 64 + chl * 32;
      u32x4 cur = *(const u32x4*)ui, prv = *(const u32x4*)(ui - 64);
      if (q == 0) prv = u32x4{0, 0, 0, 0};
      u32 wd[8] = {prv[0], prv[1], prv[2], prv[3], cur[0], cur[1], cur[2], cur[3]};
      u32 o[5];
#pragma unroll
      for (int k = 0; k < 5; k++) o[k] = __builtin_amdgcn_alignbit(wd[k + 1], wd[k], 16);
      char* d = base + (q + 1) * 16;
      *(u32x4*)(d) = u32x4{o[0], o[1], o[2], o[3]};
      *(u32x4*)(d + CV_COPY) = u32x4{wd[1], wd[2], wd[3], wd[4]};
      *(u32x4*)(d + 2 * CV_COPY) = u32x4{o[1], o[2], o[3], o[4]};
      *(u32x4*)(d + 3 * CV_COPY) = u32x4{wd[2], wd[3], wd[4], wd[5]};
      if (q == 255) {
        u32 o4 = __builtin_amdgcn_alignbit(cur[1], cur[0], 16), o5 = __builtin_amdgcn_alignbit(cur[2], cur[1], 16);
        u32 o6 = __builtin_amdgcn_alignbit(cur[3], cur[2], 16), o7 = cur[3] >> 16;
        char* d2 = base + 257 * 16;
        *(u32x4*)(d2) = u32x4{o4, o5, o6, o7};
        *(u32x4*)(d2 + CV_COPY) = u32x4{cur[1], cur[2], cur[3], 0};
        *(u32x4*)(d2 + 2 * CV_COPY) = u32x4{o5, o6, o7, 0};
        *(u32x4*)(d2 + 3 * CV_COPY) = u32x4{cur[2], cur[3], 0, 0};
      }
    }
    const int chl = (w >> 1) & 1, hh = w & 1;
    const float dsk = bf2f(p.dskip[cp * 2 + chl]);
    const char* base = smem + chl * CV_CH;
    const int j2 = 2 * (hh + 2 * (w >> 2));
    char* red = smem + CV_RED + chl * 16384 + lane * 16;
    lds_bar();
    f32x4 f0 = {0, 0, 0, 0}, f1 = {0, 0, 0, 0};
    if (w < 4) {
      f32x4 acc[8];
      const char* pa = base + (r16 & 3) * CV_COPY + 48 - 16 * g + 2 * (r16 & 12) + 256 * hh;
      const char* pb = base + CV_KP + 448 + 32 * r16 + 16 * g - 256 * hh;
      u32x4 A0[8], B0[8], A1[8], B1[8];
#pragma unroll
      for (int i = 0; i < 8; i++) conv_load1(pa, pb, 0, i, A0, B0);
      conv_step(pa, pb, 1, A0, B0, A1, B1, acc);
      conv_step(pa, pb, 2, A1, B1, A0, B0, acc);
      conv_step(pa, pb, 3, A0, B0, A1, B1, acc);
      conv_step(pa, pb, 4, A1, B1, A0, B0, acc);
      mfence();
#pragma unroll
      for (int i = 0; i < 8; i++) pin(acc[i]);
      char* d = red + hh * 8192;
      if (hh) { *(f32x4*)(d) = acc[0]; *(f32x4*)(d + 1024) = acc[1]; f0 = acc[2]; f1 = acc[3]; }
      else { *(f32x4*)(d + 2048) = acc[2]; *(f32x4*)(d + 3072) = acc[3]; f0 = acc[0]; f1 = acc[1]; }
#pragma unroll
      for (int t = 4; t < 8; t++) *(f32x4*)(d + t * 1024) = acc[t];
    } else {
      w64_issue<D_>(p.w1 + col0, lds0 + GA_B, w - 4, lane);
      w64_issue<D_>(p.w1 + col0, lds0 + GA_B, w, lane);
      wait_vm<0>();
    }
    const float kinv = *(const float*)(base + CV_KP + 4576);
    const int l0 = 256 * j2 + 16 * r16 + 4 * g;
    u32x2 xx[2];
    u32 dd[2][2];
#pragma unroll
    for (int k = 0; k < 2; k++) {
      const char* ui = smem + CV_UI + ((l0 + 256 * k) >> 3) * 64 + chl * 32 + (l0 & 4) * 2;
      u32x2 uu = *(const u32x2*)ui;
      xx[k] = *(const u32x2*)(ui + 16);
      dd[k][0] = pkbf2(hup(uu[0]) * dsk); dd[k][1] = pkbf2(hup(uu[1]) * dsk);
    }
    lds_bar();
    if (w < 4) {
      f0 += *(const f32x4*)(red + (hh ^ 1) * 8192 + j2 * 1024);
      f1 += *(const f32x4*)(red + (hh ^ 1) * 8192 + j2 * 1024 + 1024);
    } else {
      f0 = *(const f32x4*)(red + j2 * 1024) + *(const f32x4*)(red + 8192 + j2 * 1024);
      f1 = *(const f32x4*)(red + j2 * 1024 + 1024) + *(const f32x4*)(red + 8192 + j2 * 1024 + 1024);
    }
    char* stg = smem + CV_STG;
#pragma unroll
    for (int k = 0; k < 2; k++) {
      f32x4 f = k ? f1 : f0;
      u32 y01 = pkbf2(f32x2{f[0], f[1]} * kinv), y23 = pkbf2(f32x2{f[2], f[3]} * kinv);
      u32 s01 = pkbf2(bfup(y01) + bfup(dd[k][0])), s23 = pkbf2(bfup(y23) + bfup(dd[k][1]));
      u32 h01 = pkbf2(bfup(xx[k][0]) + bfup(s01)), h23 = pkbf2(bfup(xx[k][1]) + bfup(s23));
      *(u32x2*)(stg + (16 * (j2 + k) + r16) * 80 + chl * 32 + g * 8) = u32x2{h01, h23};
    }
    lds_bar();
    {
      const int lb = tid >> 2, pc = tid & 3;
      st16_dev(wsr, WS_HT + cp * 8192 + tid * 16, *(const u32x4*)(stg + lb * 80 + pc * 16));
    }
  }
  const int mt = w & 3, wn = w >> 2;
  const int rt = (pm >> 1) * 8 + (pm & 1) * 2 + (mt >> 1) * 4 + (mt & 1);
  const int pc4 = (g & 1) * 2 + (g >> 1);
  const u32 boff = (u32)((rt * 16 + pn * 2 + wn) * 1024 + r16 * 64 + pc4 * 16);
  u32x4 ONES4 = {ONES, ONES, ONES, ONES};
  asm volatile("s_nop 1" : "+v"(ONES4));
  const u32 dw = lds0 + w * 1024;
  const u32 vbD = kn_voff(D_, w, lane);
  u32x4 hres, xres;
  u32x4 B[16][2];
  constexpr int CBD = 128 * D_, CB3 = 128 * F_;
  const int idx3 = bid >> 3;
  const bool has3 = idx3 < 28;
  const int pm3 = (bid & 7) * 2 + (idx3 >= 14 ? 1 : 0), pn3 = idx3 >= 28 ? idx3 - 28 : idx3 >= 14 ? idx3 - 14 : idx3;
  const u16* sb3 = p.w3 + pn3 * 128;
  const u32 vbF = kn_voff(F_, w, lane);
  const u32 vbF4 = kn_voff(F_, w - 4, lane);
  const int wm3 = w & 1, wn3 = w >> 1;
  const int ob0 = (kn_base(lane) + (wn3 >> 1) * 8192) ^ ((2 * (wn3 & 1)) << 5), ob1 = ob0 ^ 32;
  f32x4 cw0, cw1;
  u32x4 Bt[2][8];
  const char* pb0 = smem + GA_B + (kn_base(lane) ^ ((2 * wn) << 5));
  const char* pb1 = smem + GA_B + (kn_base(lane) ^ ((2 * wn + 1) << 5));
  auto ldB = [&](int j) {
#pragma unroll
    for (int i = 0; i < 4; i++) { Bt[j & 1][2 * i] = kn_frag(pb0 + (4 * j + i) * 4096); Bt[j & 1][2 * i + 1] = kn_frag(pb1 + (4 * j + i) * 4096); }
    SCHED();
  };
  auto ldB3 = [&](int s0, int n, int o) {
#pragma unroll
    for (int s = 0; s < 8; s++) if (s < n) {
      B[s0 + s][0] = kn_frag(smem + o + (s >> 1) * 16384 + (s & 1) * 4096 + ob0);
      B[s0 + s][1] = kn_frag(smem + o + (s >> 1) * 16384 + (s & 1) * 4096 + ob1);
    }
#pragma unroll
    for (int s = 0; s < 8; s++) if (s < n) { mmaz(s0 + s == 0, cw0, B[s0 + s][0], ONES4); mmaz(s0 + s == 0, cw1, B[s0 + s][1], ONES4); }
  };
#define W2C(c) glds<GA_B + (c) * 8192, (c) * CBD>(vbD, p.w2 + col0, dw);
#define W3C(c, dst) glds<(dst), (c) * CB3>(vbF, sb3, dw); glds<(dst) + 8192, (c) * CB3 + 128>(vbF, sb3, dw);
#define W3C4(c, dst) W3C(c, dst) glds<(dst), (c) * CB3>(vbF4, sb3, dw - 4096); glds<(dst) + 8192, (c) * CB3 + 128>(vbF4, sb3, dw - 4096);
  {
    f32x4 cv0, cv1;
    {
#pragma unroll
      for (int i = 0; i < 4; i++) { Bt[0][2 * i] = kn_frag(pb0 + (4 * mt + i) * 4096); Bt[0][2 * i + 1] = kn_frag(pb1 + (4 * mt + i) * 4096); }
#pragma unroll
      for (int i = 0; i < 4; i++) { mmaz(i == 0, cv0, Bt[0][2 * i], ONES4); mmaz(i == 0, cv1, Bt[0][2 * i + 1], ONES4); }
      mfence(); pin(cv0); pin(cv1);
      char* ct = smem + 65536 + wn * 128 + g * 16;
      if (r16 == 0) { *(f32x4*)(ct + mt * 256) = cv0; *(f32x4*)(ct + mt * 256 + 64) = cv1; }
      lds_bar();
      cv0 = (*(const f32x4*)(ct) + *(const f32x4*)(ct + 256)) + (*(const f32x4*)(ct + 512) + *(const f32x4*)(ct + 768));
      cv1 = (*(const f32x4*)(ct + 64) + *(const f32x4*)(ct + 320)) + (*(const f32x4*)(ct + 576) + *(const f32x4*)(ct + 832));
    }
    wait_vm_lgkm<0>(); bar();
    if (tid < 16) st8_dev(wsr, FL_2 + tid * 2048 + bid * 8, tag);
    if (w >= 4) { W3C4(0, 65536) W3C4(1, 81920) }
    ldB(0); ldB(1);
    if (w == 0) poll256(wsr, FL_2 + (bid & 15) * 2048, tag, lane);
    bar();
    {
      const int v4 = (mt & 1) + 2 * wn;
      const char* sa = p.ws + WS_HT + (rt & ~1) * 64 + (size_t)v4 * 131072;
      const u32 vo = (u32)((lane >> 3) * 8192 + ((lane & 7) ^ (((lane >> 5) & 1) << 2)) * 16);
      const u32 d = lds0 + GA_A + (mt >> 1) * 32768 + v4 * 2048;
      gldsd<0, 0>(vo, sa, d); gldsd<1024, 65536>(vo, sa, d); gldsd<8192, 4 * 131072>(vo, sa, d); gldsd<9216, 4 * 131072 + 65536>(vo, sa, d);
      gldsd<16384, 8 * 131072>(vo, sa, d); gldsd<17408, 8 * 131072 + 65536>(vo, sa, d); gldsd<24576, 12 * 131072>(vo, sa, d); gldsd<25600, 12 * 131072 + 65536>(vo, sa, d);
    }
    const char* aw = smem + GA_A + (mt >> 1) * 32768 + ((lane & 15) >> 3) * 128 + ((lane >> 2) & 1) * 32 + (lane & 3) * 8;
    const char* pa0 = aw + g * 512 + (((mt ^ g) & 1) << 6);
    f32x4 acc0, acc1, sacc, gacc;
    u32x4 a[2][4];
    auto ld = [&](int j) {
#pragma unroll
      for (int i = 0; i < 4; i++) {
        u32x2 x0 = tr_read(pa0 + (4 * j + i) * 2048), x1 = tr_read(pa0 + (4 * j + i) * 2048 + 256);
        a[j & 1][i] = u32x4{x0[0], x0[1], x1[0], x1[1]};
      }
      SCHED();
    };
    auto mm = [&](int j) {
#pragma unroll
      for (int i = 0; i < 4; i++) {
        mmaz(j + i == 0, sacc, ONES4, a[j & 1][i]);
        mmaz(j + i == 0, gacc, a[j & 1][i], a[j & 1][i]);
        mmaz(j + i == 0, acc0, Bt[j & 1][2 * i], a[j & 1][i]);
        mmaz(j + i == 0, acc1, Bt[j & 1][2 * i + 1], a[j & 1][i]);
      }
      SCHED();
    };
    SCHED();
    wait_vm<6>(); bar(); ld(0);
    mm(0); ldB(2);
    wait_vm<4>(); bar(); ld(1); mm(1); ldB(3);
    wait_vm<2>(); bar(); ld(2); mm(2);
    wait_vm_lgkm<0>(); bar(); ld(3);
    W2C(0) W2C(1)
    const char* hs = aw + (2 * pn + wn) * 2048 + pc4 * 512 + (((mt ^ pc4) & 1) << 6);
    u32x2 hx0 = tr_read(hs), hx1 = tr_read(hs + 256);
    SCHED(); mm(3); W2C(2) W2C(3)
    mfence(); pin(acc0); pin(acc1); pin(sacc); pin(gacc);
    float dg = (r16 & 2) ? ((r16 & 1) ? gacc[3] : gacc[2]) : ((r16 & 1) ? gacc[1] : gacc[0]);
    f32x2 sa2 = {sacc[0], gsum(g == (r16 >> 2) ? dg : 0.f)};
    float mean1 = sa2[0] * (1.0f / D_);
    float var1 = fmaxf(sa2[1] * (1.0f / D_) - mean1 * mean1, 0.f);
    float m1 = rbf(mean1);
    float sd1 = rbf(__builtin_amdgcn_sqrtf(rbf(rbf(var1) + 1e-5f)));
    float i1 = __builtin_amdgcn_rcpf(sd1);
    float m2 = rbf((mean1 - m1) * i1);
    float sd2 = rbf(__builtin_amdgcn_sqrtf(rbf(rbf(var1 * i1 * i1) + 1e-5f)));
    float rs = i1 * __builtin_amdgcn_rcpf(sd2), tt = -(m1 + m2 * sd1) * rs;
    {
      f32x4 rt4 = {rs, rs, tt, tt};
      pin(rt4);
      u32x2 o0 = act4(acc0, cv0, rt4), o1 = act4(acc1, cv1, rt4);
      st16_dev(wsr, WS_H1 + 16384 + boff, pair_swap(o0[0], o0[1], o1[0], o1[1]));
    }
    if (tid < 16) {
      const u32 o = (u32)((pn * 2 + (tid >> 3)) * 1024 + (tid & 7) * 16);
      if (pm == 0) st16_dev(wsr, WS_H1 + 14 * 64 + o, u32x4{0, 0, 0, 0});
      if (pm == 31) st16_dev(wsr, WS_H1 + 129 * 16384 + o, u32x4{0, 0, 0, 0});
    }
    hres = u32x4{hx0[0], hx0[1], hx1[0], hx1[1]};
#pragma unroll
    for (int i = 0; i < 4; i++) xres[i] = pkbf2(bfup(pkbf2(bfup(hres[i]) - m1)) * i1);
    W2C(4) W2C(5) W2C(6) W2C(7)
    wait_vm_lgkm<4>(); bar();
    if (tid < 8) st8_dev(wsr, FL_3 + tid * 2048 + (pm * 8 + pn) * 8, tag);
    ldB3(0, 4, 65536);
    ldB(0); ldB(1);
  }
  const int ar = lane >> 3, asp = (lane & 7) ^ ar;
  const u32 apo = (u32)((asp >> 2) * 1024 + (asp & 3) * 16);
  {
    u32 vo[8];
#pragma unroll
    for (int k = 0; k < 8; k++) {
      const int sh = (k >> 1) == 0 ? -1 : (k >> 1) == 1 ? 1 : (k >> 1) == 2 ? -2 : 2;
      const int row = 16 + ar + (k & 1) * 8 + sh;
      vo[k] = (u32)((row >> 4) * 16384 + (row & 15) * 64) + apo;
    }
    if (w == 0) polln(wsr, FL_3 + pn * 2048, max((pm & ~1) - 1, 0) * 8, min((pm & ~1) + 3, 32) * 8, tag, lane);
    lds_bar();
    {
      const char* sa = p.ws + WS_H1 + (size_t)rt * 16384 + wn * 2048;
      const u32 d = lds0 + GA_A + mt * 16384 + wn * 2048;
      gldsd<0, 0>(vo[0], sa, d); gldsd<1024, 0>(vo[1], sa, d); gldsd<4096, 4096>(vo[2], sa, d); gldsd<5120, 4096>(vo[3], sa, d);
      gldsd<8192, 8192>(vo[4], sa, d); gldsd<9216, 8192>(vo[5], sa, d); gldsd<12288, 12288>(vo[6], sa, d); gldsd<13312, 12288>(vo[7], sa, d);
    }

    const char* pa0 = smem + GA_A + mt * 16384 + mk_base(lane, 0);
    const char* pa1 = smem + GA_A + mt * 16384 + mk_base(lane, 1);
    f32x4 acc0, acc1;
    u32x4 a[2][4];
    auto ld = [&](int j) {
      a[j & 1][0] = *(const u32x4*)(pa0 + j * 4096); a[j & 1][1] = *(const u32x4*)(pa1 + j * 4096);
      a[j & 1][2] = *(const u32x4*)(pa0 + j * 4096 + 2048); a[j & 1][3] = *(const u32x4*)(pa1 + j * 4096 + 2048);
      SCHED();
    };
    auto mm = [&](int j) {
#pragma unroll
      for (int i = 0; i < 4; i++) {
        mmaz(j + i == 0, acc0, Bt[j & 1][2 * i], a[j & 1][i]);
        mmaz(j + i == 0, acc1, Bt[j & 1][2 * i + 1], a[j & 1][i]);
      }
      SCHED();
    };
    SCHED();
    wait_vm<6>(); bar(); ld(0);
    wait_vm<4>(); bar(); ld(1); mm(0); ldB(2);
    wait_vm<2>(); bar(); ld(2); mm(1); ldB(3);
    wait_vm_lgkm<0>(); bar(); ld(3);
    W3C(2, 65536)
    mm(2); W3C(3, 81920) mm(3);
    mfence(); pin(acc0); pin(acc1);
    u32x4 v = pair_swap(pkbf(acc0[0], acc0[1]), pkbf(acc0[2], acc0[3]), pkbf(acc1[0], acc1[1]), pkbf(acc1[2], acc1[3]));
#pragma unroll
    for (int i = 0; i < 4; i++) xres[i] = pkbf2(bfup(xres[i]) + bfup(v[i]));
    st16_dev(wsr, WS_X1 + boff, xres);
    float s1 = 0, s2 = 0;
#pragma unroll
    for (int i = 0; i < 4; i++) { s1 = dot2(xres[i], ONES, s1); s2 = dot2(xres[i], xres[i], s2); }
    s1 = gsum(s1); s2 = gsum(s2);
    if (g == 0) st8_dev(wsr, WS_ST + (rt * 16 + pn * 2 + wn) * 128 + r16 * 8, u32x2{__float_as_uint(s1), __float_as_uint(s2)});
    W3C(4, GA_B) W3C(5, GA_B + 16384)
    wait_vm_lgkm<4>(); bar();
    if (tid < 8) st8_dev(wsr, FL_4 + tid * 2048 + (pm * 8 + pn) * 8, tag);
    W3C(6, GA_B + 32768) W3C(7, GA_B + 49152)
  }
  const u16* sb4 = p.w4 + col0;
#define G4_R1A(i) glds<GA_B + (i) * 8192, (((i) >> 1) * 7 + ((i) & 1)) * CBD>(vbD, sb4, dw);
#define G4_R1B(i) glds<((i) < 2 ? G3_ST : 0) + (i) * 8192, (((i) >> 1) * 7 + 2 + ((i) & 1)) * CBD>(vbD, sb4, dw);
#define G4_R2POS(i) ((i) < 4 ? 32768 + (i) * 8192 : GA_B + ((i) - 4) * 8192)
#define G4_R2(i) glds<G4_R2POS(i), (((i) / 3) * 7 + 4 + ((i) % 3)) * CBD>(vbD, sb4, dw);
  const int wm4 = mt >> 1, kq = (mt & 1) + 2 * wn;
  const int kb = kn_base(lane);
  u32x4(&B4)[8][4] = *reinterpret_cast<u32x4(*)[8][4]>(&B);
  const char* sa4 = p.ws + WS_HID + (size_t)((rt & ~1) * 56 + 14 * kq) * 1024;
  const u32 va4 = (u32)(ar * 64) + apo;
  const int ring = (wm4 ? 0 : 65536) + kq * 8192;
  const u32 da = lds0 + ring;
#define G4_A4(b) gldsd<((b) & 1) * 4096, (b) * 2048>(va4, sa4, da); gldsd<((b) & 1) * 4096 + 1024, (b) * 2048 + 512>(va4, sa4, da); \
                 gldsd<((b) & 1) * 4096 + 2048, (b) * 2048 + 57344>(va4, sa4, da); gldsd<((b) & 1) * 4096 + 3072, (b) * 2048 + 57344 + 512>(va4, sa4, da);
  auto ldB4 = [&](int s) {
    const char* p0 = smem + GA_B + kq * 16384 + s * 4096;
    const char* p1 = smem + (kq ? kq * 16384 : G3_ST) + s * 4096;
#pragma unroll
    for (int nt = 0; nt < 4; nt++) { B4[s][nt] = kn_frag(p0 + (kb ^ (nt << 5))); B4[4 + s][nt] = kn_frag(p1 + (kb ^ (nt << 5))); }
    SCHED();
  };
  {
    const int wm = wm3;
    ldB3(4, 4, 65536);
    wait_vm<0>(); bar();
    ldB3(8, 8, GA_B);
    if (w == 0) polln(wsr, FL_4 + pn * 2048, pm3 * 16, pm3 * 16 + 16, tag, lane);
    lds_bar();
    const char* sa = p.ws + WS_X1 + (size_t)(pm3 * 8 + (w >> 1)) * 16384 + (w & 1) * 512;
    const u32 va = (u32)(ar * 64) + apo;
#define G3_A(i) gldsd<(((i) + 4) % 9) * 8192, ((i) >> 3) * 65536 + ((i) & 7) * 2048>(va, sa, dw);
    {
      const char* ss = p.ws + WS_ST + (size_t)pm3 * 16384 + w * 1024;
      gldsd<G3_ST, 0>(lane * 16, ss, dw); gldsd<G3_ST + 8192, 8192>(lane * 16, ss, dw);
    }
    G3_A(0) G3_A(1) G3_A(2) G3_A(3) G3_A(4) G3_A(5) G3_A(6) G3_A(7) G3_A(8)
    wait_vm<9>(); bar();
    {
      const char* sst = smem + G3_ST + (tid >> 6) * 2048 + (tid & 3) * 512 + ((tid >> 2) & 15) * 8;
      f32x2 st = (*(const f32x2*)(sst) + *(const f32x2*)(sst + 128)) + (*(const f32x2*)(sst + 256) + *(const f32x2*)(sst + 384));
      float t1 = st[0] + dppf<0xB1>(st[0]), t2 = st[1] + dppf<0xB1>(st[1]);
      t1 += dppf<0x4E>(t1); t2 += dppf<0x4E>(t2);
      float mean = t1 * (1.0f / D_);
      float var = fmaxf(t2 * (1.0f / D_) - mean * mean, 0.f);
      float rs = __builtin_amdgcn_rcpf(rbf(__builtin_amdgcn_sqrtf(rbf(rbf(var) + 1e-5f))));
      if ((tid & 3) == 0) { float tt = -rbf(mean) * rs; *(f32x4*)(smem + G3_TAB + (tid >> 2) * 16) = f32x4{rs, rs, tt, tt}; }
    }
    const f32x4& cv0 = cw0;
    const f32x4& cv1 = cw1;
    float* rowt = (float*)(smem + G3_TAB);
    const int oa0 = mk_base(lane, 0) + wm * 4096, oa1 = mk_base(lane, 1) + wm * 4096;
    f32x4 acc[2][2][2];
    u32x4 a[2][4];
    auto ld = [&](int slot, u32x4 (&d)[4]) {
      const char* s = smem + slot * 8192;
      d[0] = *(const u32x4*)(s + oa0); d[1] = *(const u32x4*)(s + oa0 + 2048);
      d[2] = *(const u32x4*)(s + oa1); d[3] = *(const u32x4*)(s + oa1 + 2048);
      SCHED();
    };
    auto m1 = [&](int ps, int b, int i, const u32x4 (&d)[4]) {
      mmaz(b == 0 && i < 4, acc[ps][(i >> 1) & 1][i & 1], B[2 * b + (i >> 2)][(i >> 1) & 1], d[2 * (i >> 2) + (i & 1)]);
      SCHED();
    };
    auto mm = [&](int ps, int b, const u32x4 (&d)[4]) {
#pragma unroll
      for (int i = 0; i < 8; i++) m1(ps, b, i, d);
    };
    const u32 hoff = (has3 ? WS_HID : 0x40000000u) + (u32)(((pm3 * 8 + 2 * wm) * 56 + pn3 * 4 + wn3) * 1024 + r16 * 64 + pc4 * 16);
    u32x2 o0;
    f32x4 rtv[2];
    auto ldrt = [&](int ps) {
      rtv[0] = *(const f32x4*)(rowt + (64 * ps + 32 * wm + r16) * 4);
      rtv[1] = *(const f32x4*)(rowt + (64 * ps + 32 * wm + 16 + r16) * 4);
    };
    auto ep = [&](int ps, int i) {
      const int mi = i >> 1;
      if ((i & 1) == 0) o0 = act4(acc[ps][0][mi], cv0, rtv[mi]);
      else {
        u32x2 o1 = act4(acc[ps][1][mi], cv1, rtv[mi]);
        st16_dev(wsr, hoff + (4 * ps + mi) * 57344, pair_swap(o0[0], o0[1], o1[0], o1[1]));
      }
    };
#define SL(i) (((i) + 4) % 9)
    wait_vm_lgkm<8>(); bar(); ld(SL(0), a[0]); G4_R1A(0)
    wait_vm_lgkm<8>(); bar(); ld(SL(1), a[1]); G4_R1A(1) mm(0, 0, a[0]); SCHED();
    wait_vm_lgkm<8>(); bar(); G3_A(9) ld(SL(2), a[0]); G4_R1A(2) mm(0, 1, a[1]); SCHED();
    wait_vm_lgkm<9>(); bar(); G3_A(10) ld(SL(3), a[1]); G4_R1A(3) mm(0, 2, a[0]); SCHED();
    wait_vm_lgkm<10>(); bar(); G3_A(11) ld(SL(4), a[0]); G4_R1A(4) mm(0, 3, a[1]); SCHED();
    wait_vm_lgkm<11>(); bar(); G3_A(12) ld(SL(5), a[1]); G4_R1A(5) mm(0, 4, a[0]); SCHED();
    wait_vm_lgkm<12>(); bar(); G3_A(13) ld(SL(6), a[0]); G4_R1A(6) mm(0, 5, a[1]); SCHED();
    wait_vm_lgkm<13>(); bar(); G3_A(14) ld(SL(7), a[1]); G4_R1A(7) mm(0, 6, a[0]); SCHED();
    wait_vm_lgkm<14>(); bar(); G3_A(15) ld(SL(8), a[0]); ldrt(0); mm(0, 7, a[1]); SCHED();
    mfence();
#pragma unroll
    for (int i = 0; i < 4; i++) pin(acc[0][i >> 1][i & 1]);
#pragma unroll
    for (int i = 0; i < 4; i++) ep(0, i);
    SCHED();
    wait_vm_lgkm<14>(); bar(); ld(SL(9), a[1]); G4_R1B(2) G4_R1B(0) mm(1, 0, a[0]); SCHED();
    wait_vm_lgkm<14>(); bar(); ld(SL(10), a[0]); G4_R1B(3) G4_R1B(1) mm(1, 1, a[1]); SCHED();
    wait_vm_lgkm<14>(); bar(); ld(SL(11), a[1]); G4_R1B(4) mm(1, 2, a[0]); SCHED();
    wait_vm_lgkm<5>(); bar();
    if (has3 && tid < 8) st8_dev(wsr, FL_5 + tid * 2048 + (pm3 * 14 + pn3) * 8, tag);
    ld(SL(12), a[0]); G4_R1B(5) mm(1, 3, a[1]); SCHED();
    lds_bar(); ld(SL(13), a[1]); G4_R1B(6) mm(1, 4, a[0]); SCHED();
    lds_bar(); ld(SL(14), a[0]); G4_R1B(7) mm(1, 5, a[1]); SCHED();
    lds_bar(); ld(SL(15), a[1]); ldrt(1); mm(1, 6, a[0]); SCHED();
    wait_vm_lgkm<0>(); bar();
    mm(1, 7, a[1]);
    mfence();
#pragma unroll
    for (int i = 0; i < 4; i++) pin(acc[1][i >> 1][i & 1]);
#pragma unroll
    for (int i = 0; i < 4; i++) { ldB4(i); ep(1, i); SCHED(); }
    if (w == 0) polln(wsr, FL_5 + pn * 2048, (pm >> 1) * 14, (pm >> 1) * 14 + 14, tag, lane);
    lds_bar();
    if (wm4 == 0) { G4_A4(0) G4_A4(1) wait_vm<8>(); } else wait_vm<0>();
    bar();
    if (has3 && tid < 8) st8_dev(wsr, FL_6 + tid * 2048 + (pm3 * 14 + pn3) * 8, tag);
  }
  {
    const int wm = wm4;
    if (wm) {
      const u32 vb2 = kn_voff(D_, w - 2, lane), dw2 = dw - 2048;
#define G4_R2X(i) G4_R2(i) glds<G4_R2POS(i), (((i) / 3) * 7 + 4 + ((i) % 3)) * CBD>(vb2, sb4, dw2);
      G4_R2X(0) G4_R2X(3) G4_R2X(6) G4_R2X(9) G4_R2X(1) G4_R2X(4) G4_R2X(7) G4_R2X(10) G4_R2X(2) G4_R2X(5) G4_R2X(8) G4_R2X(11)
      polln(wsr, FL_6 + (pn ^ kq) * 2048, (pm >> 1) * 14, (pm >> 1) * 14 + 14, tag, lane);
      G4_A4(0) G4_A4(1)
      wait_vm<8>(); bar();
    }
    const char* pa0 = smem + ring + mk_base(lane, 0);
    const char* pa1 = smem + ring + mk_base(lane, 1);
    f32x4 acc[4][2];
    u32x4 a[4];
    auto ld = [&](int b) {
      a[0] = *(const u32x4*)(pa0 + (b & 1) * 4096); a[1] = *(const u32x4*)(pa0 + (b & 1) * 4096 + 2048);
      a[2] = *(const u32x4*)(pa1 + (b & 1) * 4096); a[3] = *(const u32x4*)(pa1 + (b & 1) * 4096 + 2048);
      asm volatile("s_waitcnt lgkmcnt(0)" ::: "memory");
      SCHED();
    };
    auto mm = [&](int s0, bool first = false) {
#pragma unroll
      for (int ks = 0; ks < 2; ks++)
#pragma unroll
        for (int nt = 0; nt < 4; nt++)
#pragma unroll
          for (int mi = 0; mi < 2; mi++) mmaz(first && ks == 0, acc[nt][mi], B4[s0 + ks][nt], a[2 * ks + mi]);
      SCHED();
    };
    auto ldb = [&](int j, int s0) {
      const int i = kq * 3 + j;
      const char* bl = smem + (i < 4 ? 32768 + i * 8192 : GA_B + (i - 4) * 8192);
#pragma unroll
      for (int ks = 0; ks < 2; ks++)
#pragma unroll
        for (int nt = 0; nt < 4; nt++) B4[s0 + ks][nt] = kn_frag(bl + ks * 4096 + (kb ^ (nt << 5)));
      SCHED();
    };
    wait_vm<4>(); ld(0); G4_A4(2) mm(0, true);
    wait_vm<4>(); ld(1); G4_A4(3) mm(2);
    wait_vm<4>(); ld(2); G4_A4(4) mm(4);
    wait_vm<4>(); ld(3); G4_A4(5) mm(6);
    if (wm == 0) bar();
    ldb(0, 0); ldb(1, 2);
    wait_vm<4>(); ld(4); G4_A4(6) ldb(2, 4); mm(0);
    wait_vm<4>(); ld(5); mm(2);
    wait_vm<0>(); ld(6); mm(4);
    mfence();
#pragma unroll
    for (int i = 0; i < 8; i++) pin(acc[i >> 1][i & 1]);
    {
      char* d = smem + ring + lane * 16;
#pragma unroll
      for (int k2 = 0; k2 < 4; k2++) {
        *(f32x4*)(d + k2 * 2048) = acc[2 * (k2 >> 1)][k2 & 1];
        *(f32x4*)(d + k2 * 2048 + 1024) = acc[2 * (k2 >> 1) + 1][k2 & 1];
      }
    }
    lds_bar();
    const char* s = smem + (wm ? 0 : 65536) + kq * 2048 + lane * 16;
    f32x4 f0 = (*(const f32x4*)(s) + *(const f32x4*)(s + 8192)) + (*(const f32x4*)(s + 16384) + *(const f32x4*)(s + 24576));
    f32x4 f1 = (*(const f32x4*)(s + 1024) + *(const f32x4*)(s + 9216)) + (*(const f32x4*)(s + 17408) + *(const f32x4*)(s + 25600));
    u32x4 v = pair_swap(pkbf(f0[0], f0[1]), pkbf(f0[2], f0[3]), pkbf(f1[0], f1[1]), pkbf(f1[2], f1[3]));
    u32x4 o;
#pragma unroll
    for (int i = 0; i < 4; i++) o[i] = pkbf2(bfup(hres[i]) + bfup(pkbf2(bfup(xres[i]) + bfup(v[i]))));
    __builtin_amdgcn_raw_buffer_store_b128(o, __builtin_amdgcn_make_buffer_rsrc(p.out, 0, L_ * D_ * 2, 0x00020000),
                                           (u32)(((rt * 16 + r16) * D_ + col0 + 32 * wn + pc4 * 8) * 2), 0, 16);
  }
  asm volatile(".Lk_end:");
}

extern "C" void kernel_launch(void* const* d_in, const int* in_sizes, int n_in, void* d_out, int out_size, void* d_ws,
                              size_t ws_size, hipStream_t stream) {
  Params p;
  p.x = (const u16*)d_in[0]; p.are = (const u16*)d_in[1]; p.aim = (const u16*)d_in[2]; p.bre = (const u16*)d_in[3];
  p.bim = (const u16*)d_in[4]; p.cre = (const u16*)d_in[5]; p.cim = (const u16*)d_in[6]; p.logdt = (const u16*)d_in[7];
  p.dskip = (const u16*)d_in[8]; p.w1 = (const u16*)d_in[10]; p.w2 = (const u16*)d_in[11]; p.w3 = (const u16*)d_in[12];
  p.w4 = (const u16*)d_in[13]; p.out = (u16*)d_out; p.ws = (char*)d_ws;
  k_fused<<<256, 512, 0, stream>>>(p);
}
```

```cpp
#include <hip/hip_runtime.h>
#include <stdint.h>

typedef unsigned short u16;
typedef unsigned int u32;
typedef unsigned long long u64;
typedef __bf16 bf16x2 __attribute__((ext_vector_type(2)));
typedef __bf16 bf16x8 __attribute__((ext_vector_type(8)));
typedef _Float16 f16x2 __attribute__((ext_vector_type(2)));
typedef _Float16 f16x8 __attribute__((ext_vector_type(8)));
typedef float f32x2 __attribute__((ext_vector_type(2)));
typedef float f32x4 __attribute__((ext_vector_type(4)));
typedef short s16x4 __attribute__((ext_vector_type(4)));
typedef u32 u32x2 __attribute__((ext_vector_type(2)));
typedef u32 u32x4 __attribute__((ext_vector_type(4)));
#define LDSP __attribute__((address_space(3)))
#define DEV __device__ __forceinline__

constexpr int L_ = 2048, D_ = 512, NS_ = 128, F_ = 1792;
constexpr u32 WS_U8 = 0;
constexpr u32 WS_HT = 7u << 20;
constexpr u32 WS_H1 = 13u << 20;
constexpr u32 WS_X1 = 16u << 20;
constexpr u32 WS_ST = 18u << 20;
constexpr u32 WS_HID = 19u << 20;
constexpr u32 WS_FL = 28u << 20;
constexpr u32 FL_EPOCH = WS_FL, FL_1 = WS_FL + 4096, FL_2 = FL_1 + 32768, FL_3 = FL_2 + 32768, FL_4 = FL_3 + 16384, FL_5 = FL_4 + 16384, FL_6 = FL_5 + 16384;

struct Params {
  const u16 *x, *are, *aim, *bre, *bim, *cre, *cim, *logdt, *dskip, *w1, *w2, *w3, *w4;
  u16* out;
  char* ws;
};

DEV float bf2f(u32 v) { return __uint_as_float(v << 16); }
DEV float bflo(u32 v) { return __uint_as_float(v << 16); }
DEV float bfhi(u32 v) { return __uint_as_float(v & 0xffff0000u); }
DEV f32x2 bfup(u32 v) { return f32x2{bflo(v), bfhi(v)}; }
DEV u32 pkbf2(f32x2 v) { return __builtin_bit_cast(u32, __builtin_convertvector(v, bf16x2)); }
DEV u32 pkbf(float a, float b) { return pkbf2(f32x2{a, b}); }
DEV u32 pkh(float a, float b) { f32x2 v = {a, b}; return __builtin_bit_cast(u32, __builtin_convertvector(v, f16x2)); }
DEV float rbf(float a) { return bflo(pkbf(a, a)); }
DEV f32x2 hup(u32 v) { return __builtin_convertvector(__builtin_bit_cast(f16x2, v), f32x2); }

template <int CTRL> DEV float dppf(float v) {
  return __builtin_bit_cast(float, __builtin_amdgcn_update_dpp(0, __builtin_bit_cast(int, v), CTRL, 0xf, 0xf, true));
}
DEV float gsum(float v) {
  u32 a = __float_as_uint(v);
  auto r = __builtin_amdgcn_permlane16_swap(a, a, false, false);
  v = __uint_as_float(r[0]) + __uint_as_float(r[1]);
  a = __float_as_uint(v);
  auto q = __builtin_amdgcn_permlane32_swap(a, a, false, false);
  return __uint_as_float(q[0]) + __uint_as_float(q[1]);
}
DEV float gmax(float v) {
  u32 a = __float_as_uint(v);
  auto r = __builtin_amdgcn_permlane16_swap(a, a, false, false);
  v = fmaxf(__uint_as_float(r[0]), __uint_as_float(r[1]));
  a = __float_as_uint(v);
  auto q = __builtin_amdgcn_permlane32_swap(a, a, false, false);
  return fmaxf(__uint_as_float(q[0]), __uint_as_float(q[1]));
}
DEV float wave_sum(float v) {
  v += dppf<0xB1>(v); v += dppf<0x4E>(v); v += dppf<0x141>(v); v += dppf<0x140>(v);
  return gsum(v);
}
DEV float wave_max(float v) {
  v = fmaxf(v, dppf<0xB1>(v)); v = fmaxf(v, dppf<0x4E>(v)); v = fmaxf(v, dppf<0x141>(v)); v = fmaxf(v, dppf<0x140>(v));
  return gmax(v);
}
DEV u32x2 tr_read(const char* p) {
  return __builtin_bit_cast(u32x2, __builtin_amdgcn_ds_read_tr16_b64_v4i16((LDSP s16x4*)p));
}
DEV void mma(f32x4& c, u32x4 a, u32x4 b) { asm volatile("v_mfma_f32_16x16x32_bf16 %0, %1, %2, %0" : "+v"(c) : "v"(a), "v"(b)); }
DEV void mma0(f32x4& c, u32x4 a, u32x4 b) { asm volatile("v_mfma_f32_16x16x32_bf16 %0, %1, %2, 0" : "=&v"(c) : "v"(a), "v"(b)); }
DEV void mmah(f32x4& c, u32x4 a, u32x4 b) { asm volatile("v_mfma_f32_16x16x32_f16 %0, %1, %2, %0" : "+v"(c) : "v"(a), "v"(b)); }
DEV void mmah0(f32x4& c, u32x4 a, u32x4 b) { asm volatile("v_mfma_f32_16x16x32_f16 %0, %1, %2, 0" : "=&v"(c) : "v"(a), "v"(b)); }
DEV void mmaz(bool first, f32x4& c, u32x4 a, u32x4 b) { if (first) mma0(c, a, b); else mma(c, a, b); }
DEV void mfence() { asm volatile("s_nop 7" ::: "memory"); __builtin_amdgcn_sched_barrier(0); }
DEV float smul(float a, float b) { float r; asm("v_mul_f32 %0, %1, %2" : "=v"(r) : "v"(a), "v"(b)); return r; }
DEV float sfma(float a, float b, float c) { float r; asm("v_fma_f32 %0, %1, %2, %3" : "=v"(r) : "v"(a), "v"(b), "v"(c)); return r; }
DEV float sfmak(float a, float k1, float k0) { float r; asm("v_fma_f32 %0, %1, %2, %3" : "=v"(r) : "v"(a), "s"(k1), "v"(k0)); return r; }
DEV float sadd1(float a) { float r; asm("v_add_f32 %0, 1.0, %1" : "=v"(r) : "v"(a)); return r; }
DEV void pin(f32x4& c) { asm volatile("" : "+v"(c)); }
DEV float dot2(u32 a, u32 b, float c) {
  return __builtin_amdgcn_fdot2_f32_bf16(__builtin_bit_cast(bf16x2, a), __builtin_bit_cast(bf16x2, b), c, false);
}
template <int LOFF, int MOFF> DEV void glds(u32 voff, const void* sbase, u32 lds) {
  asm volatile("s_add_u32 m0, %1, %3\n\ts_nop 0\n\tglobal_load_lds_dwordx4 %0, %2"
               :: "v"(voff + (u32)MOFF), "s"(lds), "s"(sbase), "n"(LOFF) : "memory", "m0", "scc");
}
template <int LOFF, int MOFF> DEV void gldsd(u32 voff, const void* sbase, u32 lds) {
  asm volatile("s_add_u32 m0, %1, %3\n\ts_nop 0\n\tglobal_load_lds_dwordx4 %0, %2 sc1"
               :: "v"(voff + (u32)MOFF), "s"(lds), "s"(sbase), "n"(LOFF) : "memory", "m0", "scc");
}
DEV void touch(u32& t, const void* sbase, u32 voff) {
  asm volatile("global_load_dword %0, %1, %2" : "+v"(t) : "v"(voff), "s"(sbase) : "memory");
}
DEV void keep(u32 t) { asm volatile("" :: "v"(t)); }
DEV u32 lds_addr(const char* p) { return (u32)(size_t)(LDSP const char*)p; }
#define SCHED() __builtin_amdgcn_sched_barrier(0)
template <int N> DEV void wait_vm() { asm volatile("s_waitcnt vmcnt(%0)" ::"n"(N) : "memory"); }
template <int N> DEV void wait_vm_lgkm() { asm volatile("s_waitcnt vmcnt(%0) lgkmcnt(0)" ::"n"(N) : "memory"); }
DEV void bar() { __builtin_amdgcn_s_barrier(); }
DEV void lds_bar() { asm volatile("s_waitcnt lgkmcnt(0)" ::: "memory"); __builtin_amdgcn_s_barrier(); }

#define STAMP(i)

typedef __amdgpu_buffer_rsrc_t rsrc_t;
DEV void st16_dev(rsrc_t r, u32 off, u32x4 v) { __builtin_amdgcn_raw_buffer_store_b128(v, r, off, 0, 16); }
DEV void st8_dev(rsrc_t r, u32 off, u32x2 v) { __builtin_amdgcn_raw_buffer_store_b64(v, r, off, 0, 16); }
DEV u32x4 ld16_dev(rsrc_t r, u32 off) { return __builtin_amdgcn_raw_buffer_load_b128(r, off, 0, 16); }
DEV u32x2 ld8_dev(rsrc_t r, u32 off) { return __builtin_amdgcn_raw_buffer_load_b64(r, off, 0, 16); }

constexpr int SPIN_MAX = 1 << 18;
DEV void poll256(rsrc_t r, u32 base, u32x2 tag, int lane) {
#pragma unroll 1
  for (int s = 0; s < SPIN_MAX; s++) {
    u32x4 a = ld16_dev(r, base + lane * 32), b = ld16_dev(r, base + lane * 32 + 16);
    bool ok = a[0] == tag[0] && a[1] == tag[1] && a[2] == tag[0] && a[3] == tag[1] &&
              b[0] == tag[0] && b[1] == tag[1] && b[2] == tag[0] && b[3] == tag[1];
    if (__all(ok)) break;
    __builtin_amdgcn_s_sleep(2);
    asm volatile("" ::: "memory");
  }
}
DEV void polln(rsrc_t r, u32 base, int lo, int hi, u32x2 tag, int lane) {
  const int i = min(lo + lane, hi - 1);
#pragma unroll 1
  for (int s = 0; s < SPIN_MAX; s++) {
    u32x2 a = ld8_dev(r, base + i * 8);
    if (__all(a[0] == tag[0] && a[1] == tag[1])) break;
    __builtin_amdgcn_s_sleep(2);
    asm volatile("" ::: "memory");
  }
}

DEV u32x2 act4(f32x4 a, f32x4 cs, f32x4 rt) {
  const float c0 = -2.0f * 0.7978845608028654f * 1.4426950408889634f;
  const float c1 = c0 * 0.044715f;
  const f32x2 rs = {rt[0], rt[1]}, t = {rt[2], rt[3]};
  f32x2 v0 = f32x2{a[0], a[1]} * rs + f32x2{cs[0], cs[1]} * t, v1 = f32x2{a[2], a[3]} * rs + f32x2{cs[2], cs[3]} * t;
  f32x4 v = {v0[0], v0[1], v1[0], v1[1]};
  f32x4 x = v;
  f32x4 z = x * (x * x * c1 + c0);
  f32x4 e = {__builtin_amdgcn_exp2f(z[0]), __builtin_amdgcn_exp2f(z[1]), __builtin_amdgcn_exp2f(z[2]), __builtin_amdgcn_exp2f(z[3])};
  e += 1.0f;
  f32x4 r = {__builtin_amdgcn_rcpf(e[0]), __builtin_amdgcn_rcpf(e[1]), __builtin_amdgcn_rcpf(e[2]), __builtin_amdgcn_rcpf(e[3])};
  x *= r;
  return u32x2{pkbf(x[0], x[1]), pkbf(x[2], x[3])};
}

constexpr int LDS_ALL = 163840;
constexpr int KG_ROW = 528;
constexpr int KG_CH = 73 * KG_ROW;
constexpr int P0_KT = 77312;
constexpr int P0_LN = 101888;
constexpr int P0_MS = 119296;
constexpr int CV_COPY = 4160, CV_KP = 4 * CV_COPY, CV_XS = CV_KP + 4608, CV_CH = CV_XS + 4096;
constexpr int CV_UI = 2 * CV_CH;
constexpr int CV_RED = CV_UI;
constexpr int CV_STG = 0;
constexpr int GA_A = 0, GA_TAB = 65536, GA_B = 98304;
constexpr int G3_ST = 73728, G3_TAB = 90112;
constexpr int G4_B = 65536;

struct KTbl {
  u16 v[2048];
  constexpr KTbl() : v() {
    for (int pos = 0; pos < 2048; pos++) {
      int l = (pos & ~7) + 7 - (pos & 7);
      int sh = l < 256 ? 0 : l < 512 ? 1 : l < 1024 ? 2 : 3;
      int lv = sh ? (((l + (1 << (sh - 1)) - 1 + ((l >> sh) & 1)) >> sh) << sh) : l;
      int s = lv < 256 ? 0 : lv < 512 ? 1 : lv < 1024 ? 2 : 3;
      int rr = 8 * s + ((lv >> 7) << (3 - s)) + ((lv & 7) >> s);
      v[pos] = (u16)((rr * 16 + ((lv >> 3) & 15)) * 16);
    }
  }
};
__device__ __attribute__((aligned(16))) constexpr KTbl KTBL{};

typedef f32x2 cf;
DEV cf cmul(cf a, cf b) {
  cf r;
  asm("v_pk_mul_f32 %0, %1, %2 op_sel:[1,1] op_sel_hi:[1,0]\n\t"
      "v_pk_fma_f32 %0, %1, %2, %0 op_sel:[0,0,0] op_sel_hi:[0,1,1] neg_lo:[0,0,1]"
      : "=&v"(r) : "v"(a), "v"(b));
  return r;
}
DEV cf cscale(cf a, float s) { return a * s; }
DEV cf cexpk(float tl2, float rh, float rl, float s) {
  float mag = __builtin_amdgcn_exp2f(tl2 * s);
  float p = rh * s;
  float f = __builtin_fmaf(rl, s, p - __builtin_rintf(p));
  return cf{__builtin_amdgcn_cosf(f), __builtin_amdgcn_sinf(f)} * mag;
}
DEV void st_x(char* p, cf a) { *(u32*)p = pkh(a[0], a[1]); }
DEV void st_y(char* p, cf a) {
  u32 hi = pkh(a[0], a[1]);
  f32x2 r = a - hup(hi);
  *(u32*)p = hi;
  *(u32*)(p + 16 * KG_ROW) = pkh(r[0], r[1]);
}
DEV float pow2_scale(float mx, u32 target, float& inv) {
  u32 eb = __float_as_uint(mx) >> 23;
  u32 se = min(max(254u + target - eb, 1u), 253u);
  inv = __uint_as_float((254u - se) << 23);
  return __uint_as_float(se << 23);
}

DEV void conv_load1(const char* pa, const char* pb, int b0, int i, u32x4 (&A)[8], u32x4 (&B)[8]) {
  u32x2 lo = *(const volatile LDSP u32x2*)(pa + 64 * b0 + 512 * i), hi = *(const volatile LDSP u32x2*)(pa + 64 * b0 + 512 * i + 8);
  A[i] = u32x4{lo[0], lo[1], hi[0], hi[1]};
  B[i] = *(const u32x4*)(pb - 64 * b0 + 512 * i);
}
DEV void conv_step(const char* pa, const char* pb, int nb, const u32x4 (&A)[8], const u32x4 (&B)[8], u32x4 (&An)[8], u32x4 (&Bn)[8], f32x4 (&acc)[8]) {
  u32x2 lo[8], hi[8];
  int c = 0;
#pragma unroll
  for (int i = 0; i < 8; i++) {
#pragma unroll
    for (int t = i; t < 8; t++) {
      if (nb == 1 && i == 0) mmah0(acc[t], A[i], B[t - i]); else mmah(acc[t], A[i], B[t - i]);
      if (nb < 4 && c < 24) {
        const int f = c / 3, k = c % 3;
        if (k == 0) Bn[f] = *(const u32x4*)(pb - 64 * nb + 512 * f);
        else if (k == 1) lo[f] = *(const volatile LDSP u32x2*)(pa + 64 * nb + 512 * f);
        else hi[f] = *(const volatile LDSP u32x2*)(pa + 64 * nb + 512 * f + 8);
      }
      c++;
      SCHED();
    }
  }
  if (nb < 4) {
#pragma unroll
    for (int f = 0; f < 8; f++) An[f] = u32x4{lo[f][0], lo[f][1], hi[f][0], hi[f][1]};
  }
}

DEV u32 kn_voff(int ld, int w, int lane) {
  int kr = lane >> 3, pos = lane & 7;
  int f2 = ((lane >> 4) & 1) | ((w & 1) << 1);
  return (u32)((8 * w + kr) * ld * 2 + ((pos ^ (f2 << 1)) << 4));
}
DEV u32 mk_voff(int ld, int lane) { int r = lane >> 3, pos = lane & 7; return (u32)(r * ld * 2 + ((pos ^ r) << 4)); }
DEV int kn_base(int lane) {
  int g = lane >> 4, q = (lane & 15) >> 2, pp = lane & 3;
  int f2 = ((q >> 1) & 1) | ((g & 1) << 1);
  return (8 * g + q) * 128 + (f2 << 5) + pp * 8;
}
DEV u32x4 kn_frag(const char* p) { u32x2 a = tr_read(p), b = tr_read(p + 512); return u32x4{a[0], a[1], b[0], b[1]}; }
DEV int mk_base(int lane, int ks) { int r16 = lane & 15, g = lane >> 4; return r16 * 128 + (((4 * ks + g) ^ (r16 & 7)) << 4); }
constexpr u32 ONES = 0x3f803f80u;
DEV u32x4 pair_swap(u32 p01, u32 p23, u32 q01, u32 q23) {
  auto r0 = __builtin_amdgcn_permlane16_swap(p01, q01, false, false);
  auto r1 = __builtin_amdgcn_permlane16_swap(p23, q23, false, false);
  return u32x4{r0[0], r1[0], r0[1], r1[1]};
}
template <int LDW> DEV void w64_issue(const u16* wsrc, u32 dst, int w, int lane) {
  const u32 vo = kn_voff(LDW, w, lane);
  const u32 d = dst + w * 1024;
  glds<0 * 8192, 0 * 128 * LDW>(vo, wsrc, d); glds<1 * 8192, 1 * 128 * LDW>(vo, wsrc, d);
  glds<2 * 8192, 2 * 128 * LDW>(vo, wsrc, d); glds<3 * 8192, 3 * 128 * LDW>(vo, wsrc, d);
  glds<4 * 8192, 4 * 128 * LDW>(vo, wsrc, d); glds<5 * 8192, 5 * 128 * LDW>(vo, wsrc, d);
  glds<6 * 8192, 6 * 128 * LDW>(vo, wsrc, d); glds<7 * 8192, 7 * 128 * LDW>(vo, wsrc, d);
}
DEV void w64_regs(const char* img, int wn, int lane, u32x4 (&B)[16][2]) {
  const char* p0 = img + (kn_base(lane) ^ ((2 * wn) << 5));
  const char* p1 = img + (kn_base(lane) ^ ((2 * wn + 1) << 5));
#pragma unroll
  for (int s = 0; s < 16; s++) { B[s][0] = kn_frag(p0 + s * 4096); B[s][1] = kn_frag(p1 + s * 4096); }
}
template <int LDW> DEV void w64_issue4(const u16* wsrc, u32 dst, int w, int lane) {
  w64_issue<LDW>(wsrc, dst, w - 4, lane); w64_issue<LDW>(wsrc, dst, w, lane);
}
template <int H> DEV void w3_issue(const u16* sb, u32 lds0, int w, int lane) {
  constexpr int CB3 = 128 * F_;
  const u32 vb = kn_voff(F_, w, lane), dw = lds0 + GA_B + w * 1024;
  glds<0, (4 * H) * CB3>(vb, sb, dw); glds<8192, (4 * H) * CB3 + 128>(vb, sb, dw);
  glds<16384, (4 * H + 1) * CB3>(vb, sb, dw); glds<24576, (4 * H + 1) * CB3 + 128>(vb, sb, dw);
  glds<32768, (4 * H + 2) * CB3>(vb, sb, dw); glds<40960, (4 * H + 2) * CB3 + 128>(vb, sb, dw);
  glds<49152, (4 * H + 3) * CB3>(vb, sb, dw); glds<57344, (4 * H + 3) * CB3 + 128>(vb, sb, dw);
}

__global__ __launch_bounds__(512, 2) void k_fused(Params p) {
  __shared__ __attribute__((aligned(16))) char smem[LDS_ALL];
  const int bid = blockIdx.x;
  const int tid = threadIdx.x, w = __builtin_amdgcn_readfirstlane(tid >> 6), lane = tid & 63;
  const int r16 = lane & 15, g = lane >> 4;
  const u32 lds0 = lds_addr(smem);
  const rsrc_t wsr = __builtin_amdgcn_make_buffer_rsrc(p.ws, 0, 0x4000000, 0x00020000);
  STAMP(0);
  const int cp = (bid & 7) * 32 + (bid >> 3);
  const int pm = (bid & 7) * 4 + (bid >> 6), pn = (bid >> 3) & 7;
  const int row0 = pm * 64, col0 = pn * 64;
  u32x2 tag;
  u32 TKW = 0, DSK;
  {
    u32x2 ep = ld8_dev(wsr, FL_EPOCH);
    u32x4 xv = *(const u32x4*)(p.x + (bid * 8 + w) * D_ + lane * 8);
    const int chl = w >> 2, role = (w >> 1) & 1, n = tid & 127;
    const int ch = cp * 2 + chl;
    const int pi = ch * NS_ + n;
    u32 r_are = p.are[pi], r_aim = p.aim[pi], r_bre = p.bre[pi], r_bim = p.bim[pi], r_cre = p.cre[pi], r_cim = p.cim[pi];
    u32 r_ldt = p.logdt[ch];
    DSK = p.dskip[cp * 2 + ((w >> 1) & 1)];
    u32x4 kidx = *(const u32x4*)(KTBL.v + (tid & 255) * 8);
    SCHED();
    u32 sk0, sk1, sk2;
    {
      u64 pc; u32 len;
      asm volatile("s_getpc_b64 %0\n.Lk_here:\n\ts_mov_b32 %1, .Lk_end-.Lk_here" : "=s"(pc), "=s"(len));
      const u32 o = (u32)((bid >> 3) * 24 + w * 3) * 64;
      asm volatile("s_load_dword %0, %3, %4\n\ts_load_dword %1, %3, %5\n\ts_load_dword %2, %3, %6"
                   : "=&s"(sk0), "=&s"(sk1), "=&s"(sk2) : "s"(pc), "s"(min(o, len)), "s"(min(o + 64, len)), "s"(min(o + 128, len)) : "memory");
    }
    {
      u64 e = ((u64)ep[1] << 32 | ep[0]) + 1;
      tag = u32x2{(u32)__builtin_amdgcn_readfirstlane((u32)e), (u32)__builtin_amdgcn_readfirstlane((u32)(e >> 32))};
    }
    char* su = smem + P0_LN;
    float* ms = (float*)(smem + P0_MS);
    {
      f32x2 v[4];
#pragma unroll
      for (int i = 0; i < 4; i++) v[i] = bfup(xv[i]);
      f32x2 s2 = (v[0] + v[1]) + (v[2] + v[3]);
      float mean = wave_sum(s2[0] + s2[1]) * (1.0f / D_);
      f32x2 q2 = {0, 0};
#pragma unroll
      for (int i = 0; i < 4; i++) { f32x2 d = v[i] - mean; q2 += d * d; }
      float var = wave_sum(q2[0] + q2[1]) * (1.0f / D_);
      float m = rbf(mean);
      float inv = __builtin_amdgcn_rcpf(rbf(__builtin_amdgcn_sqrtf(rbf(rbf(var) + 1e-5f))));
      u32x4 uo;
#pragma unroll
      for (int i = 0; i < 4; i++) {
        f32x2 u = bfup(pkbf2(bfup(pkbf2(v[i] - m)) * inv));
        uo[i] = pkh(u[0], u[1]);
      }
      u32x4 lo4, hi4;
#pragma unroll
      for (int i = 0; i < 2; i++) {
        lo4[2 * i] = __builtin_amdgcn_perm(xv[i], uo[i], 0x05040100u); lo4[2 * i + 1] = __builtin_amdgcn_perm(xv[i], uo[i], 0x07060302u);
        hi4[2 * i] = __builtin_amdgcn_perm(xv[2 + i], uo[2 + i], 0x05040100u); hi4[2 * i + 1] = __builtin_amdgcn_perm(xv[2 + i], uo[2 + i], 0x07060302u);
      }
      *(u32x4*)(su + w * 2112 + lane * 32) = lo4;
      *(u32x4*)(su + w * 2112 + lane * 32 + 16) = hi4;
    }
    asm volatile("" : "+v"(kidx), "+v"(DSK));
    STAMP(1);
    lds_bar();
    asm volatile("" :: "s"(sk0), "s"(sk1), "s"(sk2));
    {
      const int q4 = (lane & 15) >> 2, pp = lane & 3;
      const char* a = su + q4 * 2112 + (128 * w + 16 * g + 4 * pp) * 2;
      u32x2 u0 = tr_read(a), u1 = tr_read(a + 4 * 2112), x0 = tr_read(a + 128), x1 = tr_read(a + 4 * 2112 + 128);
      const u32 dst = WS_U8 + bid * 16384 + w * 2048 + lane * 16;
      st16_dev(wsr, dst, u32x4{u0[0], u0[1], u1[0], u1[1]});
      st16_dev(wsr, dst + 1024, u32x4{x0[0], x0[1], x1[0], x1[1]});
    }
    float dt = rbf(expf(bf2f(r_ldt)));
    float ar = dt * bf2f(r_are), ai = dt * bf2f(r_aim);
    cf CB = cmul(cf{bf2f(r_cre), bf2f(r_cim)}, cf{dt * bf2f(r_bre), dt * bf2f(r_bim)});
    float tl2 = ar * 1.4426950408889634f;
    const float I2PH = 0.15915494309189535f;
    const float I2PL = (float)(0.15915494309189533576888 - (double)0.15915494309189535f);
    float rh = ai * I2PH;
    float rl = __builtin_fmaf(ai, I2PH, -rh) + ai * I2PL;
    {
      float bs = wave_sum(fabsf(CB[0]) + fabsf(CB[1])), isx;
      float sx = pow2_scale(bs, 14, isx);
      CB = cscale(CB, sx);
      if (role == 0 && lane == 0) { ms[chl * 2 + (w & 1)] = bs; ms[4 + chl * 2 + (w & 1)] = isx; }
    }
    {
      const u16* wb = w == 0 ? p.w3 : w == 1 ? p.w4 : w == 2 ? p.w1 : p.w2;
      touch(TKW, wb, w < 2 ? (u32)(bid * 56 + min(lane, 55)) * 128 : (u32)(bid * 16 + (lane & 15)) * 128);
    }
    STAMP(2);
    char* img = smem + chl * KG_CH;
    {
      char* wp = img + n * 4;
      if (role == 0) {
        cf e3 = cexpk(tl2, -rh, -rl, 8.f), e4 = cexpk(tl2, -rh, -rl, 16.f), e5 = cexpk(tl2, -rh, -rl, 32.f), e6 = cexpk(tl2, -rh, -rl, 64.f);
        cf y[8];
        y[0] = cf{16384.f, 0.f}; y[1] = cscale(e3, 16384.f); y[2] = cscale(e4, 16384.f); y[3] = cmul(y[2], e3);
        y[4] = cscale(e5, 16384.f); y[5] = cmul(y[4], e3); y[6] = cmul(y[4], e4); y[7] = cmul(y[6], e3);
#pragma unroll
        for (int j = 0; j < 8; j++) st_y(wp + (41 + j) * KG_ROW, y[j]);
#pragma unroll
        for (int j = 0; j < 8; j++) st_y(wp + (49 + j) * KG_ROW, j ? cmul(y[j], e6) : cscale(e6, 16384.f));
        cf e7 = cexpk(tl2, rh, rl, 128.f), e8 = cexpk(tl2, rh, rl, 256.f), e9 = cexpk(tl2, rh, rl, 512.f);
        cf e10 = cexpk(tl2, rh, rl, 1024.f), e11 = cexpk(tl2, rh, rl, 2048.f);
        cf g8 = cmul(CB, e10), g12 = cmul(g8, e9), g16 = cmul(CB, e11);
        cf g10 = cmul(g8, e8), g14 = cmul(g12, e8);
        st_x(wp + 32 * KG_ROW, g8); st_x(wp + 33 * KG_ROW, cmul(g8, e7));
        st_x(wp + 34 * KG_ROW, g10); st_x(wp + 35 * KG_ROW, cmul(g10, e7));
        st_x(wp + 36 * KG_ROW, g12); st_x(wp + 37 * KG_ROW, cmul(g12, e7));
        st_x(wp + 38 * KG_ROW, g14); st_x(wp + 39 * KG_ROW, cmul(g14, e7));
        st_x(wp + 40 * KG_ROW, g16);
      } else {
        cf e0 = cexpk(tl2, rh, rl, 1.f), e1 = cexpk(tl2, rh, rl, 2.f), e2 = cexpk(tl2, rh, rl, 4.f);
        cf e7 = cexpk(tl2, rh, rl, 128.f), e8 = cexpk(tl2, rh, rl, 256.f), e9 = cexpk(tl2, rh, rl, 512.f);
        cf eb[8];
        eb[1] = e0; eb[2] = e1; eb[3] = cmul(e1, e0); eb[4] = e2; eb[5] = cmul(e2, e0); eb[6] = cmul(e2, e1); eb[7] = cmul(eb[6], e0);
        cf gq[8];
        gq[0] = CB; gq[1] = cmul(CB, e7); gq[2] = cmul(CB, e8); gq[3] = cmul(gq[2], e7);
        gq[4] = cmul(CB, e9); gq[5] = cmul(gq[4], e7); gq[6] = cmul(gq[4], e8); gq[7] = cmul(gq[6], e7);
#pragma unroll
        for (int q = 0; q < 2; q++)
#pragma unroll
          for (int b = 0; b < 8; b++) st_x(wp + (q * 8 + b) * KG_ROW, b ? cmul(gq[q], eb[b]) : gq[q]);
#pragma unroll
        for (int q = 2; q < 4; q++)
#pragma unroll
          for (int b = 0; b < 4; b++) st_x(wp + (16 + (q - 2) * 4 + b) * KG_ROW, b ? cmul(gq[q], eb[2 * b]) : gq[q]);
#pragma unroll
        for (int q = 4; q < 8; q++)
#pragma unroll
          for (int b = 0; b < 2; b++) st_x(wp + (24 + (q - 4) * 2 + b) * KG_ROW, b ? cmul(gq[q], e2) : gq[q]);
      }
    }
    STAMP(3);
    wait_vm_lgkm<1>(); bar();
    if (tid < 16) st8_dev(wsr, FL_1 + tid * 2048 + bid * 8, tag);
    char* kt = smem + P0_KT + chl * 12288;
    float kinv;
    const float kscale = pow2_scale(ms[chl * 2] + ms[chl * 2 + 1], 14, kinv);
    const float fsc = ms[4 + chl * 2 + ((w & 3) >> 1)] * (kscale * (1.0f / 16384.f));
    {
      const int wv = w & 3;
      const char* pa = img + r16 * KG_ROW + wv * 128 + g * 16;
      u32x4 a[3][2], yh[2], yl[2];
#pragma unroll
      for (int s = 0; s < 2; s++) {
#pragma unroll
        for (int t = 0; t < 3; t++) a[t][s] = *(const u32x4*)(pa + t * 16 * KG_ROW + s * 64);
        yh[s] = *(const u32x4*)(pa + 41 * KG_ROW + s * 64);
        yl[s] = *(const u32x4*)(pa + 57 * KG_ROW + s * 64);
      }
      const float f = fsc;
      char* kw = kt + (4 * g * 16 + r16) * 16 + ((wv + (r16 >> 3) + 2 * (g & 1)) & 3) * 4;
      f32x4 c[3];
#pragma unroll
      for (int t = 0; t < 3; t++) { mmah0(c[t], a[t][0], yl[0]); mmah(c[t], a[t][1], yl[1]); }
#pragma unroll
      for (int t = 0; t < 3; t++) { mmah(c[t], a[t][0], yh[0]); mmah(c[t], a[t][1], yh[1]); }
      mfence(); pin(c[0]); pin(c[1]); pin(c[2]);
#pragma unroll
      for (int t = 0; t < 3; t++)
#pragma unroll
        for (int r = 0; r < 4; r++) *(float*)(kw + (t * 16 + r) * 256) = c[t][r] * f;
    }
    if (w == 0) {
      poll256(wsr, FL_1 + (bid & 15) * 2048, tag, lane);
      if (bid == 0 && lane == 0) st8_dev(wsr, FL_EPOCH, tag);
    }
    lds_bar();
    STAMP(4);
    {
      const char* sa = p.ws + WS_U8 + (size_t)w * 16 * 16384 + cp * 64;
      const u32 vo = (u32)((lane >> 2) * 16384 + ((lane & 3) ^ (lane >> 4)) * 16);
      gldsd<CV_UI, 0>(vo, sa, lds0 + w * 1024); gldsd<CV_UI + 8192, 128 * 16384>(vo, sa, lds0 + w * 1024);
    }
    {
      u32x4 o;
#pragma unroll
      for (int i = 0; i < 4; i++) {
        f32x4 a = *(const f32x4*)(kt + (kidx[i] & 0xffffu)), b = *(const f32x4*)(kt + (kidx[i] >> 16));
        o[i] = pkh((a[0] + a[1]) + (a[2] + a[3]), (b[0] + b[1]) + (b[2] + b[3]));
      }
      const int e = tid & 255;
      char* base = smem + chl * CV_CH;
      *(u32x4*)(base + CV_KP + (30 + e) * 16) = o;
      if (e < 30) *(u32x4*)(base + CV_KP + e * 16) = u32x4{0, 0, 0, 0};
      else if (e < 34) *(u32x4*)(base + (e - 30) * CV_COPY) = u32x4{0, 0, 0, 0};
      else if (e == 34) *(float*)(base + CV_KP + 4576) = kinv;
    }
  }
  STAMP(5);
  STAMP(6);
  {
    {
      const int chl = w >> 2, q = tid & 255;
      char* base = smem + chl * CV_CH;
      wait_vm<0>(); keep(TKW); bar();
      const char* ui = smem + CV_UI + q * 64;
      const int sw = ((2 * chl) ^ ((q >> 2) & 3)) * 16, swp = ((2 * chl) ^ (((q - 1) >> 2) & 3)) * 16;
      u32x4 cur = *(const u32x4*)(ui + sw), prv = *(const u32x4*)(ui - 64 + swp);
      *(u32x4*)(base + CV_XS + q * 16) = *(const u32x4*)(ui + (sw ^ 16));
      if (q == 0) prv = u32x4{0, 0, 0, 0};
      u32 wd[8] = {prv[0], prv[1], prv[2], prv[3], cur[0], cur[1], cur[2], cur[3]};
      u32 o[5];
#pragma unroll
      for (int k = 0; k < 5; k++) o[k] = __builtin_amdgcn_alignbit(wd[k + 1], wd[k], 16);
      char* d = base + (q + 1) * 16;
      *(u32x4*)(d) = u32x4{o[0], o[1], o[2], o[3]};
      *(u32x4*)(d + CV_COPY) = u32x4{wd[1], wd[2], wd[3], wd[4]};
      *(u32x4*)(d + 2 * CV_COPY) = u32x4{o[1], o[2], o[3], o[4]};
      *(u32x4*)(d + 3 * CV_COPY) = u32x4{wd[2], wd[3], wd[4], wd[5]};
      if (q == 255) {
        u32 o4 = __builtin_amdgcn_alignbit(cur[1], cur[0], 16), o5 = __builtin_amdgcn_alignbit(cur[2], cur[1], 16);
        u32 o6 = __builtin_amdgcn_alignbit(cur[3], cur[2], 16), o7 = cur[3] >> 16;
        char* d2 = base + 257 * 16;
        *(u32x4*)(d2) = u32x4{o4, o5, o6, o7};
        *(u32x4*)(d2 + CV_COPY) = u32x4{cur[1], cur[2], cur[3], 0};
        *(u32x4*)(d2 + 2 * CV_COPY) = u32x4{o5, o6, o7, 0};
        *(u32x4*)(d2 + 3 * CV_COPY) = u32x4{cur[2], cur[3], 0, 0};
      }
    }
    const int chl = (w >> 1) & 1, hh = w & 1;
    const float dsk = bf2f(DSK);
    const char* base = smem + chl * CV_CH;
    const int j2 = 2 * (hh + 2 * (w >> 2));
    char* red = smem + CV_RED + chl * 16384 + lane * 16;
    lds_bar();
    STAMP(7);
    const float kinv = *(const float*)(base + CV_KP + 4576);
    const int l0 = 256 * j2 + 16 * r16 + 4 * g;
    u32x2 xx[2];
    u32 dd[2][2];
#pragma unroll
    for (int k = 0; k < 2; k++) {
      u32x2 uu = *(const u32x2*)(base + 3 * CV_COPY + (l0 + 256 * k + 12) * 2);
      xx[k] = *(const u32x2*)(base + CV_XS + (l0 + 256 * k) * 2);
      dd[k][0] = pkbf2(hup(uu[0]) * dsk); dd[k][1] = pkbf2(hup(uu[1]) * dsk);
    }
    asm volatile("" : "+v"(dd[0][0]), "+v"(dd[0][1]), "+v"(dd[1][0]), "+v"(dd[1][1]), "+v"(xx[0]), "+v"(xx[1]));
    f32x4 f0 = {0, 0, 0, 0}, f1 = {0, 0, 0, 0};
    if (w < 4) {
      f32x4 acc[8];
      const char* pa = base + (r16 & 3) * CV_COPY + 48 - 16 * g + 2 * (r16 & 12) + 256 * hh;
      const char* pb = base + CV_KP + 448 + 32 * r16 + 16 * g - 256 * hh;
      u32x4 A0[8], B0[8], A1[8], B1[8];
#pragma unroll
      for (int i = 0; i < 8; i++) conv_load1(pa, pb, 0, i, A0, B0);
      conv_step(pa, pb, 1, A0, B0, A1, B1, acc);
      conv_step(pa, pb, 2, A1, B1, A0, B0, acc);
      conv_step(pa, pb, 3, A0, B0, A1, B1, acc);
      conv_step(pa, pb, 4, A1, B1, A0, B0, acc);
      mfence();
#pragma unroll
      for (int i = 0; i < 8; i++) pin(acc[i]);
      STAMP(27);
      char* d = red + hh * 8192;
      if (hh) { *(f32x4*)(d) = acc[0]; *(f32x4*)(d + 1024) = acc[1]; f0 = acc[2]; f1 = acc[3]; }
      else { *(f32x4*)(d + 2048) = acc[2]; *(f32x4*)(d + 3072) = acc[3]; f0 = acc[0]; f1 = acc[1]; }
#pragma unroll
      for (int t = 4; t < 8; t++) *(f32x4*)(d + t * 1024) = acc[t];
    } else {
      w64_issue<D_>(p.w1 + col0, lds0 + GA_B, w - 4, lane);
      w64_issue<D_>(p.w1 + col0, lds0 + GA_B, w, lane);
      wait_vm<0>();
    }
    lds_bar();
    STAMP(8);
    if (w < 4) {
      f0 += *(const f32x4*)(red + (hh ^ 1) * 8192 + j2 * 1024);
      f1 += *(const f32x4*)(red + (hh ^ 1) * 8192 + j2 * 1024 + 1024);
    } else {
      f0 = *(const f32x4*)(red + j2 * 1024) + *(const f32x4*)(red + 8192 + j2 * 1024);
      f1 = *(const f32x4*)(red + j2 * 1024 + 1024) + *(const f32x4*)(red + 8192 + j2 * 1024 + 1024);
    }
    char* stg = smem + CV_STG;
#pragma unroll
    for (int k = 0; k < 2; k++) {
      f32x4 f = k ? f1 : f0;
      u32 y01 = pkbf2(f32x2{f[0], f[1]} * kinv), y23 = pkbf2(f32x2{f[2], f[3]} * kinv);
      u32 s01 = pkbf2(bfup(y01) + bfup(dd[k][0])), s23 = pkbf2(bfup(y23) + bfup(dd[k][1]));
      u32 h01 = pkbf2(bfup(xx[k][0]) + bfup(s01)), h23 = pkbf2(bfup(xx[k][1]) + bfup(s23));
      *(u32x2*)(stg + (16 * (j2 + k) + r16) * 80 + chl * 32 + g * 8) = u32x2{h01, h23};
    }
    lds_bar();
    {
      const int lb = tid >> 2, pc = tid & 3;
      st16_dev(wsr, WS_HT + cp * 8192 + tid * 16, *(const u32x4*)(stg + lb * 80 + pc * 16));
    }
  }
  const int mt = w & 3, wn = w >> 2;
  const int rt = (pm >> 1) * 8 + (pm & 1) * 2 + (mt >> 1) * 4 + (mt & 1);
  const int pc4 = (g & 1) * 2 + (g >> 1);
  const u32 boff = (u32)((rt * 16 + pn * 2 + wn) * 1024 + r16 * 64 + pc4 * 16);
  u32x4 ONES4 = {ONES, ONES, ONES, ONES};
  asm volatile("s_nop 1" : "+v"(ONES4));
  const u32 dw = lds0 + w * 1024;
  const u32 vbD = kn_voff(D_, w, lane);
  u32x4 hres, xres;
  u32x4 B[16][2];
  constexpr int CBD = 128 * D_, CB3 = 128 * F_;
  const int idx3 = bid >> 3;
  const bool has3 = idx3 < 28;
  const int pm3 = (bid & 7) * 2 + (idx3 >= 14 ? 1 : 0), pn3 = idx3 >= 28 ? idx3 - 28 : idx3 >= 14 ? idx3 - 14 : idx3;
  const u16* sb3 = p.w3 + pn3 * 128;
  const u32 vbF = kn_voff(F_, w, lane);
  const u32 vbF4 = kn_voff(F_, w - 4, lane);
  const int wm3 = w & 1, wn3 = w >> 1;
  const int ob0 = (kn_base(lane) + (wn3 >> 1) * 8192) ^ ((2 * (wn3 & 1)) << 5), ob1 = ob0 ^ 32;
  f32x4 cw0, cw1;
  u32x4 Bt[2][8];
  const char* pb0 = smem + GA_B + (kn_base(lane) ^ ((2 * wn) << 5));
  const char* pb1 = smem + GA_B + (kn_base(lane) ^ ((2 * wn + 1) << 5));
  auto ldB = [&](int j) {
#pragma unroll
    for (int i = 0; i < 4; i++) { Bt[j & 1][2 * i] = kn_frag(pb0 + (4 * j + i) * 4096); Bt[j & 1][2 * i + 1] = kn_frag(pb1 + (4 * j + i) * 4096); }
    SCHED();
  };
  auto ldB3 = [&](int s0, int n, int o) {
#pragma unroll
    for (int s = 0; s < 8; s++) if (s < n) {
      B[s0 + s][0] = kn_frag(smem + o + (s >> 1) * 16384 + (s & 1) * 4096 + ob0);
      B[s0 + s][1] = kn_frag(smem + o + (s >> 1) * 16384 + (s & 1) * 4096 + ob1);
    }
#pragma unroll
    for (int s = 0; s < 8; s++) if (s < n) { mmaz(s0 + s == 0, cw0, B[s0 + s][0], ONES4); mmaz(s0 + s == 0, cw1, B[s0 + s][1], ONES4); }
  };
#define W2C(c) glds<GA_B + (c) * 8192, (c) * CBD>(vbD, p.w2 + col0, dw);
#define W3C(c, dst) glds<(dst), (c) * CB3>(vbF, sb3, dw); glds<(dst) + 8192, (c) * CB3 + 128>(vbF, sb3, dw);
#define W3C4(c, dst) W3C(c, dst) glds<(dst), (c) * CB3>(vbF4, sb3, dw - 4096); glds<(dst) + 8192, (c) * CB3 + 128>(vbF4, sb3, dw - 4096);
  {
    wait_vm_lgkm<0>(); bar();
    if (tid < 16) st8_dev(wsr, FL_2 + tid * 2048 + bid * 8, tag);
    STAMP(9);
    f32x4 cv0, cv1;
    {
#pragma unroll
      for (int i = 0; i < 4; i++) { Bt[0][2 * i] = kn_frag(pb0 + (4 * mt + i) * 4096); Bt[0][2 * i + 1] = kn_frag(pb1 + (4 * mt + i) * 4096); }
#pragma unroll
      for (int i = 0; i < 4; i++) { mmaz(i == 0, cv0, Bt[0][2 * i], ONES4); mmaz(i == 0, cv1, Bt[0][2 * i + 1], ONES4); }
      mfence(); pin(cv0); pin(cv1);
      char* ct = smem + 65536 + wn * 128 + g * 16;
      if (r16 == 0) { *(f32x4*)(ct + mt * 256) = cv0; *(f32x4*)(ct + mt * 256 + 64) = cv1; }
      lds_bar();
      cv0 = (*(const f32x4*)(ct) + *(const f32x4*)(ct + 256)) + (*(const f32x4*)(ct + 512) + *(const f32x4*)(ct + 768));
      cv1 = (*(const f32x4*)(ct + 64) + *(const f32x4*)(ct + 320)) + (*(const f32x4*)(ct + 576) + *(const f32x4*)(ct + 832));
    }
    lds_bar();
    if (w >= 4) { W3C4(0, 65536) W3C4(1, 81920) }
    ldB(0); ldB(1);
    if (w == 0) poll256(wsr, FL_2 + (bid & 15) * 2048, tag, lane);
    bar();
    STAMP(10);
    {
      const int v4 = (mt & 1) + 2 * wn;
      const char* sa = p.ws + WS_HT + (rt & ~1) * 64 + (size_t)v4 * 131072;
      const u32 vo = (u32)((lane >> 3) * 8192 + ((lane & 7) ^ (((lane >> 5) & 1) << 2)) * 16);
      const u32 d = lds0 + GA_A + (mt >> 1) * 32768 + v4 * 2048;
      gldsd<0, 0>(vo, sa, d); gldsd<1024, 65536>(vo, sa, d); gldsd<8192, 4 * 131072>(vo, sa, d); gldsd<9216, 4 * 131072 + 65536>(vo, sa, d);
      gldsd<16384, 8 * 131072>(vo, sa, d); gldsd<17408, 8 * 131072 + 65536>(vo, sa, d); gldsd<24576, 12 * 131072>(vo, sa, d); gldsd<25600, 12 * 131072 + 65536>(vo, sa, d);
    }
    const char* aw = smem + GA_A + (mt >> 1) * 32768 + ((lane & 15) >> 3) * 128 + ((lane >> 2) & 1) * 32 + (lane & 3) * 8;
    const char* pa0 = aw + g * 512 + (((mt ^ g) & 1) << 6);
    f32x4 acc0, acc1, sacc, gacc;
    u32x4 a[2][4];
    auto ld = [&](int j) {
#pragma unroll
      for (int i = 0; i < 4; i++) {
        u32x2 x0 = tr_read(pa0 + (4 * j + i) * 2048), x1 = tr_read(pa0 + (4 * j + i) * 2048 + 256);
        a[j & 1][i] = u32x4{x0[0], x0[1], x1[0], x1[1]};
      }
      SCHED();
    };
    auto mm = [&](int j) {
#pragma unroll
      for (int i = 0; i < 4; i++) {
        mmaz(j + i == 0, sacc, ONES4, a[j & 1][i]);
        mmaz(j + i == 0, gacc, a[j & 1][i], a[j & 1][i]);
        mmaz(j + i == 0, acc0, Bt[j & 1][2 * i], a[j & 1][i]);
        mmaz(j + i == 0, acc1, Bt[j & 1][2 * i + 1], a[j & 1][i]);
      }
      SCHED();
    };
    SCHED();
    wait_vm<6>(); bar(); ld(0);
    STAMP(23);
    mm(0); ldB(2);
    wait_vm<4>(); bar(); ld(1); mm(1); ldB(3);
    wait_vm<2>(); bar(); ld(2); mm(2);
    wait_vm_lgkm<0>(); bar(); ld(3);
    W2C(0) W2C(1)
    STAMP(24);
    const char* hs = aw + (2 * pn + wn) * 2048 + pc4 * 512 + (((mt ^ pc4) & 1) << 6);
    u32x2 hx0 = tr_read(hs), hx1 = tr_read(hs + 256);
    SCHED(); mm(3); W2C(2) W2C(3)
    mfence(); pin(acc0); pin(acc1); pin(sacc); pin(gacc);
    STAMP(11);
    float dg = (r16 & 2) ? ((r16 & 1) ? gacc[3] : gacc[2]) : ((r16 & 1) ? gacc[1] : gacc[0]);
    f32x2 sa2 = {sacc[0], gsum(g == (r16 >> 2) ? dg : 0.f)};
    float mean1 = sa2[0] * (1.0f / D_);
    float var1 = fmaxf(sa2[1] * (1.0f / D_) - mean1 * mean1, 0.f);
    float m1 = rbf(mean1);
    float sd1 = rbf(__builtin_amdgcn_sqrtf(rbf(rbf(var1) + 1e-5f)));
    float i1 = __builtin_amdgcn_rcpf(sd1);
    float m2 = rbf((mean1 - m1) * i1);
    float sd2 = rbf(__builtin_amdgcn_sqrtf(rbf(rbf(var1 * i1 * i1) + 1e-5f)));
    float rs = i1 * __builtin_amdgcn_rcpf(sd2), tt = -(m1 + m2 * sd1) * rs;
    {
      f32x4 rt4 = {rs, rs, tt, tt};
      pin(rt4);
      u32x2 o0 = act4(acc0, cv0, rt4), o1 = act4(acc1, cv1, rt4);
      st16_dev(wsr, WS_H1 + 16384 + boff, pair_swap(o0[0], o0[1], o1[0], o1[1]));
    }
    if (tid < 16) {
      const u32 o = (u32)((pn * 2 + (tid >> 3)) * 1024 + (tid & 7) * 16);
      if (pm == 0) st16_dev(wsr, WS_H1 + 14 * 64 + o, u32x4{0, 0, 0, 0});
      if (pm == 31) st16_dev(wsr, WS_H1 + 129 * 16384 + o, u32x4{0, 0, 0, 0});
    }
    hres = u32x4{hx0[0], hx0[1], hx1[0], hx1[1]};
#pragma unroll
    for (int i = 0; i < 4; i++) xres[i] = pkbf2(bfup(pkbf2(bfup(hres[i]) - m1)) * i1);
    W2C(4) W2C(5) W2C(6) W2C(7)
    wait_vm_lgkm<4>(); bar();
    if (tid < 8) st8_dev(wsr, FL_3 + tid * 2048 + (pm * 8 + pn) * 8, tag);
    ldB3(0, 4, 65536);
    ldB(0); ldB(1);
  }
  STAMP(12);
  const int ar = lane >> 3, asp = (lane & 7) ^ ar;
  const u32 apo = (u32)((asp >> 2) * 1024 + (asp & 3) * 16);
  {
    u32 vo[8];
#pragma unroll
    for (int k = 0; k < 8; k++) {
      const int sh = (k >> 1) == 0 ? -1 : (k >> 1) == 1 ? 1 : (k >> 1) == 2 ? -2 : 2;
      const int row = 16 + ar + (k & 1) * 8 + sh;
      vo[k] = (u32)((row >> 4) * 16384 + (row & 15) * 64) + apo;
    }
    if (w == 0) polln(wsr, FL_3 + pn * 2048, max((pm & ~1) - 1, 0) * 8, min((pm & ~1) + 3, 32) * 8, tag, lane);
    lds_bar();
    STAMP(13);
    {
      const char* sa = p.ws + WS_H1 + (size_t)rt * 16384 + wn * 2048;
      const u32 d = lds0 + GA_A + mt * 16384 + wn * 2048;
      gldsd<0, 0>(vo[0], sa, d); gldsd<1024, 0>(vo[1], sa, d); gldsd<4096, 4096>(vo[2], sa, d); gldsd<5120, 4096>(vo[3], sa, d);
      gldsd<8192, 8192>(vo[4], sa, d); gldsd<9216, 8192>(vo[5], sa, d); gldsd<12288, 12288>(vo[6], sa, d); gldsd<13312, 12288>(vo[7], sa, d);
    }

    const char* pa0 = smem + GA_A + mt * 16384 + mk_base(lane, 0);
    const char* pa1 = smem + GA_A + mt * 16384 + mk_base(lane, 1);
    f32x4 acc0, acc1;
    u32x4 a[2][4];
    auto ld = [&](int j) {
      a[j & 1][0] = *(const u32x4*)(pa0 + j * 4096); a[j & 1][1] = *(const u32x4*)(pa1 + j * 4096);
      a[j & 1][2] = *(const u32x4*)(pa0 + j * 4096 + 2048); a[j & 1][3] = *(const u32x4*)(pa1 + j * 4096 + 2048);
      SCHED();
    };
    auto mm = [&](int j) {
#pragma unroll
      for (int i = 0; i < 4; i++) {
        mmaz(j + i == 0, acc0, Bt[j & 1][2 * i], a[j & 1][i]);
        mmaz(j + i == 0, acc1, Bt[j & 1][2 * i + 1], a[j & 1][i]);
      }
      SCHED();
    };
    SCHED();
    wait_vm<6>(); bar(); ld(0);
    STAMP(25);
    wait_vm<4>(); bar(); ld(1); mm(0); ldB(2);
    wait_vm<2>(); bar(); ld(2); mm(1); ldB(3);
    wait_vm_lgkm<0>(); bar(); ld(3);
    W3C(2, 65536)
    STAMP(26);
    mm(2); W3C(3, 81920) mm(3);
    mfence(); pin(acc0); pin(acc1);
    STAMP(14);
    u32x4 v = pair_swap(pkbf(acc0[0], acc0[1]), pkbf(acc0[2], acc0[3]), pkbf(acc1[0], acc1[1]), pkbf(acc1[2], acc1[3]));
#pragma unroll
    for (int i = 0; i < 4; i++) xres[i] = pkbf2(bfup(xres[i]) + bfup(v[i]));
    st16_dev(wsr, WS_X1 + boff, xres);
    float s1 = 0, s2 = 0;
#pragma unroll
    for (int i = 0; i < 4; i++) { s1 = dot2(xres[i], ONES, s1); s2 = dot2(xres[i], xres[i], s2); }
    s1 = gsum(s1); s2 = gsum(s2);
    if (g == 0) st8_dev(wsr, WS_ST + (rt * 16 + pn * 2 + wn) * 128 + r16 * 8, u32x2{__float_as_uint(s1), __float_as_uint(s2)});
    W3C(4, GA_B) W3C(5, GA_B + 16384)
    wait_vm_lgkm<4>(); bar();
    if (tid < 8) st8_dev(wsr, FL_4 + tid * 2048 + (pm * 8 + pn) * 8, tag);
    W3C(6, GA_B + 32768) W3C(7, GA_B + 49152)
  }
  STAMP(15);
  const u16* sb4 = p.w4 + col0;
#define G4_R1A(i) glds<GA_B + (i) * 8192, (((i) >> 1) * 7 + ((i) & 1)) * CBD>(vbD, sb4, dw);
#define G4_R1A4(i) G4_R1A(i) glds<GA_B + (i) * 8192, (((i) >> 1) * 7 + ((i) & 1)) * CBD>(vbD4, sb4, dw - 4096);
#define G4_R1A_ALL4 G4_R1A4(0) G4_R1A4(1) G4_R1A4(2) G4_R1A4(3) G4_R1A4(4) G4_R1A4(5) G4_R1A4(6) G4_R1A4(7)
  const u32 vbD4 = kn_voff(D_, w - 4, lane);
#define G4_R1B(i) glds<((i) < 2 ? G3_ST : 0) + (i) * 8192, (((i) >> 1) * 7 + 2 + ((i) & 1)) * CBD>(vbD, sb4, dw);
#define G4_R2POS(i) ((i) < 4 ? 32768 + (i) * 8192 : GA_B + ((i) - 4) * 8192)
#define G4_R2(i) glds<G4_R2POS(i), (((i) / 3) * 7 + 4 + ((i) % 3)) * CBD>(vbD, sb4, dw);
  const int wm4 = mt >> 1, kq = (mt & 1) + 2 * wn;
  const int kb = kn_base(lane);
  u32x4(&B4)[8][4] = *reinterpret_cast<u32x4(*)[8][4]>(&B);
  const char* sa4 = p.ws + WS_HID + (size_t)((rt & ~1) * 56 + 14 * kq) * 1024;
  const u32 va4 = (u32)(ar * 64) + apo;
  const int ring = (wm4 ? 0 : 65536) + kq * 8192;
  const u32 da = lds0 + ring;
#define G4_A4(b) gldsd<((b) & 1) * 4096, (b) * 2048>(va4, sa4, da); gldsd<((b) & 1) * 4096 + 1024, (b) * 2048 + 512>(va4, sa4, da); \
                 gldsd<((b) & 1) * 4096 + 2048, (b) * 2048 + 57344>(va4, sa4, da); gldsd<((b) & 1) * 4096 + 3072, (b) * 2048 + 57344 + 512>(va4, sa4, da);
  auto ldB4 = [&](int s) {
    const char* p0 = smem + GA_B + kq * 16384 + s * 4096;
    const char* p1 = smem + (kq ? kq * 16384 : G3_ST) + s * 4096;
#pragma unroll
    for (int nt = 0; nt < 4; nt++) { B4[s][nt] = kn_frag(p0 + (kb ^ (nt << 5))); B4[4 + s][nt] = kn_frag(p1 + (kb ^ (nt << 5))); }
    SCHED();
  };
  {
    const int wm = wm3;
    ldB3(4, 4, 65536);
    wait_vm<0>(); bar();
    ldB3(8, 8, GA_B);
    if (w == 0) polln(wsr, FL_4 + pn * 2048, pm3 * 16, pm3 * 16 + 16, tag, lane);
    lds_bar();
    STAMP(16);
    const char* sa = p.ws + WS_X1 + (size_t)(pm3 * 8 + (w >> 1)) * 16384 + (w & 1) * 512;
    const u32 va = (u32)(ar * 64) + apo;
#define G3_A(i) gldsd<(((i) + 4) % 9) * 8192, ((i) >> 3) * 65536 + ((i) & 7) * 2048>(va, sa, dw);
    {
      const char* ss = p.ws + WS_ST + (size_t)pm3 * 16384 + w * 1024;
      gldsd<G3_ST, 0>(lane * 16, ss, dw); gldsd<G3_ST + 8192, 8192>(lane * 16, ss, dw);
    }
    G3_A(0) G3_A(1) G3_A(2) G3_A(3) G3_A(4) G3_A(5) G3_A(6) G3_A(7) G3_A(8)
    wait_vm<9>(); bar();
    {
      const char* sst = smem + G3_ST + (tid >> 6) * 2048 + (tid & 3) * 512 + ((tid >> 2) & 15) * 8;
      f32x2 st = (*(const f32x2*)(sst) + *(const f32x2*)(sst + 128)) + (*(const f32x2*)(sst + 256) + *(const f32x2*)(sst + 384));
      float t1 = st[0] + dppf<0xB1>(st[0]), t2 = st[1] + dppf<0xB1>(st[1]);
      t1 += dppf<0x4E>(t1); t2 += dppf<0x4E>(t2);
      float mean = t1 * (1.0f / D_);
      float var = fmaxf(t2 * (1.0f / D_) - mean * mean, 0.f);
      float rs = __builtin_amdgcn_rcpf(rbf(__builtin_amdgcn_sqrtf(rbf(rbf(var) + 1e-5f))));
      if ((tid & 3) == 0) { float tt = -rbf(mean) * rs; *(f32x4*)(smem + G3_TAB + (tid >> 2) * 16) = f32x4{rs, rs, tt, tt}; }
    }
    const f32x4& cv0 = cw0;
    const f32x4& cv1 = cw1;
    float* rowt = (float*)(smem + G3_TAB);
    const int oa0 = mk_base(lane, 0) + wm * 4096, oa1 = mk_base(lane, 1) + wm * 4096;
    f32x4 acc[2][2][2];
    u32x4 a[2][4];
    auto ld = [&](int slot, u32x4 (&d)[4]) {
      const char* s = smem + slot * 8192;
      d[0] = *(const u32x4*)(s + oa0); d[1] = *(const u32x4*)(s + oa0 + 2048);
      d[2] = *(const u32x4*)(s + oa1); d[3] = *(const u32x4*)(s + oa1 + 2048);
      SCHED();
    };
    auto m1 = [&](int ps, int b, int i, const u32x4 (&d)[4]) {
      mmaz(b == 0 && i < 4, acc[ps][(i >> 1) & 1][i & 1], B[2 * b + (i >> 2)][(i >> 1) & 1], d[2 * (i >> 2) + (i & 1)]);
      SCHED();
    };
    auto mm = [&](int ps, int b, const u32x4 (&d)[4]) {
#pragma unroll
      for (int i = 0; i < 8; i++) m1(ps, b, i, d);
    };
    const u32 hoff = (has3 ? WS_HID : 0x40000000u) + (u32)(((pm3 * 8 + 2 * wm) * 56 + pn3 * 4 + wn3) * 1024 + r16 * 64 + pc4 * 16);
    u32x2 o0;
    f32x4 rtv[2];
    auto ldrt = [&](int ps) {
      rtv[0] = *(const f32x4*)(rowt + (64 * ps + 32 * wm + r16) * 4);
      rtv[1] = *(const f32x4*)(rowt + (64 * ps + 32 * wm + 16 + r16) * 4);
    };
    auto ep = [&](int ps, int i) {
      const int mi = i >> 1;
      if ((i & 1) == 0) o0 = act4(acc[ps][0][mi], cv0, rtv[mi]);
      else {
        u32x2 o1 = act4(acc[ps][1][mi], cv1, rtv[mi]);
        st16_dev(wsr, hoff + (4 * ps + mi) * 57344, pair_swap(o0[0], o0[1], o1[0], o1[1]));
      }
    };
    float c0v = -2.0f * 0.7978845608028654f * 1.4426950408889634f;
    asm volatile("" : "+v"(c0v));
    auto mme = [&](int b, const u32x4 (&d)[4], int e) {
      const float c1 = -2.0f * 0.7978845608028654f * 1.4426950408889634f * 0.044715f;
      const int mi = e >> 1;
      const f32x4 aa = acc[0][e & 1][mi], cc = (e & 1) ? cv1 : cv0;
      float v[4], x[4], z[4];
      m1(1, b, 0, d);
#pragma unroll
      for (int q = 0; q < 4; q++) v[q] = smul(aa[q], rtv[mi][0]);
      SCHED(); m1(1, b, 1, d);
#pragma unroll
      for (int q = 0; q < 4; q++) v[q] = sfma(cc[q], rtv[mi][2], v[q]);
      u32 p0 = pkbf(v[0], v[1]), p1 = pkbf(v[2], v[3]);
      SCHED(); m1(1, b, 2, d);
      x[0] = bflo(p0); x[1] = bfhi(p0); x[2] = bflo(p1); x[3] = bfhi(p1);
      z[0] = smul(x[0], x[0]); z[1] = smul(x[1], x[1]);
      SCHED(); m1(1, b, 3, d);
      z[2] = smul(x[2], x[2]); z[3] = smul(x[3], x[3]);
#pragma unroll
      for (int q = 0; q < 4; q++) z[q] = sfmak(z[q], c1, c0v);
      SCHED(); m1(1, b, 4, d);
#pragma unroll
      for (int q = 0; q < 4; q++) z[q] = smul(z[q], x[q]);
      z[0] = __builtin_amdgcn_exp2f(z[0]); z[1] = __builtin_amdgcn_exp2f(z[1]);
      SCHED(); m1(1, b, 5, d);
      z[2] = __builtin_amdgcn_exp2f(z[2]); z[3] = __builtin_amdgcn_exp2f(z[3]);
#pragma unroll
      for (int q = 0; q < 4; q++) z[q] = sadd1(z[q]);
      SCHED(); m1(1, b, 6, d);
#pragma unroll
      for (int q = 0; q < 4; q++) z[q] = __builtin_amdgcn_rcpf(z[q]);
      SCHED(); m1(1, b, 7, d);
#pragma unroll
      for (int q = 0; q < 4; q++) x[q] = smul(x[q], z[q]);
      u32x2 o = {pkbf(x[0], x[1]), pkbf(x[2], x[3])};
      if (e & 1) st16_dev(wsr, hoff + mi * 57344, pair_swap(o0[0], o0[1], o[0], o[1])); else o0 = o;
      SCHED();
    };
    SCHED();
#define SL(i) (((i) + 4) % 9)
    wait_vm_lgkm<8>(); bar(); ld(SL(0), a[0]); G4_R1A(0)
    wait_vm_lgkm<8>(); bar(); ld(SL(1), a[1]); G4_R1A(1) mm(0, 0, a[0]); SCHED();
    wait_vm_lgkm<8>(); bar(); G3_A(9) ld(SL(2), a[0]); G4_R1A(2) mm(0, 1, a[1]); SCHED();
    wait_vm_lgkm<9>(); bar(); G3_A(10) ld(SL(3), a[1]); G4_R1A(3) mm(0, 2, a[0]); SCHED();
    wait_vm_lgkm<10>(); bar(); G3_A(11) ld(SL(4), a[0]); G4_R1A(4) mm(0, 3, a[1]); SCHED();
    wait_vm_lgkm<11>(); bar(); G3_A(12) ld(SL(5), a[1]); G4_R1A(5) mm(0, 4, a[0]); SCHED();
    wait_vm_lgkm<12>(); bar(); G3_A(13) ld(SL(6), a[0]); G4_R1A(6) mm(0, 5, a[1]); SCHED();
    wait_vm_lgkm<13>(); bar(); G3_A(14) ld(SL(7), a[1]); G4_R1A(7) mm(0, 6, a[0]); SCHED();
    wait_vm_lgkm<14>(); bar(); G3_A(15) ld(SL(8), a[0]); ldrt(0); mm(0, 7, a[1]); SCHED();
    STAMP(17);
    mfence();
#pragma unroll
    for (int i = 0; i < 4; i++) pin(acc[0][i >> 1][i & 1]);
#pragma unroll
    for (int i = 0; i < 4; i++) ep(0, i);
    SCHED();
    wait_vm_lgkm<14>(); bar(); ld(SL(9), a[1]); G4_R1B(2) G4_R1B(0) mm(1, 0, a[0]); SCHED();
    wait_vm_lgkm<14>(); bar(); ld(SL(10), a[0]); G4_R1B(3) G4_R1B(1) mm(1, 1, a[1]); SCHED();
    wait_vm_lgkm<14>(); bar(); ld(SL(11), a[1]); G4_R1B(4) mm(1, 2, a[0]); SCHED();
    wait_vm_lgkm<5>(); bar();
    if (has3 && tid < 8) st8_dev(wsr, FL_5 + tid * 2048 + (pm3 * 14 + pn3) * 8, tag);
    ld(SL(12), a[0]); G4_R1B(5) mm(1, 3, a[1]); SCHED();
    lds_bar(); ld(SL(13), a[1]); G4_R1B(6) mm(1, 4, a[0]); SCHED();
    lds_bar(); ld(SL(14), a[0]); G4_R1B(7) mm(1, 5, a[1]); SCHED();
    lds_bar(); ld(SL(15), a[1]); ldrt(1); mm(1, 6, a[0]); SCHED();
    wait_vm_lgkm<0>(); bar();
    mm(1, 7, a[1]);
    mfence();
#pragma unroll
    for (int i = 0; i < 4; i++) pin(acc[1][i >> 1][i & 1]);
    STAMP(18);
#pragma unroll
    for (int i = 0; i < 4; i++) { ldB4(i); ep(1, i); SCHED(); }
    if (w == 0) polln(wsr, FL_5 + pn * 2048, (pm >> 1) * 14, (pm >> 1) * 14 + 14, tag, lane);
    lds_bar();
    if (wm4 == 0) { G4_A4(0) G4_A4(1) wait_vm<8>(); } else wait_vm<0>();
    bar();
    if (has3 && tid < 8) st8_dev(wsr, FL_6 + tid * 2048 + (pm3 * 14 + pn3) * 8, tag);
  }
  STAMP(19);
  {
    const int wm = wm4;
    if (wm) {
      const u32 vb2 = kn_voff(D_, w - 2, lane), dw2 = dw - 2048;
#define G4_R2X(i) G4_R2(i) glds<G4_R2POS(i), (((i) / 3) * 7 + 4 + ((i) % 3)) * CBD>(vb2, sb4, dw2);
      G4_R2X(0) G4_R2X(3) G4_R2X(6) G4_R2X(9) G4_R2X(1) G4_R2X(4) G4_R2X(7) G4_R2X(10) G4_R2X(2) G4_R2X(5) G4_R2X(8) G4_R2X(11)
      polln(wsr, FL_6 + (pn ^ kq) * 2048, (pm >> 1) * 14, (pm >> 1) * 14 + 14, tag, lane);
      G4_A4(0) G4_A4(1)
      wait_vm<8>(); bar();
    }
    STAMP(20);
    const char* pa0 = smem + ring + mk_base(lane, 0);
    const char* pa1 = smem + ring + mk_base(lane, 1);
    f32x4 acc[4][2];
    u32x4 a[4];
    auto ld = [&](int b) {
      a[0] = *(const u32x4*)(pa0 + (b & 1) * 4096); a[1] = *(const u32x4*)(pa0 + (b & 1) * 4096 + 2048);
      a[2] = *(const u32x4*)(pa1 + (b & 1) * 4096); a[3] = *(const u32x4*)(pa1 + (b & 1) * 4096 + 2048);
      asm volatile("s_waitcnt lgkmcnt(0)" ::: "memory");
      SCHED();
    };
    auto mm = [&](int s0, bool first = false) {
#pragma unroll
      for (int ks = 0; ks < 2; ks++)
#pragma unroll
        for (int nt = 0; nt < 4; nt++)
#pragma unroll
          for (int mi = 0; mi < 2; mi++) mmaz(first && ks == 0, acc[nt][mi], B4[s0 + ks][nt], a[2 * ks + mi]);
      SCHED();
    };
    auto ldb = [&](int j, int s0) {
      const int i = kq * 3 + j;
      const char* bl = smem + (i < 4 ? 32768 + i * 8192 : GA_B + (i - 4) * 8192);
#pragma unroll
      for (int ks = 0; ks < 2; ks++)
#pragma unroll
        for (int nt = 0; nt < 4; nt++) B4[s0 + ks][nt] = kn_frag(bl + ks * 4096 + (kb ^ (nt << 5)));
      SCHED();
    };
    wait_vm<4>(); ld(0); G4_A4(2) mm(0, true);
    wait_vm<4>(); ld(1); G4_A4(3) mm(2);
    wait_vm<4>(); ld(2); G4_A4(4) mm(4);
    wait_vm<4>(); ld(3); G4_A4(5) mm(6);
    if (wm == 0) bar();
    ldb(0, 0); ldb(1, 2);
    wait_vm<4>(); ld(4); G4_A4(6) ldb(2, 4); mm(0);
    if (wm) bar();
    wait_vm<4>(); ld(5); mm(2);
    wait_vm<0>(); ld(6); mm(4);
    mfence();
#pragma unroll
    for (int i = 0; i < 8; i++) pin(acc[i >> 1][i & 1]);
    STAMP(21);
    {
      char* d = smem + ring + lane * 16;
#pragma unroll
      for (int k2 = 0; k2 < 4; k2++) {
        *(f32x4*)(d + k2 * 2048) = acc[2 * (k2 >> 1)][k2 & 1];
        *(f32x4*)(d + k2 * 2048 + 1024) = acc[2 * (k2 >> 1) + 1][k2 & 1];
      }
    }
    lds_bar();
    const char* s = smem + (wm ? 0 : 65536) + kq * 2048 + lane * 16;
    f32x4 f0 = (*(const f32x4*)(s) + *(const f32x4*)(s + 8192)) + (*(const f32x4*)(s + 16384) + *(const f32x4*)(s + 24576));
    f32x4 f1 = (*(const f32x4*)(s + 1024) + *(const f32x4*)(s + 9216)) + (*(const f32x4*)(s + 17408) + *(const f32x4*)(s + 25600));
    u32x4 v = pair_swap(pkbf(f0[0], f0[1]), pkbf(f0[2], f0[3]), pkbf(f1[0], f1[1]), pkbf(f1[2], f1[3]));
    u32x4 o;
#pragma unroll
    for (int i = 0; i < 4; i++) o[i] = pkbf2(bfup(hres[i]) + bfup(pkbf2(bfup(xres[i]) + bfup(v[i]))));
    __builtin_amdgcn_raw_buffer_store_b128(o, __builtin_amdgcn_make_buffer_rsrc(p.out, 0, L_ * D_ * 2, 0x00020000),
                                           (u32)(((rt * 16 + r16) * D_ + col0 + 32 * wn + pc4 * 8) * 2), 0, 16);
  }
  STAMP(22);
  asm volatile(".Lk_end:");
}

extern "C" void kernel_launch(void* const* d_in, const int* in_sizes, int n_in, void* d_out, int out_size, void* d_ws,
                              size_t ws_size, hipStream_t stream) {
  Params p;
  p.x = (const u16*)d_in[0]; p.are = (const u16*)d_in[1]; p.aim = (const u16*)d_in[2]; p.bre = (const u16*)d_in[3];
  p.bim = (const u16*)d_in[4]; p.cre = (const u16*)d_in[5]; p.cim = (const u16*)d_in[6]; p.logdt = (const u16*)d_in[7];
  p.dskip = (const u16*)d_in[8]; p.w1 = (const u16*)d_in[10]; p.w2 = (const u16*)d_in[11]; p.w3 = (const u16*)d_in[12];
  p.w4 = (const u16*)d_in[13]; p.out = (u16*)d_out; p.ws = (char*)d_ws;
  k_fused<<<256, 512, 0, stream>>>(p);
}
```

```cpp
#include <hip/hip_runtime.h>
#include <stdint.h>

typedef unsigned short u16;
typedef unsigned int u32;
typedef unsigned long long u64;
typedef __bf16 bf16x2 __attribute__((ext_vector_type(2)));
typedef __bf16 bf16x8 __attribute__((ext_vector_type(8)));
typedef _Float16 f16x2 __attribute__((ext_vector_type(2)));
typedef _Float16 f16x8 __attribute__((ext_vector_type(8)));
typedef float f32x2 __attribute__((ext_vector_type(2)));
typedef float f32x4 __attribute__((ext_vector_type(4)));
typedef short s16x4 __attribute__((ext_vector_type(4)));
typedef u32 u32x2 __attribute__((ext_vector_type(2)));
typedef u32 u32x4 __attribute__((ext_vector_type(4)));
#define LDSP __attribute__((address_space(3)))
#define DEV __device__ __forceinline__

constexpr int L_ = 2048, D_ = 512, NS_ = 128, F_ = 1792;
constexpr u32 WS_U8 = 0;
constexpr u32 WS_HT = 7u << 20;
constexpr u32 WS_H1 = 13u << 20;
constexpr u32 WS_X1 = 16u << 20;
constexpr u32 WS_ST = 18u << 20;
constexpr u32 WS_HID = 19u << 20;
constexpr u32 WS_FL = 28u << 20;
constexpr u32 FL_EPOCH = WS_FL, FL_1 = WS_FL + 4096, FL_2 = FL_1 + 32768, FL_3 = FL_2 + 32768, FL_4 = FL_3 + 16384, FL_5 = FL_4 + 16384, FL_6 = FL_5 + 16384;

struct Params {
  const u16 *x, *are, *aim, *bre, *bim, *cre, *cim, *logdt, *dskip, *w1, *w2, *w3, *w4;
  u16* out;
  char* ws;
};

DEV float bf2f(u32 v) { return __uint_as_float(v << 16); }
DEV float bflo(u32 v) { return __uint_as_float(v << 16); }
DEV float bfhi(u32 v) { return __uint_as_float(v & 0xffff0000u); }
DEV f32x2 bfup(u32 v) { return f32x2{bflo(v), bfhi(v)}; }
DEV u32 pkbf2(f32x2 v) { return __builtin_bit_cast(u32, __builtin_convertvector(v, bf16x2)); }
DEV u32 pkbf(float a, float b) { return pkbf2(f32x2{a, b}); }
DEV u32 pkh(float a, float b) { f32x2 v = {a, b}; return __builtin_bit_cast(u32, __builtin_convertvector(v, f16x2)); }
DEV float rbf(float a) { return bflo(pkbf(a, a)); }
DEV f32x2 hup(u32 v) { return __builtin_convertvector(__builtin_bit_cast(f16x2, v), f32x2); }

template <int CTRL> DEV float dppf(float v) {
  return __builtin_bit_cast(float, __builtin_amdgcn_update_dpp(0, __builtin_bit_cast(int, v), CTRL, 0xf, 0xf, true));
}
DEV float gsum(float v) {
  u32 a = __float_as_uint(v);
  auto r = __builtin_amdgcn_permlane16_swap(a, a, false, false);
  v = __uint_as_float(r[0]) + __uint_as_float(r[1]);
  a = __float_as_uint(v);
  auto q = __builtin_amdgcn_permlane32_swap(a, a, false, false);
  return __uint_as_float(q[0]) + __uint_as_float(q[1]);
}
DEV float wave_sum(float v) {
  v += dppf<0xB1>(v); v += dppf<0x4E>(v); v += dppf<0x141>(v); v += dppf<0x140>(v);
  return gsum(v);
}
DEV u32x2 tr_read(const char* p) {
  return __builtin_bit_cast(u32x2, __builtin_amdgcn_ds_read_tr16_b64_v4i16((LDSP s16x4*)p));
}
DEV void mma(f32x4& c, u32x4 a, u32x4 b) { asm volatile("v_mfma_f32_16x16x32_bf16 %0, %1, %2, %0" : "+v"(c) : "v"(a), "v"(b)); }
DEV void mma0(f32x4& c, u32x4 a, u32x4 b) { asm volatile("v_mfma_f32_16x16x32_bf16 %0, %1, %2, 0" : "=&v"(c) : "v"(a), "v"(b)); }
DEV void mmah(f32x4& c, u32x4 a, u32x4 b) { asm volatile("v_mfma_f32_16x16x32_f16 %0, %1, %2, %0" : "+v"(c) : "v"(a), "v"(b)); }
DEV void mmah0(f32x4& c, u32x4 a, u32x4 b) { asm volatile("v_mfma_f32_16x16x32_f16 %0, %1, %2, 0" : "=&v"(c) : "v"(a), "v"(b)); }
DEV void mmaz(bool first, f32x4& c, u32x4 a, u32x4 b) { if (first) mma0(c, a, b); else mma(c, a, b); }
DEV void mfence() { asm volatile("s_nop 7" ::: "memory"); __builtin_amdgcn_sched_barrier(0); }
DEV void pin(f32x4& c) { asm volatile("" : "+v"(c)); }
DEV float dot2(u32 a, u32 b, float c) {
  return __builtin_amdgcn_fdot2_f32_bf16(__builtin_bit_cast(bf16x2, a), __builtin_bit_cast(bf16x2, b), c, false);
}
template <int LOFF, int MOFF> DEV void glds(u32 voff, const void* sbase, u32 lds) {
  asm volatile("s_add_u32 m0, %1, %3\n\ts_nop 0\n\tglobal_load_lds_dwordx4 %0, %2"
               :: "v"(voff + (u32)MOFF), "s"(lds), "s"(sbase), "n"(LOFF) : "memory", "m0", "scc");
}
template <int LOFF, int MOFF> DEV void gldsd(u32 voff, const void* sbase, u32 lds) {
  asm volatile("s_add_u32 m0, %1, %3\n\ts_nop 0\n\tglobal_load_lds_dwordx4 %0, %2 sc1"
               :: "v"(voff + (u32)MOFF), "s"(lds), "s"(sbase), "n"(LOFF) : "memory", "m0", "scc");
}
DEV void touch(u32& t, const void* sbase, u32 voff) {
  asm volatile("global_load_dword %0, %1, %2" : "+v"(t) : "v"(voff), "s"(sbase) : "memory");
}
DEV void keep(u32 t) { asm volatile("" :: "v"(t)); }
DEV u32 lds_addr(const char* p) { return (u32)(size_t)(LDSP const char*)p; }
#define SCHED() __builtin_amdgcn_sched_barrier(0)
template <int N> DEV void wait_vm() { asm volatile("s_waitcnt vmcnt(%0)" ::"n"(N) : "memory"); }
template <int N> DEV void wait_vm_lgkm() { asm volatile("s_waitcnt vmcnt(%0) lgkmcnt(0)" ::"n"(N) : "memory"); }
DEV void bar() { __builtin_amdgcn_s_barrier(); }
DEV void lds_bar() { asm volatile("s_waitcnt lgkmcnt(0)" ::: "memory"); __builtin_amdgcn_s_barrier(); }


typedef __amdgpu_buffer_rsrc_t rsrc_t;
DEV void st16_dev(rsrc_t r, u32 off, u32x4 v) { __builtin_amdgcn_raw_buffer_store_b128(v, r, off, 0, 16); }
DEV void st8_dev(rsrc_t r, u32 off, u32x2 v) { __builtin_amdgcn_raw_buffer_store_b64(v, r, off, 0, 16); }
DEV u32x4 ld16_dev(rsrc_t r, u32 off) { return __builtin_amdgcn_raw_buffer_load_b128(r, off, 0, 16); }
DEV u32x2 ld8_dev(rsrc_t r, u32 off) { return __builtin_amdgcn_raw_buffer_load_b64(r, off, 0, 16); }

constexpr int SPIN_MAX = 1 << 18;
DEV void poll256(rsrc_t r, u32 base, u32x2 tag, int lane) {
#pragma unroll 1
  for (int s = 0; s < SPIN_MAX; s++) {
    u32x4 a = ld16_dev(r, base + lane * 32), b = ld16_dev(r, base + lane * 32 + 16);
    bool ok = a[0] == tag[0] && a[1] == tag[1] && a[2] == tag[0] && a[3] == tag[1] &&
              b[0] == tag[0] && b[1] == tag[1] && b[2] == tag[0] && b[3] == tag[1];
    if (__all(ok)) break;
    __builtin_amdgcn_s_sleep(2);
    asm volatile("" ::: "memory");
  }
}
DEV void polln(rsrc_t r, u32 base, int lo, int hi, u32x2 tag, int lane) {
  const int i = min(lo + lane, hi - 1);
#pragma unroll 1
  for (int s = 0; s < SPIN_MAX; s++) {
    u32x2 a = ld8_dev(r, base + i * 8);
    if (__all(a[0] == tag[0] && a[1] == tag[1])) break;
    __builtin_amdgcn_s_sleep(2);
    asm volatile("" ::: "memory");
  }
}

DEV u32x2 act4(f32x4 a, f32x4 cs, f32x4 rt) {
  const float c0 = -2.0f * 0.7978845608028654f * 1.4426950408889634f;
  const float c1 = c0 * 0.044715f;
  const f32x2 rs = {rt[0], rt[1]}, t = {rt[2], rt[3]};
  f32x2 v0 = f32x2{a[0], a[1]} * rs + f32x2{cs[0], cs[1]} * t, v1 = f32x2{a[2], a[3]} * rs + f32x2{cs[2], cs[3]} * t;
  f32x4 x = {v0[0], v0[1], v1[0], v1[1]};
  f32x4 z = x * (x * x * c1 + c0);
  f32x4 e = {__builtin_amdgcn_exp2f(z[0]), __builtin_amdgcn_exp2f(z[1]), __builtin_amdgcn_exp2f(z[2]), __builtin_amdgcn_exp2f(z[3])};
  e += 1.0f;
  f32x4 r = {__builtin_amdgcn_rcpf(e[0]), __builtin_amdgcn_rcpf(e[1]), __builtin_amdgcn_rcpf(e[2]), __builtin_amdgcn_rcpf(e[3])};
  x *= r;
  return u32x2{pkbf(x[0], x[1]), pkbf(x[2], x[3])};
}

constexpr int LDS_ALL = 163840;
constexpr int KG_ROW = 528;
constexpr int KG_CH = 73 * KG_ROW;
constexpr int P0_KT = 77312;
constexpr int P0_LN = 101888;
constexpr int P0_MS = 119296;
constexpr int CV_COPY = 4160, CV_KP = 4 * CV_COPY, CV_XS = CV_KP + 4608, CV_CH = CV_XS + 4096;
constexpr int CV_UI = 2 * CV_CH;
constexpr int CV_RED = CV_UI;
constexpr int CV_STG = 0;
constexpr int GA_A = 0, GA_B = 98304;
constexpr int G3_ST = 73728, G3_TAB = 90112;

struct KTbl {
  u16 v[2048];
  constexpr KTbl() : v() {
    for (int pos = 0; pos < 2048; pos++) {
      int l = (pos & ~7) + 7 - (pos & 7);
      int sh = l < 256 ? 0 : l < 512 ? 1 : l < 1024 ? 2 : 3;
      int lv = sh ? (((l + (1 << (sh - 1)) - 1 + ((l >> sh) & 1)) >> sh) << sh) : l;
      int s = lv < 256 ? 0 : lv < 512 ? 1 : lv < 1024 ? 2 : 3;
      int rr = 8 * s + ((lv >> 7) << (3 - s)) + ((lv & 7) >> s);
      v[pos] = (u16)((rr * 16 + ((lv >> 3) & 15)) * 16);
    }
  }
};
__device__ __attribute__((aligned(16))) constexpr KTbl KTBL{};

typedef f32x2 cf;
DEV cf cmul(cf a, cf b) {
  cf r;
  asm("v_pk_mul_f32 %0, %1, %2 op_sel:[1,1] op_sel_hi:[1,0]\n\t"
      "v_pk_fma_f32 %0, %1, %2, %0 op_sel:[0,0,0] op_sel_hi:[0,1,1] neg_lo:[0,0,1]"
      : "=&v"(r) : "v"(a), "v"(b));
  return r;
}
DEV cf cscale(cf a, float s) { return a * s; }
DEV cf cexpk(float tl2, float rh, float rl, float s) {
  float mag = __builtin_amdgcn_exp2f(tl2 * s);
  float p = rh * s;
  float f = __builtin_fmaf(rl, s, p - __builtin_rintf(p));
  return cf{__builtin_amdgcn_cosf(f), __builtin_amdgcn_sinf(f)} * mag;
}
DEV void st_x(char* p, cf a) { *(u32*)p = pkh(a[0], a[1]); }
DEV void st_y(char* p, cf a) {
  u32 hi = pkh(a[0], a[1]);
  f32x2 r = a - hup(hi);
  *(u32*)p = hi;
  *(u32*)(p + 16 * KG_ROW) = pkh(r[0], r[1]);
}
DEV float pow2_scale(float mx, u32 target, float& inv) {
  u32 eb = __float_as_uint(mx) >> 23;
  u32 se = min(max(254u + target - eb, 1u), 253u);
  inv = __uint_as_float((254u - se) << 23);
  return __uint_as_float(se << 23);
}

DEV void conv_load1(const char* pa, const char* pb, int b0, int i, u32x4 (&A)[8], u32x4 (&B)[8]) {
  u32x2 lo = *(const volatile LDSP u32x2*)(pa + 64 * b0 + 512 * i), hi = *(const volatile LDSP u32x2*)(pa + 64 * b0 + 512 * i + 8);
  A[i] = u32x4{lo[0], lo[1], hi[0], hi[1]};
  B[i] = *(const u32x4*)(pb - 64 * b0 + 512 * i);
}
DEV void conv_step(const char* pa, const char* pb, int nb, const u32x4 (&A)[8], const u32x4 (&B)[8], u32x4 (&An)[8], u32x4 (&Bn)[8], f32x4 (&acc)[8]) {
  u32x2 lo[8], hi[8];
  int c = 0;
#pragma unroll
  for (int i = 0; i < 8; i++) {
#pragma unroll
    for (int t = i; t < 8; t++) {
      if (nb == 1 && i == 0) mmah0(acc[t], A[i], B[t - i]); else mmah(acc[t], A[i], B[t - i]);
      if (nb < 4 && c < 24) {
        const int f = c / 3, k = c % 3;
        if (k == 0) Bn[f] = *(const u32x4*)(pb - 64 * nb + 512 * f);
        else if (k == 1) lo[f] = *(const volatile LDSP u32x2*)(pa + 64 * nb + 512 * f);
        else hi[f] = *(const volatile LDSP u32x2*)(pa + 64 * nb + 512 * f + 8);
      }
      c++;
      SCHED();
    }
  }
  if (nb < 4) {
#pragma unroll
    for (int f = 0; f < 8; f++) An[f] = u32x4{lo[f][0], lo[f][1], hi[f][0], hi[f][1]};
  }
}

DEV u32 kn_voff(int ld, int w, int lane) {
  int kr = lane >> 3, pos = lane & 7;
  int f2 = ((lane >> 4) & 1) | ((w & 1) << 1);
  return (u32)((8 * w + kr) * ld * 2 + ((pos ^ (f2 << 1)) << 4));
}
DEV int kn_base(int lane) {
  int g = lane >> 4, q = (lane & 15) >> 2, pp = lane & 3;
  int f2 = ((q >> 1) & 1) | ((g & 1) << 1);
  return (8 * g + q) * 128 + (f2 << 5) + pp * 8;
}
DEV u32x4 kn_frag(const char* p) { u32x2 a = tr_read(p), b = tr_read(p + 512); return u32x4{a[0], a[1], b[0], b[1]}; }
DEV int mk_base(int lane, int ks) { int r16 = lane & 15, g = lane >> 4; return r16 * 128 + (((4 * ks + g) ^ (r16 & 7)) << 4); }
constexpr u32 ONES = 0x3f803f80u;
DEV u32x4 pair_swap(u32 p01, u32 p23, u32 q01, u32 q23) {
  auto r0 = __builtin_amdgcn_permlane16_swap(p01, q01, false, false);
  auto r1 = __builtin_amdgcn_permlane16_swap(p23, q23, false, false);
  return u32x4{r0[0], r1[0], r0[1], r1[1]};
}
template <int LDW> DEV void w64_issue(const u16* wsrc, u32 dst, int w, int lane) {
  const u32 vo = kn_voff(LDW, w, lane);
  const u32 d = dst + w * 1024;
  glds<0 * 8192, 0 * 128 * LDW>(vo, wsrc, d); glds<1 * 8192, 1 * 128 * LDW>(vo, wsrc, d);
  glds<2 * 8192, 2 * 128 * LDW>(vo, wsrc, d); glds<3 * 8192, 3 * 128 * LDW>(vo, wsrc, d);
  glds<4 * 8192, 4 * 128 * LDW>(vo, wsrc, d); glds<5 * 8192, 5 * 128 * LDW>(vo, wsrc, d);
  glds<6 * 8192, 6 * 128 * LDW>(vo, wsrc, d); glds<7 * 8192, 7 * 128 * LDW>(vo, wsrc, d);
}
__global__ __launch_bounds__(512, 2) void k_fused(Params p) {
  __shared__ __attribute__((aligned(16))) char smem[LDS_ALL];
  const int bid = blockIdx.x;
  const int tid = threadIdx.x, w = __builtin_amdgcn_readfirstlane(tid >> 6), lane = tid & 63;
  const int r16 = lane & 15, g = lane >> 4;
  const u32 lds0 = lds_addr(smem);
  const rsrc_t wsr = __builtin_amdgcn_make_buffer_rsrc(p.ws, 0, 0x4000000, 0x00020000);
  const int cp = (bid & 7) * 32 + (bid >> 3);
  const int pm = (bid & 7) * 4 + (bid >> 6), pn = (bid >> 3) & 7;
  const int col0 = pn * 64;
  u32x2 tag;
  u32 TKW = 0, DSK;
  {
    u32x2 ep = ld8_dev(wsr, FL_EPOCH);
    u32x4 xv = *(const u32x4*)(p.x + (bid * 8 + w) * D_ + lane * 8);
    const int chl = w >> 2, role = (w >> 1) & 1, n = tid & 127;
    const int ch = cp * 2 + chl;
    const int pi = ch * NS_ + n;
    u32 r_are = p.are[pi], r_aim = p.aim[pi], r_bre = p.bre[pi], r_bim = p.bim[pi], r_cre = p.cre[pi], r_cim = p.cim[pi];
    u32 r_ldt = p.logdt[ch];
    DSK = p.dskip[cp * 2 + ((w >> 1) & 1)];
    u32x4 kidx = *(const u32x4*)(KTBL.v + (tid & 255) * 8);
    SCHED();
    u32 sk0, sk1, sk2;
    {
      u64 pc; u32 len;
      asm volatile("s_getpc_b64 %0\n.Lk_here:\n\ts_mov_b32 %1, .Lk_end-.Lk_here" : "=s"(pc), "=s"(len));
      const u32 o = (u32)((bid >> 3) * 24 + w * 3) * 64;
      asm volatile("s_load_dword %0, %3, %4\n\ts_load_dword %1, %3, %5\n\ts_load_dword %2, %3, %6"
                   : "=&s"(sk0), "=&s"(sk1), "=&s"(sk2) : "s"(pc), "s"(min(o, len)), "s"(min(o + 64, len)), "s"(min(o + 128, len)) : "memory");
    }
    {
      u64 e = ((u64)ep[1] << 32 | ep[0]) + 1;
      tag = u32x2{(u32)__builtin_amdgcn_readfirstlane((u32)e), (u32)__builtin_amdgcn_readfirstlane((u32)(e >> 32))};
    }
    char* su = smem + P0_LN;
    float* ms = (float*)(smem + P0_MS);
    {
      f32x2 v[4];
#pragma unroll
      for (int i = 0; i < 4; i++) v[i] = bfup(xv[i]);
      f32x2 s2 = (v[0] + v[1]) + (v[2] + v[3]);
      float mean = wave_sum(s2[0] + s2[1]) * (1.0f / D_);
      f32x2 q2 = {0, 0};
#pragma unroll
      for (int i = 0; i < 4; i++) { f32x2 d = v[i] - mean; q2 += d * d; }
      float var = wave_sum(q2[0] + q2[1]) * (1.0f / D_);
      float m = rbf(mean);
      float inv = __builtin_amdgcn_rcpf(rbf(__builtin_amdgcn_sqrtf(rbf(rbf(var) + 1e-5f))));
      u32x4 uo;
#pragma unroll
      for (int i = 0; i < 4; i++) {
        f32x2 u = bfup(pkbf2(bfup(pkbf2(v[i] - m)) * inv));
        uo[i] = pkh(u[0], u[1]);
      }
      u32x4 lo4, hi4;
#pragma unroll
      for (int i = 0; i < 2; i++) {
        lo4[2 * i] = __builtin_amdgcn_perm(xv[i], uo[i], 0x05040100u); lo4[2 * i + 1] = __builtin_amdgcn_perm(xv[i], uo[i], 0x07060302u);
        hi4[2 * i] = __builtin_amdgcn_perm(xv[2 + i], uo[2 + i], 0x05040100u); hi4[2 * i + 1] = __builtin_amdgcn_perm(xv[2 + i], uo[2 + i], 0x07060302u);
      }
      *(u32x4*)(su + w * 2112 + lane * 32) = lo4;
      *(u32x4*)(su + w * 2112 + lane * 32 + 16) = hi4;
    }
    asm volatile("" : "+v"(kidx), "+v"(DSK));
    lds_bar();
    asm volatile("" :: "s"(sk0), "s"(sk1), "s"(sk2));
    {
      const int q4 = (lane & 15) >> 2, pp = lane & 3;
      const char* a = su + q4 * 2112 + (128 * w + 16 * g + 4 * pp) * 2;
      u32x2 u0 = tr_read(a), u1 = tr_read(a + 4 * 2112), x0 = tr_read(a + 128), x1 = tr_read(a + 4 * 2112 + 128);
      const u32 dst = WS_U8 + bid * 16384 + w * 2048 + lane * 16;
      st16_dev(wsr, dst, u32x4{u0[0], u0[1], u1[0], u1[1]});
      st16_dev(wsr, dst + 1024, u32x4{x0[0], x0[1], x1[0], x1[1]});
    }
    float dt = rbf(expf(bf2f(r_ldt)));
    float ar = dt * bf2f(r_are), ai = dt * bf2f(r_aim);
    cf CB = cmul(cf{bf2f(r_cre), bf2f(r_cim)}, cf{dt * bf2f(r_bre), dt * bf2f(r_bim)});
    float tl2 = ar * 1.4426950408889634f;
    const float I2PH = 0.15915494309189535f;
    const float I2PL = (float)(0.15915494309189533576888 - (double)0.15915494309189535f);
    float rh = ai * I2PH;
    float rl = __builtin_fmaf(ai, I2PH, -rh) + ai * I2PL;
    {
      float bs = wave_sum(fabsf(CB[0]) + fabsf(CB[1])), isx;
      float sx = pow2_scale(bs, 14, isx);
      CB = cscale(CB, sx);
      if (role == 0 && lane == 0) { ms[chl * 2 + (w & 1)] = bs; ms[4 + chl * 2 + (w & 1)] = isx; }
    }
    {
      const u16* wb = w == 0 ? p.w3 : w == 1 ? p.w4 : w == 2 ? p.w1 : p.w2;
      touch(TKW, wb, w < 2 ? (u32)(bid * 56 + min(lane, 55)) * 128 : (u32)(bid * 16 + (lane & 15)) * 128);
    }
    char* img = smem + chl * KG_CH;
    {
      char* wp = img + n * 4;
      if (role == 0) {
        cf e3 = cexpk(tl2, -rh, -rl, 8.f), e4 = cexpk(tl2, -rh, -rl, 16.f), e5 = cexpk(tl2, -rh, -rl, 32.f), e6 = cexpk(tl2, -rh, -rl, 64.f);
        cf y[8];
        y[0] = cf{16384.f, 0.f}; y[1] = cscale(e3, 16384.f); y[2] = cscale(e4, 16384.f); y[3] = cmul(y[2], e3);
        y[4] = cscale(e5, 16384.f); y[5] = cmul(y[4], e3); y[6] = cmul(y[4], e4); y[7] = cmul(y[6], e3);
#pragma unroll
        for (int j = 0; j < 8; j++) st_y(wp + (41 + j) * KG_ROW, y[j]);
#pragma unroll
        for (int j = 0; j < 8; j++) st_y(wp + (49 + j) * KG_ROW, j ? cmul(y[j], e6) : cscale(e6, 16384.f));
        cf e7 = cexpk(tl2, rh, rl, 128.f), e8 = cexpk(tl2, rh, rl, 256.f), e9 = cexpk(tl2, rh, rl, 512.f);
        cf e10 = cexpk(tl2, rh, rl, 1024.f), e11 = cexpk(tl2, rh, rl, 2048.f);
        cf g8 = cmul(CB, e10), g12 = cmul(g8, e9), g16 = cmul(CB, e11);
        cf g10 = cmul(g8, e8), g14 = cmul(g12, e8);
        st_x(wp + 32 * KG_ROW, g8); st_x(wp + 33 * KG_ROW, cmul(g8, e7));
        st_x(wp + 34 * KG_ROW, g10); st_x(wp + 35 * KG_ROW, cmul(g10, e7));
        st_x(wp + 36 * KG_ROW, g12); st_x(wp + 37 * KG_ROW, cmul(g12, e7));
        st_x(wp + 38 * KG_ROW, g14); st_x(wp + 39 * KG_ROW, cmul(g14, e7));
        st_x(wp + 40 * KG_ROW, g16);
      } else {
        cf e0 = cexpk(tl2, rh, rl, 1.f), e1 = cexpk(tl2, rh, rl, 2.f), e2 = cexpk(tl2, rh, rl, 4.f);
        cf e7 = cexpk(tl2, rh, rl, 128.f), e8 = cexpk(tl2, rh, rl, 256.f), e9 = cexpk(tl2, rh, rl, 512.f);
        cf eb[8];
        eb[1] = e0; eb[2] = e1; eb[3] = cmul(e1, e0); eb[4] = e2; eb[5] = cmul(e2, e0); eb[6] = cmul(e2, e1); eb[7] = cmul(eb[6], e0);
        cf gq[8];
        gq[0] = CB; gq[1] = cmul(CB, e7); gq[2] = cmul(CB, e8); gq[3] = cmul(gq[2], e7);
        gq[4] = cmul(CB, e9); gq[5] = cmul(gq[4], e7); gq[6] = cmul(gq[4], e8); gq[7] = cmul(gq[6], e7);
#pragma unroll
        for (int q = 0; q < 2; q++)
#pragma unroll
          for (int b = 0; b < 8; b++) st_x(wp + (q * 8 + b) * KG_ROW, b ? cmul(gq[q], eb[b]) : gq[q]);
#pragma unroll
        for (int q = 2; q < 4; q++)
#pragma unroll
          for (int b = 0; b < 4; b++) st_x(wp + (16 + (q - 2) * 4 + b) * KG_ROW, b ? cmul(gq[q], eb[2 * b]) : gq[q]);
#pragma unroll
        for (int q = 4; q < 8; q++)
#pragma unroll
          for (int b = 0; b < 2; b++) st_x(wp + (24 + (q - 4) * 2 + b) * KG_ROW, b ? cmul(gq[q], e2) : gq[q]);
      }
    }
    wait_vm_lgkm<1>(); bar();
    if (tid < 16) st8_dev(wsr, FL_1 + tid * 2048 + bid * 8, tag);
    char* kt = smem + P0_KT + chl * 12288;
    float kinv;
    const float kscale = pow2_scale(ms[chl * 2] + ms[chl * 2 + 1], 14, kinv);
    const float fsc = ms[4 + chl * 2 + ((w & 3) >> 1)] * (kscale * (1.0f / 16384.f));
    {
      const int wv = w & 3;
      const char* pa = img + r16 * KG_ROW + wv * 128 + g * 16;
      u32x4 a[3][2], yh[2], yl[2];
#pragma unroll
      for (int s = 0; s < 2; s++) {
#pragma unroll
        for (int t = 0; t < 3; t++) a[t][s] = *(const u32x4*)(pa + t * 16 * KG_ROW + s * 64);
        yh[s] = *(const u32x4*)(pa + 41 * KG_ROW + s * 64);
        yl[s] = *(const u32x4*)(pa + 57 * KG_ROW + s * 64);
      }
      const float f = fsc;
      char* kw = kt + (4 * g * 16 + r16) * 16 + ((wv + (r16 >> 3) + 2 * (g & 1)) & 3) * 4;
      f32x4 c[3];
#pragma unroll
      for (int t = 0; t < 3; t++) { mmah0(c[t], a[t][0], yl[0]); mmah(c[t], a[t][1], yl[1]); }
#pragma unroll
      for (int t = 0; t < 3; t++) { mmah(c[t], a[t][0], yh[0]); mmah(c[t], a[t][1], yh[1]); }
      mfence(); pin(c[0]); pin(c[1]); pin(c[2]);
#pragma unroll
      for (int t = 0; t < 3; t++)
#pragma unroll
        for (int r = 0; r < 4; r++) *(float*)(kw + (t * 16 + r) * 256) = c[t][r] * f;
    }
    if (w == 0) {
      poll256(wsr, FL_1 + (bid & 15) * 2048, tag, lane);
      if (bid == 0 && lane == 0) st8_dev(wsr, FL_EPOCH, tag);
    }
    lds_bar();
    {
      const char* sa = p.ws + WS_U8 + (size_t)w * 16 * 16384 + cp * 64;
      const u32 vo = (u32)((lane >> 2) * 16384 + ((lane & 3) ^ (lane >> 4)) * 16);
      gldsd<CV_UI, 0>(vo, sa, lds0 + w * 1024); gldsd<CV_UI + 8192, 128 * 16384>(vo, sa, lds0 + w * 1024);
    }
    {
      u32x4 o;
#pragma unroll
      for (int i = 0; i < 4; i++) {
        f32x4 a = *(const f32x4*)(kt + (kidx[i] & 0xffffu)), b = *(const f32x4*)(kt + (kidx[i] >> 16));
        o[i] = pkh((a[0] + a[1]) + (a[2] + a[3]), (b[0] + b[1]) + (b[2] + b[3]));
      }
      const int e = tid & 255;
      char* base = smem + chl * CV_CH;
      *(u32x4*)(base + CV_KP + (30 + e) * 16) = o;
      if (e < 30) *(u32x4*)(base + CV_KP + e * 16) = u32x4{0, 0, 0, 0};
      else if (e < 34) *(u32x4*)(base + (e - 30) * CV_COPY) = u32x4{0, 0, 0, 0};
      else if (e == 34) *(float*)(base + CV_KP + 4576) = kinv;
    }
  }
  {
    {
      const int chl = w >> 2, q = tid & 255;
      char* base = smem + chl * CV_CH;
      wait_vm<0>(); keep(TKW); bar();
      const char* ui = smem + CV_UI + q * 64;
      const int sw = ((2 * chl) ^ ((q >> 2) & 3)) * 16, swp = ((2 * chl) ^ (((q - 1) >> 2) & 3)) * 16;
      u32x4 cur = *(const u32x4*)(ui + sw), prv = *(const u32x4*)(ui - 64 + swp);
      *(u32x4*)(base + CV_XS + q * 16) = *(const u32x4*)(ui + (sw ^ 16));
      if (q == 0) prv = u32x4{0, 0, 0, 0};
      u32 wd[8] = {prv[0], prv[1], prv[2], prv[3], cur[0], cur[1], cur[2], cur[3]};
      u32 o[5];
#pragma unroll
      for (int k = 0; k < 5; k++) o[k] = __builtin_amdgcn_alignbit(wd[k + 1], wd[k], 16);
      char* d = base + (q + 1) * 16;
      *(u32x4*)(d) = u32x4{o[0], o[1], o[2], o[3]};
      *(u32x4*)(d + CV_COPY) = u32x4{wd[1], wd[2], wd[3], wd[4]};
      *(u32x4*)(d + 2 * CV_COPY) = u32x4{o[1], o[2], o[3], o[4]};
      *(u32x4*)(d + 3 * CV_COPY) = u32x4{wd[2], wd[3], wd[4], wd[5]};
      if (q == 255) {
        u32 o4 = __builtin_amdgcn_alignbit(cur[1], cur[0], 16), o5 = __builtin_amdgcn_alignbit(cur[2], cur[1], 16);
        u32 o6 = __builtin_amdgcn_alignbit(cur[3], cur[2], 16), o7 = cur[3] >> 16;
        char* d2 = base + 257 * 16;
        *(u32x4*)(d2) = u32x4{o4, o5, o6, o7};
        *(u32x4*)(d2 + CV_COPY) = u32x4{cur[1], cur[2], cur[3], 0};
        *(u32x4*)(d2 + 2 * CV_COPY) = u32x4{o5, o6, o7, 0};
        *(u32x4*)(d2 + 3 * CV_COPY) = u32x4{cur[2], cur[3], 0, 0};
      }
    }
    const int chl = (w >> 1) & 1, hh = w & 1;
    const float dsk = bf2f(DSK);
    const char* base = smem + chl * CV_CH;
    const int j2 = 2 * (hh + 2 * (w >> 2));
    char* red = smem + CV_RED + chl * 16384 + lane * 16;
    lds_bar();
    const float kinv = *(const float*)(base + CV_KP + 4576);
    const int l0 = 256 * j2 + 16 * r16 + 4 * g;
    u32x2 xx[2];
    u32 dd[2][2];
#pragma unroll
    for (int k = 0; k < 2; k++) {
      u32x2 uu = *(const u32x2*)(base + 3 * CV_COPY + (l0 + 256 * k + 12) * 2);
      xx[k] = *(const u32x2*)(base + CV_XS + (l0 + 256 * k) * 2);
      dd[k][0] = pkbf2(hup(uu[0]) * dsk); dd[k][1] = pkbf2(hup(uu[1]) * dsk);
    }
    asm volatile("" : "+v"(dd[0][0]), "+v"(dd[0][1]), "+v"(dd[1][0]), "+v"(dd[1][1]), "+v"(xx[0]), "+v"(xx[1]));
    f32x4 f0 = {0, 0, 0, 0}, f1 = {0, 0, 0, 0};
    if (w < 4) {
      f32x4 acc[8];
      const char* pa = base + (r16 & 3) * CV_COPY + 48 - 16 * g + 2 * (r16 & 12) + 256 * hh;
      const char* pb = base + CV_KP + 448 + 32 * r16 + 16 * g - 256 * hh;
      u32x4 A0[8], B0[8], A1[8], B1[8];
#pragma unroll
      for (int i = 0; i < 8; i++) conv_load1(pa, pb, 0, i, A0, B0);
      conv_step(pa, pb, 1, A0, B0, A1, B1, acc);
      conv_step(pa, pb, 2, A1, B1, A0, B0, acc);
      conv_step(pa, pb, 3, A0, B0, A1, B1, acc);
      conv_step(pa, pb, 4, A1, B1, A0, B0, acc);
      mfence();
#pragma unroll
      for (int i = 0; i < 8; i++) pin(acc[i]);
      char* d = red + hh * 8192;
      if (hh) { *(f32x4*)(d) = acc[0]; *(f32x4*)(d + 1024) = acc[1]; f0 = acc[2]; f1 = acc[3]; }
      else { *(f32x4*)(d + 2048) = acc[2]; *(f32x4*)(d + 3072) = acc[3]; f0 = acc[0]; f1 = acc[1]; }
#pragma unroll
      for (int t = 4; t < 8; t++) *(f32x4*)(d + t * 1024) = acc[t];
    } else {
      w64_issue<D_>(p.w1 + col0, lds0 + GA_B, w - 4, lane);
      w64_issue<D_>(p.w1 + col0, lds0 + GA_B, w, lane);
      wait_vm<0>();
    }
    lds_bar();
    if (w < 4) {
      f0 += *(const f32x4*)(red + (hh ^ 1) * 8192 + j2 * 1024);
      f1 += *(const f32x4*)(red + (hh ^ 1) * 8192 + j2 * 1024 + 1024);
    } else {
      f0 = *(const f32x4*)(red + j2 * 1024) + *(const f32x4*)(red + 8192 + j2 * 1024);
      f1 = *(const f32x4*)(red + j2 * 1024 + 1024) + *(const f32x4*)(red + 8192 + j2 * 1024 + 1024);
    }
    char* stg = smem + CV_STG;
#pragma unroll
    for (int k = 0; k < 2; k++) {
      f32x4 f = k ? f1 : f0;
      u32 y01 = pkbf2(f32x2{f[0], f[1]} * kinv), y23 = pkbf2(f32x2{f[2], f[3]} * kinv);
      u32 s01 = pkbf2(bfup(y01) + bfup(dd[k][0])), s23 = pkbf2(bfup(y23) + bfup(dd[k][1]));
      u32 h01 = pkbf2(bfup(xx[k][0]) + bfup(s01)), h23 = pkbf2(bfup(xx[k][1]) + bfup(s23));
      *(u32x2*)(stg + (16 * (j2 + k) + r16) * 80 + chl * 32 + g * 8) = u32x2{h01, h23};
    }
    lds_bar();
    {
      const int lb = tid >> 2, pc = tid & 3;
      st16_dev(wsr, WS_HT + cp * 8192 + tid * 16, *(const u32x4*)(stg + lb * 80 + pc * 16));
    }
  }
  const int mt = w & 3, wn = w >> 2;
  const int rt = (pm >> 1) * 8 + (pm & 1) * 2 + (mt >> 1) * 4 + (mt & 1);
  const int pc4 = (g & 1) * 2 + (g >> 1);
  const u32 boff = (u32)((rt * 16 + pn * 2 + wn) * 1024 + r16 * 64 + pc4 * 16);
  u32x4 ONES4 = {ONES, ONES, ONES, ONES};
  asm volatile("s_nop 1" : "+v"(ONES4));
  const u32 dw = lds0 + w * 1024;
  const u32 vbD = kn_voff(D_, w, lane);
  u32x4 hres, xres;
  u32x4 B[16][2];
  constexpr int CBD = 128 * D_, CB3 = 128 * F_;
  const int idx3 = bid >> 3;
  const bool has3 = idx3 < 28;
  const int pm3 = (bid & 7) * 2 + (idx3 >= 14 ? 1 : 0), pn3 = idx3 >= 28 ? idx3 - 28 : idx3 >= 14 ? idx3 - 14 : idx3;
  const u16* sb3 = p.w3 + pn3 * 128;
  const u32 vbF = kn_voff(F_, w, lane);
  const u32 vbF4 = kn_voff(F_, w - 4, lane);
  const int wm3 = w & 1, wn3 = w >> 1;
  const int ob0 = (kn_base(lane) + (wn3 >> 1) * 8192) ^ ((2 * (wn3 & 1)) << 5), ob1 = ob0 ^ 32;
  f32x4 cw0, cw1;
  u32x4 Bt[2][8];
  const char* pb0 = smem + GA_B + (kn_base(lane) ^ ((2 * wn) << 5));
  const char* pb1 = smem + GA_B + (kn_base(lane) ^ ((2 * wn + 1) << 5));
  auto ldB = [&](int j) {
#pragma unroll
    for (int i = 0; i < 4; i++) { Bt[j & 1][2 * i] = kn_frag(pb0 + (4 * j + i) * 4096); Bt[j & 1][2 * i + 1] = kn_frag(pb1 + (4 * j + i) * 4096); }
    SCHED();
  };
  auto ldB3 = [&](int s0, int n, int o) {
#pragma unroll
    for (int s = 0; s < 8; s++) if (s < n) {
      B[s0 + s][0] = kn_frag(smem + o + (s >> 1) * 16384 + (s & 1) * 4096 + ob0);
      B[s0 + s][1] = kn_frag(smem + o + (s >> 1) * 16384 + (s & 1) * 4096 + ob1);
    }
#pragma unroll
    for (int s = 0; s < 8; s++) if (s < n) { mmaz(s0 + s == 0, cw0, B[s0 + s][0], ONES4); mmaz(s0 + s == 0, cw1, B[s0 + s][1], ONES4); }
  };
#define W2C(c) glds<GA_B + (c) * 8192, (c) * CBD>(vbD, p.w2 + col0, dw);
#define W3C(c, dst) glds<(dst), (c) * CB3>(vbF, sb3, dw); glds<(dst) + 8192, (c) * CB3 + 128>(vbF, sb3, dw);
#define W3C4(c, dst) W3C(c, dst) glds<(dst), (c) * CB3>(vbF4, sb3, dw - 4096); glds<(dst) + 8192, (c) * CB3 + 128>(vbF4, sb3, dw - 4096);
  {
    wait_vm_lgkm<0>(); bar();
    if (tid < 16) st8_dev(wsr, FL_2 + tid * 2048 + bid * 8, tag);
    f32x4 cv0, cv1;
    {
#pragma unroll
      for (int i = 0; i < 4; i++) { Bt[0][2 * i] = kn_frag(pb0 + (4 * mt + i) * 4096); Bt[0][2 * i + 1] = kn_frag(pb1 + (4 * mt + i) * 4096); }
#pragma unroll
      for (int i = 0; i < 4; i++) { mmaz(i == 0, cv0, Bt[0][2 * i], ONES4); mmaz(i == 0, cv1, Bt[0][2 * i + 1], ONES4); }
      mfence(); pin(cv0); pin(cv1);
      char* ct = smem + 65536 + wn * 128 + g * 16;
      if (r16 == 0) { *(f32x4*)(ct + mt * 256) = cv0; *(f32x4*)(ct + mt * 256 + 64) = cv1; }
      lds_bar();
      cv0 = (*(const f32x4*)(ct) + *(const f32x4*)(ct + 256)) + (*(const f32x4*)(ct + 512) + *(const f32x4*)(ct + 768));
      cv1 = (*(const f32x4*)(ct + 64) + *(const f32x4*)(ct + 320)) + (*(const f32x4*)(ct + 576) + *(const f32x4*)(ct + 832));
    }
    lds_bar();
    if (w >= 4) { W3C4(0, 65536) W3C4(1, 81920) }
    ldB(0); ldB(1);
    if (w == 0) poll256(wsr, FL_2 + (bid & 15) * 2048, tag, lane);
    bar();
    {
      const int v4 = (mt & 1) + 2 * wn;
      const char* sa = p.ws + WS_HT + (rt & ~1) * 64 + (size_t)v4 * 131072;
      const u32 vo = (u32)((lane >> 3) * 8192 + ((lane & 7) ^ (((lane >> 5) & 1) << 2)) * 16);
      const u32 d = lds0 + GA_A + (mt >> 1) * 32768 + v4 * 2048;
      gldsd<0, 0>(vo, sa, d); gldsd<1024, 65536>(vo, sa, d); gldsd<8192, 4 * 131072>(vo, sa, d); gldsd<9216, 4 * 131072 + 65536>(vo, sa, d);
      gldsd<16384, 8 * 131072>(vo, sa, d); gldsd<17408, 8 * 131072 + 65536>(vo, sa, d); gldsd<24576, 12 * 131072>(vo, sa, d); gldsd<25600, 12 * 131072 + 65536>(vo, sa, d);
    }
    const char* aw = smem + GA_A + (mt >> 1) * 32768 + ((lane & 15) >> 3) * 128 + ((lane >> 2) & 1) * 32 + (lane & 3) * 8;
    const char* pa0 = aw + g * 512 + (((mt ^ g) & 1) << 6);
    f32x4 acc0, acc1, sacc, gacc;
    u32x4 a[2][4];
    auto ld = [&](int j) {
#pragma unroll
      for (int i = 0; i < 4; i++) {
        u32x2 x0 = tr_read(pa0 + (4 * j + i) * 2048), x1 = tr_read(pa0 + (4 * j + i) * 2048 + 256);
        a[j & 1][i] = u32x4{x0[0], x0[1], x1[0], x1[1]};
      }
      SCHED();
    };
    auto mm = [&](int j) {
#pragma unroll
      for (int i = 0; i < 4; i++) {
        mmaz(j + i == 0, sacc, ONES4, a[j & 1][i]);
        mmaz(j + i == 0, gacc, a[j & 1][i], a[j & 1][i]);
        mmaz(j + i == 0, acc0, Bt[j & 1][2 * i], a[j & 1][i]);
        mmaz(j + i == 0, acc1, Bt[j & 1][2 * i + 1], a[j & 1][i]);
      }
      SCHED();
    };
    SCHED();
    wait_vm<6>(); bar(); ld(0);
    mm(0); ldB(2);
    wait_vm<4>(); bar(); ld(1); mm(1); ldB(3);
    wait_vm<2>(); bar(); ld(2); mm(2);
    wait_vm_lgkm<0>(); bar(); ld(3);
    W2C(0) W2C(1)
    const char* hs = aw + (2 * pn + wn) * 2048 + pc4 * 512 + (((mt ^ pc4) & 1) << 6);
    u32x2 hx0 = tr_read(hs), hx1 = tr_read(hs + 256);
    SCHED(); mm(3); W2C(2) W2C(3)
    mfence(); pin(acc0); pin(acc1); pin(sacc); pin(gacc);
    float dg = (r16 & 2) ? ((r16 & 1) ? gacc[3] : gacc[2]) : ((r16 & 1) ? gacc[1] : gacc[0]);
    f32x2 sa2 = {sacc[0], gsum(g == (r16 >> 2) ? dg : 0.f)};
    float mean1 = sa2[0] * (1.0f / D_);
    float var1 = fmaxf(sa2[1] * (1.0f / D_) - mean1 * mean1, 0.f);
    float m1 = rbf(mean1);
    float sd1 = rbf(__builtin_amdgcn_sqrtf(rbf(rbf(var1) + 1e-5f)));
    float i1 = __builtin_amdgcn_rcpf(sd1);
    float m2 = rbf((mean1 - m1) * i1);
    float sd2 = rbf(__builtin_amdgcn_sqrtf(rbf(rbf(var1 * i1 * i1) + 1e-5f)));
    float rs = i1 * __builtin_amdgcn_rcpf(sd2), tt = -(m1 + m2 * sd1) * rs;
    {
      f32x4 rt4 = {rs, rs, tt, tt};
      pin(rt4);
      u32x2 o0 = act4(acc0, cv0, rt4), o1 = act4(acc1, cv1, rt4);
      st16_dev(wsr, WS_H1 + 16384 + boff, pair_swap(o0[0], o0[1], o1[0], o1[1]));
    }
    if (tid < 16) {
      const u32 o = (u32)((pn * 2 + (tid >> 3)) * 1024 + (tid & 7) * 16);
      if (pm == 0) st16_dev(wsr, WS_H1 + 14 * 64 + o, u32x4{0, 0, 0, 0});
      if (pm == 31) st16_dev(wsr, WS_H1 + 129 * 16384 + o, u32x4{0, 0, 0, 0});
    }
    hres = u32x4{hx0[0], hx0[1], hx1[0], hx1[1]};
#pragma unroll
    for (int i = 0; i < 4; i++) xres[i] = pkbf2(bfup(pkbf2(bfup(hres[i]) - m1)) * i1);
    W2C(4) W2C(5) W2C(6) W2C(7)
    wait_vm_lgkm<4>(); bar();
    if (tid < 8) st8_dev(wsr, FL_3 + tid * 2048 + (pm * 8 + pn) * 8, tag);
    ldB3(0, 4, 65536);
    ldB(0); ldB(1);
  }
  const int ar = lane >> 3, asp = (lane & 7) ^ ar;
  const u32 apo = (u32)((asp >> 2) * 1024 + (asp & 3) * 16);
  {
    u32 vo[8];
#pragma unroll
    for (int k = 0; k < 8; k++) {
      const int sh = (k >> 1) == 0 ? -1 : (k >> 1) == 1 ? 1 : (k >> 1) == 2 ? -2 : 2;
      const int row = 16 + ar + (k & 1) * 8 + sh;
      vo[k] = (u32)((row >> 4) * 16384 + (row & 15) * 64) + apo;
    }
    if (w == 0) polln(wsr, FL_3 + pn * 2048, max((pm & ~1) - 1, 0) * 8, min((pm & ~1) + 3, 32) * 8, tag, lane);
    lds_bar();
    {
      const char* sa = p.ws + WS_H1 + (size_t)rt * 16384 + wn * 2048;
      const u32 d = lds0 + GA_A + mt * 16384 + wn * 2048;
      gldsd<0, 0>(vo[0], sa, d); gldsd<1024, 0>(vo[1], sa, d); gldsd<4096, 4096>(vo[2], sa, d); gldsd<5120, 4096>(vo[3], sa, d);
      gldsd<8192, 8192>(vo[4], sa, d); gldsd<9216, 8192>(vo[5], sa, d); gldsd<12288, 12288>(vo[6], sa, d); gldsd<13312, 12288>(vo[7], sa, d);
    }

    const char* pa0 = smem + GA_A + mt * 16384 + mk_base(lane, 0);
    const char* pa1 = smem + GA_A + mt * 16384 + mk_base(lane, 1);
    f32x4 acc0, acc1;
    u32x4 a[2][4];
    auto ld = [&](int j) {
      a[j & 1][0] = *(const u32x4*)(pa0 + j * 4096); a[j & 1][1] = *(const u32x4*)(pa1 + j * 4096);
      a[j & 1][2] = *(const u32x4*)(pa0 + j * 4096 + 2048); a[j & 1][3] = *(const u32x4*)(pa1 + j * 4096 + 2048);
      SCHED();
    };
    auto mm = [&](int j) {
#pragma unroll
      for (int i = 0; i < 4; i++) {
        mmaz(j + i == 0, acc0, Bt[j & 1][2 * i], a[j & 1][i]);
        mmaz(j + i == 0, acc1, Bt[j & 1][2 * i + 1], a[j & 1][i]);
      }
      SCHED();
    };
    SCHED();
    wait_vm<6>(); bar(); ld(0);
    wait_vm<4>(); bar(); ld(1); mm(0); ldB(2);
    wait_vm<2>(); bar(); ld(2); mm(1); ldB(3);
    wait_vm_lgkm<0>(); bar(); ld(3);
    W3C(2, 65536)
    mm(2); W3C(3, 81920) mm(3);
    mfence(); pin(acc0); pin(acc1);
    u32x4 v = pair_swap(pkbf(acc0[0], acc0[1]), pkbf(acc0[2], acc0[3]), pkbf(acc1[0], acc1[1]), pkbf(acc1[2], acc1[3]));
#pragma unroll
    for (int i = 0; i < 4; i++) xres[i] = pkbf2(bfup(xres[i]) + bfup(v[i]));
    st16_dev(wsr, WS_X1 + boff, xres);
    float s1 = 0, s2 = 0;
#pragma unroll
    for (int i = 0; i < 4; i++) { s1 = dot2(xres[i], ONES, s1); s2 = dot2(xres[i], xres[i], s2); }
    s1 = gsum(s1); s2 = gsum(s2);
    if (g == 0) st8_dev(wsr, WS_ST + (rt * 16 + pn * 2 + wn) * 128 + r16 * 8, u32x2{__float_as_uint(s1), __float_as_uint(s2)});
    W3C(4, GA_B) W3C(5, GA_B + 16384)
    wait_vm_lgkm<4>(); bar();
    if (tid < 8) st8_dev(wsr, FL_4 + tid * 2048 + (pm * 8 + pn) * 8, tag);
    W3C(6, GA_B + 32768) W3C(7, GA_B + 49152)
  }
  const u16* sb4 = p.w4 + col0;
#define G4_R1A(i) glds<GA_B + (i) * 8192, (((i) >> 1) * 7 + ((i) & 1)) * CBD>(vbD, sb4, dw);
#define G4_R1B(i) glds<((i) < 2 ? G3_ST : 0) + (i) * 8192, (((i) >> 1) * 7 + 2 + ((i) & 1)) * CBD>(vbD, sb4, dw);
#define G4_R2POS(i) ((i) < 4 ? 32768 + (i) * 8192 : GA_B + ((i) - 4) * 8192)
#define G4_R2(i) glds<G4_R2POS(i), (((i) / 3) * 7 + 4 + ((i) % 3)) * CBD>(vbD, sb4, dw);
  const int wm4 = mt >> 1, kq = (mt & 1) + 2 * wn;
  const int kb = kn_base(lane);
  u32x4(&B4)[8][4] = *reinterpret_cast<u32x4(*)[8][4]>(&B);
  const char* sa4 = p.ws + WS_HID + (size_t)((rt & ~1) * 56 + 14 * kq) * 1024;
  const u32 va4 = (u32)(ar * 64) + apo;
  const int ring = (wm4 ? 0 : 65536) + kq * 8192;
  const u32 da = lds0 + ring;
#define G4_A4(b) gldsd<((b) & 1) * 4096, (b) * 2048>(va4, sa4, da); gldsd<((b) & 1) * 4096 + 1024, (b) * 2048 + 512>(va4, sa4, da); \
                 gldsd<((b) & 1) * 4096 + 2048, (b) * 2048 + 57344>(va4, sa4, da); gldsd<((b) & 1) * 4096 + 3072, (b) * 2048 + 57344 + 512>(va4, sa4, da);
  auto ldB4 = [&](int s) {
    const char* p0 = smem + GA_B + kq * 16384 + s * 4096;
    const char* p1 = smem + (kq ? kq * 16384 : G3_ST) + s * 4096;
#pragma unroll
    for (int nt = 0; nt < 4; nt++) { B4[s][nt] = kn_frag(p0 + (kb ^ (nt << 5))); B4[4 + s][nt] = kn_frag(p1 + (kb ^ (nt << 5))); }
    SCHED();
  };
  {
    const int wm = wm3;
    ldB3(4, 4, 65536);
    wait_vm<0>(); bar();
    ldB3(8, 8, GA_B);
    if (w == 0) polln(wsr, FL_4 + pn * 2048, pm3 * 16, pm3 * 16 + 16, tag, lane);
    lds_bar();
    const char* sa = p.ws + WS_X1 + (size_t)(pm3 * 8 + (w >> 1)) * 16384 + (w & 1) * 512;
    const u32 va = (u32)(ar * 64) + apo;
#define G3_A(i) gldsd<(((i) + 4) % 9) * 8192, ((i) >> 3) * 65536 + ((i) & 7) * 2048>(va, sa, dw);
    {
      const char* ss = p.ws + WS_ST + (size_t)pm3 * 16384 + w * 1024;
      gldsd<G3_ST, 0>(lane * 16, ss, dw); gldsd<G3_ST + 8192, 8192>(lane * 16, ss, dw);
    }
    G3_A(0) G3_A(1) G3_A(2) G3_A(3) G3_A(4) G3_A(5) G3_A(6) G3_A(7) G3_A(8)
    wait_vm<9>(); bar();
    {
      const char* sst = smem + G3_ST + (tid >> 6) * 2048 + (tid & 3) * 512 + ((tid >> 2) & 15) * 8;
      f32x2 st = (*(const f32x2*)(sst) + *(const f32x2*)(sst + 128)) + (*(const f32x2*)(sst + 256) + *(const f32x2*)(sst + 384));
      float t1 = st[0] + dppf<0xB1>(st[0]), t2 = st[1] + dppf<0xB1>(st[1]);
      t1 += dppf<0x4E>(t1); t2 += dppf<0x4E>(t2);
      float mean = t1 * (1.0f / D_);
      float var = fmaxf(t2 * (1.0f / D_) - mean * mean, 0.f);
      float rs = __builtin_amdgcn_rcpf(rbf(__builtin_amdgcn_sqrtf(rbf(rbf(var) + 1e-5f))));
      if ((tid & 3) == 0) { float tt = -rbf(mean) * rs; *(f32x4*)(smem + G3_TAB + (tid >> 2) * 16) = f32x4{rs, rs, tt, tt}; }
    }
    const f32x4& cv0 = cw0;
    const f32x4& cv1 = cw1;
    float* rowt = (float*)(smem + G3_TAB);
    const int oa0 = mk_base(lane, 0) + wm * 4096, oa1 = mk_base(lane, 1) + wm * 4096;
    f32x4 acc[2][2][2];
    u32x4 a[2][4];
    auto ld = [&](int slot, u32x4 (&d)[4]) {
      const char* s = smem + slot * 8192;
      d[0] = *(const u32x4*)(s + oa0); d[1] = *(const u32x4*)(s + oa0 + 2048);
      d[2] = *(const u32x4*)(s + oa1); d[3] = *(const u32x4*)(s + oa1 + 2048);
      SCHED();
    };
    auto m1 = [&](int ps, int b, int i, const u32x4 (&d)[4]) {
      mmaz(b == 0 && i < 4, acc[ps][(i >> 1) & 1][i & 1], B[2 * b + (i >> 2)][(i >> 1) & 1], d[2 * (i >> 2) + (i & 1)]);
      SCHED();
    };
    auto mm = [&](int ps, int b, const u32x4 (&d)[4]) {
#pragma unroll
      for (int i = 0; i < 8; i++) m1(ps, b, i, d);
    };
    const u32 hoff = (has3 ? WS_HID : 0x40000000u) + (u32)(((pm3 * 8 + 2 * wm) * 56 + pn3 * 4 + wn3) * 1024 + r16 * 64 + pc4 * 16);
    u32x2 o0;
    f32x4 rtv[2];
    auto ldrt = [&](int ps) {
      rtv[0] = *(const f32x4*)(rowt + (64 * ps + 32 * wm + r16) * 4);
      rtv[1] = *(const f32x4*)(rowt + (64 * ps + 32 * wm + 16 + r16) * 4);
    };
    auto ep = [&](int ps, int i) {
      const int mi = i >> 1;
      if ((i & 1) == 0) o0 = act4(acc[ps][0][mi], cv0, rtv[mi]);
      else {
        u32x2 o1 = act4(acc[ps][1][mi], cv1, rtv[mi]);
        st16_dev(wsr, hoff + (4 * ps + mi) * 57344, pair_swap(o0[0], o0[1], o1[0], o1[1]));
      }
    };
#define SL(i) (((i) + 4) % 9)
    wait_vm_lgkm<8>(); bar(); ld(SL(0), a[0]); G4_R1A(0)
    wait_vm_lgkm<8>(); bar(); ld(SL(1), a[1]); G4_R1A(1) mm(0, 0, a[0]); SCHED();
    wait_vm_lgkm<8>(); bar(); G3_A(9) ld(SL(2), a[0]); G4_R1A(2) mm(0, 1, a[1]); SCHED();
    wait_vm_lgkm<9>(); bar(); G3_A(10) ld(SL(3), a[1]); G4_R1A(3) mm(0, 2, a[0]); SCHED();
    wait_vm_lgkm<10>(); bar(); G3_A(11) ld(SL(4), a[0]); G4_R1A(4) mm(0, 3, a[1]); SCHED();
    wait_vm_lgkm<11>(); bar(); G3_A(12) ld(SL(5), a[1]); G4_R1A(5) mm(0, 4, a[0]); SCHED();
    wait_vm_lgkm<12>(); bar(); G3_A(13) ld(SL(6), a[0]); G4_R1A(6) mm(0, 5, a[1]); SCHED();
    wait_vm_lgkm<13>(); bar(); G3_A(14) ld(SL(7), a[1]); G4_R1A(7) mm(0, 6, a[0]); SCHED();
    wait_vm_lgkm<14>(); bar(); G3_A(15) ld(SL(8), a[0]); ldrt(0); mm(0, 7, a[1]); SCHED();
    mfence();
#pragma unroll
    for (int i = 0; i < 4; i++) pin(acc[0][i >> 1][i & 1]);
#pragma unroll
    for (int i = 0; i < 4; i++) ep(0, i);
    SCHED();
    wait_vm_lgkm<14>(); bar(); ld(SL(9), a[1]); G4_R1B(2) G4_R1B(0) mm(1, 0, a[0]); SCHED();
    wait_vm_lgkm<14>(); bar(); ld(SL(10), a[0]); G4_R1B(3) G4_R1B(1) mm(1, 1, a[1]); SCHED();
    wait_vm_lgkm<14>(); bar(); ld(SL(11), a[1]); G4_R1B(4) mm(1, 2, a[0]); SCHED();
    wait_vm_lgkm<5>(); bar();
    if (has3 && tid < 8) st8_dev(wsr, FL_5 + tid * 2048 + (pm3 * 14 + pn3) * 8, tag);
    ld(SL(12), a[0]); G4_R1B(5) mm(1, 3, a[1]); SCHED();
    lds_bar(); ld(SL(13), a[1]); G4_R1B(6) mm(1, 4, a[0]); SCHED();
    lds_bar(); ld(SL(14), a[0]); G4_R1B(7) mm(1, 5, a[1]); SCHED();
    lds_bar(); ld(SL(15), a[1]); ldrt(1); mm(1, 6, a[0]); SCHED();
    wait_vm_lgkm<0>(); bar();
    mm(1, 7, a[1]);
    mfence();
#pragma unroll
    for (int i = 0; i < 4; i++) pin(acc[1][i >> 1][i & 1]);
#pragma unroll
    for (int i = 0; i < 4; i++) { ldB4(i); ep(1, i); SCHED(); }
    if (w == 0) polln(wsr, FL_5 + pn * 2048, (pm >> 1) * 14, (pm >> 1) * 14 + 14, tag, lane);
    lds_bar();
    if (wm4 == 0) { G4_A4(0) G4_A4(1) wait_vm<8>(); } else wait_vm<0>();
    bar();
    if (has3 && tid < 8) st8_dev(wsr, FL_6 + tid * 2048 + (pm3 * 14 + pn3) * 8, tag);
  }
  {
    const int wm = wm4;
    if (wm) {
      const u32 vb2 = kn_voff(D_, w - 2, lane), dw2 = dw - 2048;
#define G4_R2X(i) G4_R2(i) glds<G4_R2POS(i), (((i) / 3) * 7 + 4 + ((i) % 3)) * CBD>(vb2, sb4, dw2);
      G4_R2X(0) G4_R2X(3) G4_R2X(6) G4_R2X(9) G4_R2X(1) G4_R2X(4) G4_R2X(7) G4_R2X(10) G4_R2X(2) G4_R2X(5) G4_R2X(8) G4_R2X(11)
      polln(wsr, FL_6 + (pn ^ kq) * 2048, (pm >> 1) * 14, (pm >> 1) * 14 + 14, tag, lane);
      G4_A4(0) G4_A4(1)
      wait_vm<8>(); bar();
    }
    const char* pa0 = smem + ring + mk_base(lane, 0);
    const char* pa1 = smem + ring + mk_base(lane, 1);
    f32x4 acc[4][2];
    u32x4 a[4];
    auto ld = [&](int b) {
      a[0] = *(const u32x4*)(pa0 + (b & 1) * 4096); a[1] = *(const u32x4*)(pa0 + (b & 1) * 4096 + 2048);
      a[2] = *(const u32x4*)(pa1 + (b & 1) * 4096); a[3] = *(const u32x4*)(pa1 + (b & 1) * 4096 + 2048);
      asm volatile("s_waitcnt lgkmcnt(0)" ::: "memory");
      SCHED();
    };
    auto mm = [&](int s0, bool first = false) {
#pragma unroll
      for (int ks = 0; ks < 2; ks++)
#pragma unroll
        for (int nt = 0; nt < 4; nt++)
#pragma unroll
          for (int mi = 0; mi < 2; mi++) mmaz(first && ks == 0, acc[nt][mi], B4[s0 + ks][nt], a[2 * ks + mi]);
      SCHED();
    };
    auto ldb = [&](int j, int s0) {
      const int i = kq * 3 + j;
      const char* bl = smem + (i < 4 ? 32768 + i * 8192 : GA_B + (i - 4) * 8192);
#pragma unroll
      for (int ks = 0; ks < 2; ks++)
#pragma unroll
        for (int nt = 0; nt < 4; nt++) B4[s0 + ks][nt] = kn_frag(bl + ks * 4096 + (kb ^ (nt << 5)));
      SCHED();
    };
    wait_vm<4>(); ld(0); G4_A4(2) mm(0, true);
    wait_vm<4>(); ld(1); G4_A4(3) mm(2);
    wait_vm<4>(); ld(2); G4_A4(4) mm(4);
    wait_vm<4>(); ld(3); G4_A4(5) mm(6);
    if (wm == 0) bar();
    ldb(0, 0); ldb(1, 2);
    wait_vm<4>(); ld(4); G4_A4(6) ldb(2, 4); mm(0);
    if (wm) bar();
    wait_vm<4>(); ld(5); mm(2);
    wait_vm<0>(); ld(6); mm(4);
    mfence();
#pragma unroll
    for (int i = 0; i < 8; i++) pin(acc[i >> 1][i & 1]);
    {
      char* d = smem + ring + lane * 16;
#pragma unroll
      for (int k2 = 0; k2 < 4; k2++) {
        *(f32x4*)(d + k2 * 2048) = acc[2 * (k2 >> 1)][k2 & 1];
        *(f32x4*)(d + k2 * 2048 + 1024) = acc[2 * (k2 >> 1) + 1][k2 & 1];
      }
    }
    lds_bar();
    const char* s = smem + (wm ? 0 : 65536) + kq * 2048 + lane * 16;
    f32x4 f0 = (*(const f32x4*)(s) + *(const f32x4*)(s + 8192)) + (*(const f32x4*)(s + 16384) + *(const f32x4*)(s + 24576));
    f32x4 f1 = (*(const f32x4*)(s + 1024) + *(const f32x4*)(s + 9216)) + (*(const f32x4*)(s + 17408) + *(const f32x4*)(s + 25600));
    u32x4 v = pair_swap(pkbf(f0[0], f0[1]), pkbf(f0[2], f0[3]), pkbf(f1[0], f1[1]), pkbf(f1[2], f1[3]));
    u32x4 o;
#pragma unroll
    for (int i = 0; i < 4; i++) o[i] = pkbf2(bfup(hres[i]) + bfup(pkbf2(bfup(xres[i]) + bfup(v[i]))));
    __builtin_amdgcn_raw_buffer_store_b128(o, __builtin_amdgcn_make_buffer_rsrc(p.out, 0, L_ * D_ * 2, 0x00020000),
                                           (u32)(((rt * 16 + r16) * D_ + col0 + 32 * wn + pc4 * 8) * 2), 0, 16);
  }
  asm volatile(".Lk_end:");
}

extern "C" void kernel_launch(void* const* d_in, const int* in_sizes, int n_in, void* d_out, int out_size, void* d_ws,
                              size_t ws_size, hipStream_t stream) {
  Params p;
  p.x = (const u16*)d_in[0]; p.are = (const u16*)d_in[1]; p.aim = (const u16*)d_in[2]; p.bre = (const u16*)d_in[3];
  p.bim = (const u16*)d_in[4]; p.cre = (const u16*)d_in[5]; p.cim = (const u16*)d_in[6]; p.logdt = (const u16*)d_in[7];
  p.dskip = (const u16*)d_in[8]; p.w1 = (const u16*)d_in[10]; p.w2 = (const u16*)d_in[11]; p.w3 = (const u16*)d_in[12];
  p.w4 = (const u16*)d_in[13]; p.out = (u16*)d_out; p.ws = (char*)d_ws;
  k_fused<<<256, 512, 0, stream>>>(p);
}
```
